```python
import torch

def _softmax(x, axis=-1):
    return torch.softmax(x, dim=axis)
def _gelu(x):
    return torch.nn.functional.gelu(x, approximate="tanh")
def _silu(x):
    return torch.nn.functional.silu(x)
def _sigmoid(x):
    return torch.sigmoid(x)
def _layernorm(x, axis=-1, eps=1e-5):
    m = x.mean(dim=axis, keepdim=True)
    v = x.var(dim=axis, keepdim=True, unbiased=False)
    return (x - m) / torch.sqrt(v + eps)
def _scatter_add(tgt, idx, src, axis=0):
    return tgt.clone().index_add_(axis, idx, src)
def _segment_sum(src, seg, n):
    out = torch.zeros((n,) + src.shape[1:], dtype=src.dtype)
    return out.index_add_(0, seg, src)
def _pad(x, pw):
    flat = tuple(v for pair in reversed(pw) for v in pair)
    return torch.nn.functional.pad(x, flat)

kernel_name = 'recurrent_scan__sliding_block_cached__mixer_token_mlp__prompt'

def setup_inputs():

    _w = (lambda a: (lambda t: t.to(torch.bfloat16) if t.is_floating_point() else t)(torch.as_tensor(a)))
    def _A_setup():
        B, Sq, D = 1, 2048, 512
        Hq, Hkv, Sk, Dh = 32, 8, 2048, 128
        assert Hq % Hkv == 0, (Hq, Hkv)
        return {
            "x": _w((lambda *s: torch.randn(*s, dtype=torch.bfloat16) * 0.05)(B, Sq, D)),
            "k_cache": _w((lambda *s: torch.randn(*s, dtype=torch.bfloat16) * 1.6)(B, Hkv, Sk, Dh)),
            "v_cache": _w((lambda *s: torch.randn(*s, dtype=torch.bfloat16) * 0.8)(B, Hkv, Sk, Dh)),
            "wq": _w((lambda *s: torch.randn(*s, dtype=torch.bfloat16) * 1.6)(D, Hq * Dh) * (D ** -0.5)),
            "wo": _w((lambda *s: torch.randn(*s, dtype=torch.bfloat16) * 0.8)(Hq * Dh, D) * ((Hq * Dh) ** -0.5)),
        }
    _A_in = _A_setup()

    def _B_setup():
        B, S, D, F = 1, 2048, 512, 1792
        return {
            "x": _w((lambda *s: torch.randn(*s, dtype=torch.bfloat16) * 0.05)(B, S, D)),
            "w1": _w((lambda *s: torch.randn(*s, dtype=torch.bfloat16) * 1.6)(S, F) * (S ** -0.5)),
            "b1": _w((lambda *s: torch.randn(*s, dtype=torch.bfloat16) * 1.6)(F) * 0.02),
            "w2": _w((lambda *s: torch.randn(*s, dtype=torch.bfloat16) * 0.2)(F, S) * (F ** -0.5)),
            "b2": _w((lambda *s: torch.randn(*s, dtype=torch.bfloat16) * 0.2)(S) * 0.02),
        }
    _B_in = _B_setup()

    return {
        "A_x": _A_in["x"],
        "A_k_cache": _A_in["k_cache"],
        "A_v_cache": _A_in["v_cache"],
        "A_wq": _A_in["wq"],
        "A_wo": _A_in["wo"],
        "B_x": _B_in["x"],
        "B_w1": _B_in["w1"],
        "B_b1": _B_in["b1"],
        "B_w2": _B_in["w2"],
        "B_b2": _B_in["b2"],
    }

def reference(A_x, A_k_cache, A_v_cache, A_wq, A_wo, B_x, B_w1, B_b1, B_w2, B_b2):
    def _A_ref(x, k_cache, v_cache, wq, wo):
        B, Sq, D = x.shape
        Hq, Dh = 32, 128
        Hkv, Sk = k_cache.shape[1], k_cache.shape[2]
        W = min(4096, max(256, Sk // 4))
        rep = Hq // Hkv
        q = (lambda x, *d: x.permute(*d))((x @ wq).reshape(B, Sq, Hq, Dh), 0, 2, 1, 3)
        k = (lambda x, n, axis: torch.repeat_interleave(x, n, dim=axis))(k_cache[:, :, Sk - W:, :], rep, axis=1)
        v = (lambda x, n, axis: torch.repeat_interleave(x, n, dim=axis))(v_cache[:, :, Sk - W:, :], rep, axis=1)
        scores = torch.einsum("bhqd,bhkd->bhqk", q, k) * (Dh ** -0.5)
        p = _softmax(scores, axis=-1)
        o = torch.einsum("bhqk,bhkd->bhqd", p, v)
        return (lambda x, *d: x.permute(*d))(o, 0, 2, 1, 3).reshape(B, Sq, Hq * Dh) @ wo

    def _B_ref(x, w1, b1, w2, b2):
        B, S, D = x.shape
        h = _layernorm(x, axis=-1)
        u = torch.einsum("bsd,sf->bdf", h, w1) + b1
        u = _gelu(u)
        y = torch.einsum("bdf,fs->bds", u, w2) + b2
        return (lambda x, *d: x.permute(*d))(y, 0, 2, 1)

    x = A_x
    xn = _layernorm(x, axis=-1)
    s = _A_ref(xn, A_k_cache, A_v_cache, A_wq, A_wo)
    g = _silu(_B_ref(xn, B_w1, B_b1, B_w2, B_b2))
    return x + s * g
```

```cpp
#include <hip/hip_runtime.h>
#include <hip/hip_bf16.h>
#include <cstdio>
typedef unsigned short u16;
__device__ __forceinline__ float bf2f(u16 v){ return __uint_as_float(((unsigned)v)<<16); }
__device__ __forceinline__ u16 f2bf(float f){ __bf16 b=(__bf16)f; return __builtin_bit_cast(u16,b); }
__device__ __forceinline__ float rbf(float f){ return bf2f(f2bf(f)); }

constexpr int S=2048, D=512, HQ=32, HKV=8, DH=128, SK=2048, W=512, F=1792;

__global__ void ln_k(const u16* x, u16* xn, u16* h){
  int row=blockIdx.x; int t=threadIdx.x;
  float v[8]; float s=0;
  for(int i=0;i<8;i++){ v[i]=bf2f(x[row*D+t*8+i]); s+=v[i]; }
  for(int o=32;o>0;o>>=1) s+=__shfl_xor(s,o);
  float m=s/D; float q=0;
  for(int i=0;i<8;i++){ float d=v[i]-m; q+=d*d; }
  for(int o=32;o>0;o>>=1) q+=__shfl_xor(q,o);
  float r=1.f/sqrtf(q/D+1e-5f);
  s=0;
  for(int i=0;i<8;i++){ v[i]=rbf((v[i]-m)*r); xn[row*D+t*8+i]=f2bf(v[i]); s+=v[i]; }
  for(int o=32;o>0;o>>=1) s+=__shfl_xor(s,o);
  m=s/D; q=0;
  for(int i=0;i<8;i++){ float d=v[i]-m; q+=d*d; }
  for(int o=32;o>0;o>>=1) q+=__shfl_xor(q,o);
  r=1.f/sqrtf(q/D+1e-5f);
  for(int i=0;i<8;i++) h[row*D+t*8+i]=f2bf((v[i]-m)*r);
}
__global__ void gemm_k(const u16* A,int sam,int sak,const u16* B,int sbk,int sbn,float* C,int M,int N,int K){
  int n=blockIdx.x*64+threadIdx.x; int m=blockIdx.y;
  float acc=0;
  for(int k=0;k<K;k++) acc+=bf2f(A[(long)m*sam+(long)k*sak])*bf2f(B[(long)k*sbk+(long)n*sbn]);
  C[(long)m*N+n]=acc;
}
__global__ void cvt_k(const float* a,u16* b,int n){ int i=blockIdx.x*256+threadIdx.x; if(i<n) b[i]=f2bf(a[i]); }
__global__ void attn_k(const u16* q,const u16* kc,const u16* vc,u16* o){
  int row=blockIdx.x, h=blockIdx.y, t=threadIdx.x; int g=h/4;
  __shared__ float sq[DH]; __shared__ float sc[W]; __shared__ float red[128];
  sq[t]=bf2f(q[row*(HQ*DH)+h*DH+t]); __syncthreads();
  float mx=-1e30f;
  for(int k=t;k<W;k+=128){ const u16* kp=kc+((long)g*SK+(SK-W)+k)*DH; float a=0; for(int d=0;d<DH;d++) a+=sq[d]*bf2f(kp[d]); a*=0.08838834764831845f; sc[k]=a; mx=fmaxf(mx,a);}
  red[t]=mx; __syncthreads();
  for(int o2=64;o2>0;o2>>=1){ if(t<o2) red[t]=fmaxf(red[t],red[t+o2]); __syncthreads(); }
  mx=red[0]; __syncthreads();
  float sm=0; for(int k=t;k<W;k+=128){ float e=__expf(sc[k]-mx); sc[k]=e; sm+=e; }
  red[t]=sm; __syncthreads();
  for(int o2=64;o2>0;o2>>=1){ if(t<o2) red[t]+=red[t+o2]; __syncthreads(); }
  sm=red[0];
  float a=0; for(int k=0;k<W;k++) a+=sc[k]*bf2f(vc[((long)g*SK+(SK-W)+k)*DH+t]);
  o[row*(HQ*DH)+h*DH+t]=f2bf(a/sm);
}
__global__ void gelu_k(const float* u,const u16* b1,u16* ub){ int i=blockIdx.x*256+threadIdx.x; int f=i%F; float x=u[i]+bf2f(b1[f]); float t=tanhf(0.7978845608028654f*(x+0.044715f*x*x*x)); ub[i]=f2bf(0.5f*x*(1.f+t)); }
__global__ void final_k(const u16* x,const float* s,const float* y,const u16* b2,u16* out){
  int i=blockIdx.x*256+threadIdx.x; int row=i/D, d=i%D; float yy=y[(long)d*S+row]+bf2f(b2[row]); float g=yy/(1.f+__expf(-yy)); out[i]=f2bf(bf2f(x[i])+s[i]*g);
}
extern "C" void kernel_launch(void* const* d_in, const int* in_sizes, int n_in, void* d_out, int out_size, void* d_ws, size_t ws_size, hipStream_t stream){
  const u16* x=(const u16*)d_in[0]; const u16* kc=(const u16*)d_in[1]; const u16* vc=(const u16*)d_in[2]; const u16* wq=(const u16*)d_in[3]; const u16* wo=(const u16*)d_in[4];
  const u16* w1=(const u16*)d_in[6]; const u16* b1=(const u16*)d_in[7]; const u16* w2=(const u16*)d_in[8]; const u16* b2=(const u16*)d_in[9];
  char* ws=(char*)d_ws;
  u16* xn=(u16*)ws; u16* h=(u16*)(ws+(2<<20)); float* qf=(float*)(ws+(4<<20)); u16* qb=(u16*)(ws+(40<<20)); u16* ob=(u16*)(ws+(60<<20)); float* sf=(float*)(ws+(80<<20));
  float* uf=(float*)(ws+(90<<20)); u16* ub=(u16*)(ws+(100<<20)); float* yf=(float*)(ws+(110<<20));
  ln_k<<<S,64,0,stream>>>(x,xn,h);
  gemm_k<<<dim3(4096/64,S),64,0,stream>>>(xn,D,1,wq,4096,1,qf,S,4096,D);
  cvt_k<<<S*4096/256,256,0,stream>>>(qf,qb,S*4096);
  attn_k<<<dim3(S,HQ),128,0,stream>>>(qb,kc,vc,ob);
  gemm_k<<<dim3(D/64,S),64,0,stream>>>(ob,4096,1,wo,D,1,sf,S,D,4096);
  gemm_k<<<dim3(F/64,D),64,0,stream>>>(h,1,D,w1,F,1,uf,D,F,S);
  gelu_k<<<D*F/256,256,0,stream>>>(uf,b1,ub);
  gemm_k<<<dim3(S/64,D),64,0,stream>>>(ub,F,1,w2,S,1,yf,D,S,F);
  final_k<<<S*D/256,256,0,stream>>>(x,sf,yf,b2,(u16*)d_out);
}
```

```cpp
#include <hip/hip_runtime.h>
#include <hip/hip_bf16.h>
typedef unsigned short u16;
typedef unsigned u32x4 __attribute__((ext_vector_type(4)));
typedef unsigned u32x2 __attribute__((ext_vector_type(2)));
typedef float f32x2 __attribute__((ext_vector_type(2)));
typedef __bf16 bf16x2 __attribute__((ext_vector_type(2)));

#define PREFETCH 3
#define K1_SC1 1
constexpr size_t WS_CNT = 35651584; constexpr int NCNT = 72;
constexpr size_t WS_XF = 0, WS_HF = 2097152, WS_UF = 10485760, WS_PF = 18874368;

__device__ __forceinline__ unsigned pk2(float lo, float hi) {
  f32x2 v = {lo, hi}; bf16x2 b = __builtin_convertvector(v, bf16x2); return __builtin_bit_cast(unsigned, b);
}
__device__ __forceinline__ float lo16(unsigned u) { return __uint_as_float(u << 16); }
__device__ __forceinline__ float hi16(unsigned u) { return __uint_as_float(u & 0xffff0000u); }
template <int CTRL, int RM> __device__ __forceinline__ float dpp_add(float v) {
  return v + __builtin_bit_cast(float, __builtin_amdgcn_update_dpp(0, __builtin_bit_cast(int, v), CTRL, RM, 0xf, false));
}
__device__ __forceinline__ void wsum2(float& a, float& b) {
  a = dpp_add<0x111, 0xf>(a); b = dpp_add<0x111, 0xf>(b);
  a = dpp_add<0x112, 0xf>(a); b = dpp_add<0x112, 0xf>(b);
  a = dpp_add<0x114, 0xf>(a); b = dpp_add<0x114, 0xf>(b);
  a = dpp_add<0x118, 0xf>(a); b = dpp_add<0x118, 0xf>(b);
  a = dpp_add<0x142, 0xa>(a); b = dpp_add<0x142, 0xa>(b);
  a = dpp_add<0x143, 0xc>(a); b = dpp_add<0x143, 0xc>(b);
  a = __builtin_bit_cast(float, __builtin_amdgcn_readlane(__builtin_bit_cast(int, a), 63));
  b = __builtin_bit_cast(float, __builtin_amdgcn_readlane(__builtin_bit_cast(int, b), 63));
}
__device__ __forceinline__ void st16(char* p, u32x4 v) {
  asm volatile("global_store_dwordx4 %0, %1, off sc1\n s_nop 1" :: "v"(p), "v"(v) : "memory");
}

__global__ __launch_bounds__(1024) void k1_ln(const u16* __restrict__ x, char* __restrict__ ws, const u16* __restrict__ wq) {
  constexpr int LDT = 512 + 8;
  __shared__ __attribute__((aligned(16))) u16 sx[16 * LDT];
  __shared__ __attribute__((aligned(16))) u16 sh[16 * LDT];
  const int tid = threadIdx.x, lane = tid & 63, w = tid >> 6, b = blockIdx.x;
  if (b >= 128) {
    unsigned long long code; unsigned clen;
    asm volatile("s_getpc_b64 s[40:41]\n s_add_u32 s40, s40, K2_START@rel32@lo+4\n s_addc_u32 s41, s41, K2_START@rel32@hi+12\n s_mov_b64 %0, s[40:41]\n s_mov_b32 %1, K2_END-K2_START"
                 : "=s"(code), "=s"(clen) :: "scc", "s40", "s41");
    const unsigned g = b & 7, n = ((b - 128) >> 3) * 256 + (tid & 255), role = tid >> 8;
    unsigned acc = 0;
    if (role == 0) { unsigned o = (((b - 128) >> 3) * 64 + (tid & 63)) * 64; if (tid < 64 && o < clen) acc ^= *(const __attribute__((address_space(1))) unsigned*)(code + o); }
    if (role == 1) acc ^= *(const unsigned*)((const char*)wq + (size_t)(n >> 3) * 8192 + g * 1024 + (n & 7) * 128);
    asm volatile("" :: "v"(acc));
    return;
  }
  if (b == 0 && tid < NCNT) *(unsigned*)(ws + WS_CNT + tid * 128) = 0u;
  const u32x4 raw = *(const u32x4*)(x + (size_t)(16 * b + w) * 512 + lane * 8);
  float v[8];
#pragma unroll
  for (int e = 0; e < 4; ++e) { v[2 * e] = lo16(raw[e]); v[2 * e + 1] = hi16(raw[e]); }
  const float piv = __builtin_bit_cast(float, __builtin_amdgcn_readfirstlane(__builtin_bit_cast(int, v[0])));
  float s1 = 0.f, s2 = 0.f;
#pragma unroll
  for (int e = 0; e < 8; ++e) { v[e] -= piv; s1 += v[e]; s2 += v[e] * v[e]; }
#pragma unroll
  for (int pass = 0; pass < 2; ++pass) {
    wsum2(s1, s2);
    const float m = s1 * (1.f / 512.f);
    const float r = __builtin_amdgcn_rsqf(fmaxf(s2 * (1.f / 512.f) - m * m, 0.f) + 1e-5f);
    u32x4 o; s1 = 0.f; s2 = 0.f;
#pragma unroll
    for (int e = 0; e < 4; ++e) {
      o[e] = pk2((v[2 * e] - m) * r, (v[2 * e + 1] - m) * r);
      v[2 * e] = lo16(o[e]); v[2 * e + 1] = hi16(o[e]);
      s1 += v[2 * e] + v[2 * e + 1]; s2 += v[2 * e] * v[2 * e] + v[2 * e + 1] * v[2 * e + 1];
    }
    *(u32x4*)((pass ? sh : sx) + w * LDT + lane * 8) = o;
  }
  __syncthreads();
  {
    const int c = lane & 15, g = lane >> 4;
    const u32x2 a = *(const u32x2*)(sx + c * LDT + 32 * w + 4 * g);
    const u32x2 e = *(const u32x2*)(sx + c * LDT + 32 * w + 16 + 4 * g);
    u32x4 o = {a[0], a[1], e[0], e[1]};
    st16(ws + WS_XF + (size_t)(b >> 2) * 65536 + w * 4096 + (b & 3) * 1024 + lane * 16, o);
  }
  {
    const int d16 = tid >> 5, l5 = tid & 31, g1 = l5 >> 4, d = d16 * 16 + (l5 & 15);
    u32x4 o;
#pragma unroll
    for (int e = 0; e < 4; ++e) {
      const int r0 = 8 * (e >> 1) + 4 * g1 + 2 * (e & 1);
      o[e] = (unsigned)sh[r0 * LDT + d] | ((unsigned)sh[(r0 + 1) * LDT + d] << 16);
    }
    st16(ws + WS_HF + (size_t)(b >> 1) * 114688 + d16 * 1024 + (b & 1) * 512 + l5 * 16, o);
  }
}

__global__ __launch_bounds__(256, 1) void k2_main(char* ws, const u16* wq, const u16* kc, const u16* vc, const u16* x, u16* out,
                                                  const u16* w1, const u16* pad, const u16* wo, const u16* b1, const u16* w2,
                                                  const u16* b2) {
  const unsigned wid = __builtin_amdgcn_readfirstlane(threadIdx.x >> 6);
  asm volatile(
    "K2_START:\n"
    "s_load_dwordx8 s[4:11], %0, 0\n"
    "s_load_dwordx4 s[16:19], %0, 32\n"
    "s_load_dwordx2 s[12:13], %0, 48\n"
    "s_lshr_b32 s97, %2, 3\n"
    "s_and_b32 s96, %2, 7\n"
    "v_mbcnt_lo_u32_b32 v240, -1, 0\n"
    "v_mbcnt_hi_u32_b32 v240, -1, v240\n"
    "v_lshlrev_b32 v224, 4, v240\n"
    "s_lshl_b32 s22, s97, 16\n"
    "s_lshl_b32 s95, %1, 12\n"
    "s_lshl_b32 s99, s97, 1\n"
    "s_add_u32 s99, s99, %1\n"
    "v_bfe_u32 v241, v240, 2, 3\n"
    "v_lshrrev_b32 v243, 3, v240\n"
    "v_and_b32 v243, 2, v243\n"
    "v_and_b32 v244, 3, v240\n"
    "v_xor_b32 v242, v243, v244\n"
    "v_lshlrev_b32 v242, 4, v242\n"
    "v_lshrrev_b32 v243, 5, v240\n"
    "v_lshl_add_u32 v242, v243, 6, v242\n"
    "v_lshl_add_u32 v231, v241, 13, v242\n"
    "s_lshl_b32 s84, s96, 2\n"
    "s_add_u32 s84, s84, %1\n"
    "s_lshl_b32 s84, s84, 8\n"
    "s_and_b32 s88, s97, 7\n"
    "s_lshl_b32 s88, s88, 19\n"
    "s_mul_i32 s94, %1, 24576\n"
    "s_add_u32 s94, s94, 65536\n"
    "s_waitcnt lgkmcnt(0)\n"
    "s_mov_b64 s[24:25], s[4:5]\n"
    "s_add_u32 s24, s24, s22\n"
    "s_addc_u32 s25, s25, 0\n"
    "s_and_b32 s25, s25, 0xffff\n"
    "s_mov_b32 s26, 65536\n"
    "s_mov_b32 s27, 0x20000\n"
    "s_add_u32 s23, s99, 0\n"
    "s_and_b32 s23, s23, 15\n"
    "s_add_u32 m0, s95, 0\n"
    "s_lshl_b32 s23, s23, 12\n"
    "buffer_load_dwordx4 v224, s[24:27], s23 offen offset:0 lds\n"
    "buffer_load_dwordx4 v224, s[24:27], s23 offen offset:1024 lds\n"
    "buffer_load_dwordx4 v224, s[24:27], s23 offen offset:2048 lds\n"
    "buffer_load_dwordx4 v224, s[24:27], s23 offen offset:3072 lds\n"
    "s_sub_u32 s85, 4194304, s84\n"
    "s_mov_b64 s[28:29], s[6:7]\n"
    "s_add_u32 s28, s28, s84\n"
    "s_addc_u32 s29, s29, 0\n"
    "s_and_b32 s29, s29, 0xffff\n"
    "s_mov_b32 s30, s85\n"
    "s_mov_b32 s31, 0x20000\n"
    "s_sub_u32 s85, 4129792, s84\n"
    "s_mov_b64 s[32:33], s[6:7]\n"
    "s_add_u32 s32, s32, s84\n"
    "s_addc_u32 s33, s33, 0\n"
    "s_add_u32 s32, s32, 64512\n"
    "s_addc_u32 s33, s33, 0\n"
    "s_and_b32 s33, s33, 0xffff\n"
    "s_mov_b32 s34, s85\n"
    "s_mov_b32 s35, 0x20000\n"
    "s_sub_u32 s85, 4065280, s84\n"
    "s_mov_b64 s[36:37], s[6:7]\n"
    "s_add_u32 s36, s36, s84\n"
    "s_addc_u32 s37, s37, 0\n"
    "s_add_u32 s36, s36, 129024\n"
    "s_addc_u32 s37, s37, 0\n"
    "s_and_b32 s37, s37, 0xffff\n"
    "s_mov_b32 s38, s85\n"
    "s_mov_b32 s39, 0x20000\n"
    "s_sub_u32 s85, 4000768, s84\n"
    "s_mov_b64 s[40:41], s[6:7]\n"
    "s_add_u32 s40, s40, s84\n"
    "s_addc_u32 s41, s41, 0\n"
    "s_add_u32 s40, s40, 193536\n"
    "s_addc_u32 s41, s41, 0\n"
    "s_and_b32 s41, s41, 0xffff\n"
    "s_mov_b32 s42, s85\n"
    "s_mov_b32 s43, 0x20000\n"
    "s_add_u32 m0, s94, 0\n"
    "s_nop 0\n"
    "buffer_load_dwordx4 v231, s[28:31], s88 offen offset:0 lds\n"
    "buffer_load_dwordx4 v231, s[32:35], s88 offen offset:1024 lds\n"
    "buffer_load_dwordx4 v231, s[36:39], s88 offen offset:2048 lds\n"
    "buffer_load_dwordx4 v231, s[40:43], s88 offen offset:3072 lds\n"
    "s_add_u32 s88, s88, 262144\n"
    "s_add_u32 m0, s94, 4096\n"
    "s_nop 0\n"
    "buffer_load_dwordx4 v231, s[28:31], s88 offen offset:0 lds\n"
    "buffer_load_dwordx4 v231, s[32:35], s88 offen offset:1024 lds\n"
    "buffer_load_dwordx4 v231, s[36:39], s88 offen offset:2048 lds\n"
    "buffer_load_dwordx4 v231, s[40:43], s88 offen offset:3072 lds\n"
    "s_sub_u32 s88, s88, 262016\n"
    "v_bfe_u32 v245, v240, 2, 2\n"
    "v_bfe_u32 v246, v240, 4, 1\n"
    "v_mul_u32_u24 v247, 288, v246\n"
    "v_lshl_add_u32 v247, v245, 6, v247\n"
    "v_lshl_add_u32 v247, v244, 3, v247\n"
    "v_lshl_add_u32 v228, v243, 10, v247\n"
    "v_add_u32 v228, s94, v228\n"
    "v_xor_b32 v229, 32, v228\n"
    "s_add_u32 m0, s94, 8192\n"
    "s_nop 0\n"
    "buffer_load_dwordx4 v231, s[28:31], s88 offen offset:0 lds\n"
    "buffer_load_dwordx4 v231, s[32:35], s88 offen offset:1024 lds\n"
    "buffer_load_dwordx4 v231, s[36:39], s88 offen offset:2048 lds\n"
    "buffer_load_dwordx4 v231, s[40:43], s88 offen offset:3072 lds\n"
    "s_add_u32 s88, s88, 262144\n"
    "s_add_u32 s23, s99, 4\n"
    "s_and_b32 s23, s23, 15\n"
    "s_add_u32 m0, s95, 16384\n"
    "s_lshl_b32 s23, s23, 12\n"
    "buffer_load_dwordx4 v224, s[24:27], s23 offen offset:0 lds\n"
    "buffer_load_dwordx4 v224, s[24:27], s23 offen offset:1024 lds\n"
    "buffer_load_dwordx4 v224, s[24:27], s23 offen offset:2048 lds\n"
    "buffer_load_dwordx4 v224, s[24:27], s23 offen offset:3072 lds\n"
    "s_waitcnt vmcnt(16)\n"
    "s_barrier\n"
    "s_waitcnt vmcnt(14)\n"
    "ds_read_b64_tr_b16 a[128:129], v228 offset:0\n"
    "s_waitcnt vmcnt(12)\n"
    "ds_read_b64_tr_b16 a[130:131], v228 offset:2048\n"
    "ds_read_b128 a[224:227], v224 offset:0\n"
    "s_waitcnt lgkmcnt(0)\n"
    "v_mfma_f32_16x16x32_bf16 v[0:3], a[128:131], a[224:227], 0\n"
    "s_lshl_b32 s22, %1, 11\n"
    "s_add_u32 m0, s94, 12288\n"
    "s_waitcnt vmcnt(10)\n"
    "ds_read_b64_tr_b16 a[144:145], v228 offset:4096\n"
    "s_waitcnt vmcnt(8)\n"
    "ds_read_b64_tr_b16 a[146:147], v228 offset:6144\n"
    "ds_read_b128 a[240:243], v224 offset:4096\n"
    "ds_read_b64_tr_b16 a[132:133], v229 offset:0\n"
    "ds_read_b64_tr_b16 a[134:135], v229 offset:2048\n"
    "ds_read_b64_tr_b16 a[148:149], v229 offset:4096\n"
    "ds_read_b64_tr_b16 a[150:151], v229 offset:6144\n"
    "ds_read_b128 a[228:231], v224 offset:1024\n"
    "ds_read_b64_tr_b16 a[136:137], v228 offset:512\n"
    "ds_read_b64_tr_b16 a[138:139], v228 offset:2560\n"
    "ds_read_b128 a[244:247], v224 offset:5120\n"
    "ds_read_b64_tr_b16 a[152:153], v228 offset:4608\n"
    "ds_read_b64_tr_b16 a[154:155], v228 offset:6656\n"
    "ds_read_b64_tr_b16 a[140:141], v229 offset:512\n"
    "ds_read_b64_tr_b16 a[142:143], v229 offset:2560\n"
    "s_waitcnt lgkmcnt(10)\n"
    "ds_read_b128 a[232:235], v224 offset:2048\n"
    "ds_read_b64_tr_b16 a[156:157], v229 offset:4608\n"
    "ds_read_b64_tr_b16 a[158:159], v229 offset:6656\n"
    "ds_read_b128 a[248:251], v224 offset:6144\n"
    "ds_read_b128 a[236:239], v224 offset:3072\n"
    "s_waitcnt lgkmcnt(10)\n"
    "ds_read_b128 a[252:255], v224 offset:7168\n"
    "buffer_load_dwordx4 v231, s[28:31], s88 offen offset:0 lds\n"
    "s_add_u32 s23, s99, 8\n"
    "s_and_b32 s23, s23, 15\n"
    "s_lshl_b32 s23, s23, 12\n"
    "v_mfma_f32_16x16x32_bf16 v[0:3], a[144:147], a[240:243], v[0:3]\n"
    "s_mov_b64 s[68:69], s[8:9]\n"
    "buffer_load_dwordx4 v231, s[36:39], s88 offen offset:2048 lds\n"
    "v_mfma_f32_16x16x32_bf16 v[8:11], a[128:131], a[228:231], 0\n"
    "s_mov_b64 s[72:73], s[8:9]\n"
    "v_bfe_u32 v252, v240, 3, 1\n"
    "buffer_load_dwordx4 v231, s[32:35], s88 offen offset:1024 lds\n"
    "s_waitcnt lgkmcnt(10)\n"
    "v_mfma_f32_16x16x32_bf16 v[8:11], a[144:147], a[244:247], v[8:11]\n"
    "s_and_b32 s89, s97, 15\n"
    "v_lshlrev_b32 v252, 12, v252\n"
    "buffer_load_dwordx4 v231, s[40:43], s88 offen offset:3072 lds\n"
    "s_add_u32 s88, s88, 262016\n"
    "s_and_b32 s88, s88, 4194303\n"
    "s_add_u32 m0, s95, 32768\n"
    "s_waitcnt lgkmcnt(5)\n"
    "v_mfma_f32_16x16x32_bf16 v[16:19], a[128:131], a[232:235], 0\n"
    "s_mov_b32 s70, 131072\n"
    "buffer_load_dwordx4 v224, s[24:27], s23 offen offset:0 lds\n"
    "s_waitcnt lgkmcnt(2)\n"
    "v_mfma_f32_16x16x32_bf16 v[16:19], a[144:147], a[248:251], v[16:19]\n"
    "s_mov_b32 s71, 0x20000\n"
    "ds_read_b128 a[192:195], v224 offset:8192\n"
    "s_lshl_b32 s89, s89, 13\n"
    "s_waitcnt lgkmcnt(2)\n"
    "v_mfma_f32_16x16x32_bf16 v[24:27], a[128:131], a[236:239], 0\n"
    "buffer_load_dwordx4 v224, s[24:27], s23 offen offset:1024 lds\n"
    "s_waitcnt lgkmcnt(1)\n"
    "v_mfma_f32_16x16x32_bf16 v[24:27], a[144:147], a[252:255], v[24:27]\n"
    "s_mov_b32 s74, 131968\n"
    "ds_read_b128 a[208:211], v224 offset:12288\n"
    "ds_read_b128 a[196:199], v224 offset:9216\n"
    "v_mfma_f32_16x16x32_bf16 v[4:7], a[132:135], a[224:227], 0\n"
    "buffer_load_dwordx4 v224, s[24:27], s23 offen offset:2048 lds\n"
    "v_mfma_f32_16x16x32_bf16 v[4:7], a[148:151], a[240:243], v[4:7]\n"
    "s_mov_b32 s75, 0x20000\n"
    "ds_read_b128 a[212:215], v224 offset:13312\n"
    "ds_read_b128 a[200:203], v224 offset:10240\n"
    "v_mfma_f32_16x16x32_bf16 v[12:15], a[132:135], a[228:231], 0\n"
    "buffer_load_dwordx4 v224, s[24:27], s23 offen offset:3072 lds\n"
    "s_add_u32 m0, s94, 16384\n"
    "s_add_u32 s23, s99, 12\n"
    "s_and_b32 s23, s23, 15\n"
    "s_lshl_b32 s23, s23, 12\n"
    "v_mfma_f32_16x16x32_bf16 v[12:15], a[148:151], a[244:247], v[12:15]\n"
    "buffer_load_dwordx4 v231, s[28:31], s88 offen offset:0 lds\n"
    "v_mfma_f32_16x16x32_bf16 v[20:23], a[132:135], a[232:235], 0\n"
    "s_mov_b64 s[76:77], s[10:11]\n"
    "s_waitcnt vmcnt(15)\n"
    "ds_read_b64_tr_b16 a[160:161], v228 offset:8192\n"
    "s_waitcnt vmcnt(13)\n"
    "ds_read_b64_tr_b16 a[162:163], v228 offset:10240\n"
    "v_mfma_f32_16x16x32_bf16 v[20:23], a[148:151], a[248:251], v[20:23]\n"
    "buffer_load_dwordx4 v231, s[36:39], s88 offen offset:2048 lds\n"
    "v_mfma_f32_16x16x32_bf16 v[28:31], a[132:135], a[236:239], 0\n"
    "s_mov_b64 s[80:81], s[10:11]\n"
    "s_waitcnt vmcnt(7)\n"
    "ds_read_b64_tr_b16 a[176:177], v228 offset:12288\n"
    "s_waitcnt vmcnt(6)\n"
    "ds_read_b64_tr_b16 a[178:179], v228 offset:14336\n"
    "v_mfma_f32_16x16x32_bf16 v[28:31], a[148:151], a[252:255], v[28:31]\n"
    "buffer_load_dwordx4 v231, s[32:35], s88 offen offset:1024 lds\n"
    "v_mfma_f32_16x16x32_bf16 v[32:35], a[136:139], a[224:227], 0\n"
    "s_add_u32 s90, s89, 8192\n"
    "ds_read_b64_tr_b16 a[164:165], v229 offset:8192\n"
    "ds_read_b64_tr_b16 a[166:167], v229 offset:10240\n"
    "v_mfma_f32_16x16x32_bf16 v[32:35], a[152:155], a[240:243], v[32:35]\n"
    "buffer_load_dwordx4 v231, s[40:43], s88 offen offset:3072 lds\n"
    "s_add_u32 s88, s88, 262144\n"
    "s_add_u32 m0, s95, 49152\n"
    "v_mfma_f32_16x16x32_bf16 v[40:43], a[136:139], a[228:231], 0\n"
    "s_and_b32 s90, s90, 131071\n"
    "ds_read_b64_tr_b16 a[180:181], v229 offset:12288\n"
    "buffer_load_dwordx4 v224, s[24:27], s23 offen offset:0 lds\n"
    "v_mfma_f32_16x16x32_bf16 v[40:43], a[152:155], a[244:247], v[40:43]\n"
    "s_mov_b32 s78, 133120\n"
    "ds_read_b64_tr_b16 a[182:183], v229 offset:14336\n"
    "ds_read_b64_tr_b16 a[168:169], v228 offset:8704\n"
    "v_mfma_f32_16x16x32_bf16 v[48:51], a[136:139], a[232:235], 0\n"
    "buffer_load_dwordx4 v224, s[24:27], s23 offen offset:1024 lds\n"
    "v_mfma_f32_16x16x32_bf16 v[48:51], a[152:155], a[248:251], v[48:51]\n"
    "s_mov_b32 s79, 0x20000\n"
    "ds_read_b64_tr_b16 a[170:171], v228 offset:10752\n"
    "s_waitcnt lgkmcnt(6)\n"
    "ds_read_b64_tr_b16 a[184:185], v228 offset:12800\n"
    "v_mfma_f32_16x16x32_bf16 v[56:59], a[136:139], a[236:239], 0\n"
    "buffer_load_dwordx4 v224, s[24:27], s23 offen offset:2048 lds\n"
    "v_mfma_f32_16x16x32_bf16 v[56:59], a[152:155], a[252:255], v[56:59]\n"
    "s_mov_b32 s82, 134016\n"
    "ds_read_b64_tr_b16 a[186:187], v228 offset:14848\n"
    "ds_read_b64_tr_b16 a[172:173], v229 offset:8704\n"
    "ds_read_b64_tr_b16 a[174:175], v229 offset:10752\n"
    "buffer_load_dwordx4 v224, s[24:27], s23 offen offset:3072 lds\n"
    "s_add_u32 m0, s94, 20480\n"
    "v_mfma_f32_16x16x32_bf16 v[36:39], a[140:143], a[224:227], 0\n"
    "s_and_b32 s23, %1, 1\n"
    "ds_read_b64_tr_b16 a[188:189], v229 offset:12800\n"
    "ds_read_b64_tr_b16 a[190:191], v229 offset:14848\n"
    "buffer_load_dwordx4 v231, s[28:31], s88 offen offset:0 lds\n"
    "v_mfma_f32_16x16x32_bf16 v[36:39], a[156:159], a[240:243], v[36:39]\n"
    "s_mul_i32 s23, s23, 3\n"
    "ds_read_b128 a[216:219], v224 offset:14336\n"
    "ds_read_b128 a[204:207], v224 offset:11264\n"
    "v_mfma_f32_16x16x32_bf16 v[44:47], a[140:143], a[228:231], 0\n"
    "buffer_load_dwordx4 v231, s[36:39], s88 offen offset:2048 lds\n"
    "v_mfma_f32_16x16x32_bf16 v[44:47], a[156:159], a[244:247], v[44:47]\n"
    "s_mov_b32 s83, 0x20000\n"
    "v_and_b32 v253, 7, v240\n"
    "buffer_load_dwordx4 v231, s[32:35], s88 offen offset:1024 lds\n"
    "v_mfma_f32_16x16x32_bf16 v[52:55], a[140:143], a[232:235], 0\n"
    "s_mul_i32 s84, %1, 1835008\n"
    "v_lshl_add_u32 v252, v253, 6, v252\n"
    "buffer_load_dwordx4 v231, s[40:43], s88 offen offset:3072 lds\n"
    "v_mfma_f32_16x16x32_bf16 v[52:55], a[156:159], a[248:251], v[52:55]\n"
    "s_add_u32 m0, s94, 0\n"
    "v_lshrrev_b32 v253, 1, v245\n"
    "v_xor_b32 v253, v253, v245\n"
    "v_mfma_f32_16x16x32_bf16 v[60:63], a[140:143], a[236:239], 0\n"
    "s_sub_u32 s88, s88, 262016\n"
    "v_lshl_add_u32 v254, v246, 1, v243\n"
    "v_xor_b32 v253, v253, v254\n"
    "v_mfma_f32_16x16x32_bf16 v[60:63], a[156:159], a[252:255], v[60:63]\n"
    "buffer_load_dwordx4 v231, s[28:31], s88 offen offset:0 lds\n"
    "v_mfma_f32_16x16x32_bf16 v[64:67], a[160:163], a[224:227], 0\n"
    "s_mov_b64 s[44:45], s[12:13]\n"
    "v_lshl_add_u32 v225, v253, 4, v252\n"
    "v_xor_b32 v253, s23, v246\n"
    "v_mfma_f32_16x16x32_bf16 v[64:67], a[176:179], a[240:243], v[64:67]\n"
    "s_mov_b64 s[48:49], s[12:13]\n"
    "v_xor_b32 v253, v253, v244\n"
    "v_lshlrev_b32 v253, 4, v253\n"
    "v_mfma_f32_16x16x32_bf16 v[72:75], a[160:163], a[228:231], 0\n"
    "buffer_load_dwordx4 v231, s[36:39], s88 offen offset:2048 lds\n"
    "v_mfma_f32_16x16x32_bf16 v[72:75], a[176:179], a[244:247], v[72:75]\n"
    "s_mov_b64 s[52:53], s[12:13]\n"
    "v_lshl_add_u32 v252, v241, 8, v242\n"
    "v_lshl_add_u32 v253, v243, 6, v253\n"
    "v_mfma_f32_16x16x32_bf16 v[80:83], a[160:163], a[232:235], 0\n"
    "s_mov_b64 s[56:57], s[12:13]\n"
    "v_add_u32 v235, s22, v252\n"
    "v_lshl_add_u32 v253, v241, 8, v253\n"
    "v_mfma_f32_16x16x32_bf16 v[80:83], a[176:179], a[248:251], v[80:83]\n"
    "buffer_load_dwordx4 v231, s[32:35], s88 offen offset:1024 lds\n"
    "v_mfma_f32_16x16x32_bf16 v[88:91], a[160:163], a[236:239], 0\n"
    "s_mov_b64 s[60:61], s[4:5]\n"
    "v_add_u32 v234, s22, v253\n"
    "v_mul_u32_u24 v253, 3584, v241\n"
    "v_mfma_f32_16x16x32_bf16 v[88:91], a[176:179], a[252:255], v[88:91]\n"
    "s_lshr_b32 s22, %1, 1\n"
    "v_lshl_add_u32 v233, v241, 10, v242\n"
    "v_add_u32 v230, s94, v224\n"
    "s_waitcnt lgkmcnt(0)\n"
    "v_mfma_f32_16x16x32_bf16 v[68:71], a[164:167], a[224:227], 0\n"
    "buffer_load_dwordx4 v231, s[40:43], s88 offen offset:3072 lds\n"
    "v_mfma_f32_16x16x32_bf16 v[68:71], a[180:183], a[240:243], v[68:71]\n"
    "s_add_u32 m0, s94, 4096\n"
    "v_lshl_add_u32 v226, v243, 12, v247\n"
    "v_xor_b32 v227, 32, v226\n"
    "v_mfma_f32_16x16x32_bf16 v[76:79], a[164:167], a[228:231], 0\n"
    "s_add_u32 s88, s88, 262144\n"
    "v_mov_b32 v248, 0x3f803f80\n"
    "v_mov_b32 v249, 0x3f803f80\n"
    "v_mfma_f32_16x16x32_bf16 v[76:79], a[180:183], a[244:247], v[76:79]\n"
    "buffer_load_dwordx4 v231, s[28:31], s88 offen offset:0 lds\n"
    "v_mfma_f32_16x16x32_bf16 v[84:87], a[164:167], a[232:235], 0\n"
    "s_lshl_b32 s22, s22, 6\n"
    "s_waitcnt vmcnt(14)\n"
    "ds_read_b64_tr_b16 a[128:129], v228 offset:16384\n"
    "s_waitcnt vmcnt(13)\n"
    "ds_read_b64_tr_b16 a[130:131], v228 offset:18432\n"
    "v_mfma_f32_16x16x32_bf16 v[84:87], a[180:183], a[248:251], v[84:87]\n"
    "s_mov_b32 s47, 0x20000\n"
    "s_waitcnt vmcnt(6)\n"
    "ds_read_b64_tr_b16 a[144:145], v228 offset:20480\n"
    "s_waitcnt vmcnt(5)\n"
    "ds_read_b64_tr_b16 a[146:147], v228 offset:22528\n"
    "v_mfma_f32_16x16x32_bf16 v[92:95], a[164:167], a[236:239], 0\n"
    "buffer_load_dwordx4 v231, s[36:39], s88 offen offset:2048 lds\n"
    "v_mfma_f32_16x16x32_bf16 v[92:95], a[180:183], a[252:255], v[92:95]\n"
    "s_mov_b32 s55, 0x20000\n"
    "ds_read_b64_tr_b16 a[132:133], v229 offset:16384\n"
    "ds_read_b64_tr_b16 a[134:135], v229 offset:18432\n"
    "v_mfma_f32_16x16x32_bf16 v[96:99], a[168:171], a[224:227], 0\n"
    "buffer_load_dwordx4 v231, s[32:35], s88 offen offset:1024 lds\n"
    "v_mfma_f32_16x16x32_bf16 v[96:99], a[184:187], a[240:243], v[96:99]\n"
    "s_mov_b32 s51, 0x20000\n"
    "ds_read_b64_tr_b16 a[148:149], v229 offset:20480\n"
    "ds_read_b64_tr_b16 a[150:151], v229 offset:22528\n"
    "v_mfma_f32_16x16x32_bf16 v[104:107], a[168:171], a[228:231], 0\n"
    "buffer_load_dwordx4 v231, s[40:43], s88 offen offset:3072 lds\n"
    "v_mfma_f32_16x16x32_bf16 v[104:107], a[184:187], a[244:247], v[104:107]\n"
    "s_add_u32 s88, s88, 262016\n"
    "ds_read_b64_tr_b16 a[136:137], v228 offset:16896\n"
    "ds_read_b64_tr_b16 a[138:139], v228 offset:18944\n"
    "v_mfma_f32_16x16x32_bf16 v[112:115], a[168:171], a[232:235], 0\n"
    "s_add_u32 m0, s94, 8192\n"
    "ds_read_b64_tr_b16 a[152:153], v228 offset:20992\n"
    "ds_read_b64_tr_b16 a[154:155], v228 offset:23040\n"
    "v_mfma_f32_16x16x32_bf16 v[112:115], a[184:187], a[248:251], v[112:115]\n"
    "s_and_b32 s88, s88, 4194303\n"
    "ds_read_b64_tr_b16 a[140:141], v229 offset:16896\n"
    "ds_read_b64_tr_b16 a[142:143], v229 offset:18944\n"
    "v_mfma_f32_16x16x32_bf16 v[120:123], a[168:171], a[236:239], 0\n"
    "buffer_load_dwordx4 v231, s[28:31], s88 offen offset:0 lds\n"
    "v_mfma_f32_16x16x32_bf16 v[120:123], a[184:187], a[252:255], v[120:123]\n"
    "s_mov_b64 s[64:65], s[4:5]\n"
    "ds_read_b64_tr_b16 a[156:157], v229 offset:20992\n"
    "s_waitcnt lgkmcnt(7)\n"
    "ds_read_b64_tr_b16 a[158:159], v229 offset:23040\n"
    "v_mfma_f32_16x16x32_bf16 v[100:103], a[172:175], a[224:227], 0\n"
    "s_mov_b32 s59, 0x20000\n"
    "ds_read_b128 a[220:223], v224 offset:15360\n"
    "s_mov_b32 s63, 0x20000\n"
    "v_mfma_f32_16x16x32_bf16 v[100:103], a[188:191], a[240:243], v[100:103]\n"
    "buffer_load_dwordx4 v231, s[36:39], s88 offen offset:2048 lds\n"
    "v_mfma_f32_16x16x32_bf16 v[108:111], a[172:175], a[228:231], 0\n"
    "s_mov_b32 s67, 0x20000\n"
    "v_xor_b32 v252, s22, v242\n"
    "v_add_u32 v232, v252, v253\n"
    "v_mfma_f32_16x16x32_bf16 v[108:111], a[188:191], a[244:247], v[108:111]\n"
    "s_lshl_b32 s22, s96, 19\n"
    "v_mov_b32 v250, 0x3f803f80\n"
    "v_mov_b32 v251, 0x3f803f80\n"
    "v_mfma_f32_16x16x32_bf16 v[116:119], a[172:175], a[232:235], 0\n"
    "buffer_load_dwordx4 v231, s[32:35], s88 offen offset:1024 lds\n"
    "v_mfma_f32_16x16x32_bf16 v[116:119], a[188:191], a[248:251], v[116:119]\n"
    "s_mov_b32 s99, 0x3e0293ee\n"
    "v_lshrrev_b32 v254, 5, v224\n"
    "v_and_b32 v254, 24, v254\n"
    "v_mfma_f32_16x16x32_bf16 v[124:127], a[172:175], a[236:239], 0\n"
    "s_load_dwordx8 s[8:15], %0, 64\n"
    "s_add_u32 s68, s68, s22\n"
    "s_addc_u32 s69, s69, 0\n"
    "v_mfma_f32_16x16x32_bf16 v[124:127], a[188:191], a[252:255], v[124:127]\n"
    "buffer_load_dwordx4 v231, s[40:43], s88 offen offset:3072 lds\n"
    "v_mfma_f32_16x16x32_bf16 v[0:3], a[128:131], a[192:195], v[0:3]\n"
    "s_add_u32 m0, s94, 12288\n"
    "s_add_u32 s88, s88, 262144\n"
    "s_waitcnt lgkmcnt(0)\n"
    "s_mov_b64 s[24:25], s[10:11]\n"
    "v_mfma_f32_16x16x32_bf16 v[0:3], a[144:147], a[208:211], v[0:3]\n"
    "s_mov_b32 s27, 0x20000\n"
    "s_add_u32 s72, s72, s22\n"
    "s_addc_u32 s73, s73, 0\n"
    "v_mfma_f32_16x16x32_bf16 v[8:11], a[128:131], a[196:199], v[8:11]\n"
    "buffer_load_dwordx4 v231, s[28:31], s88 offen offset:0 lds\n"
    "v_mfma_f32_16x16x32_bf16 v[8:11], a[144:147], a[212:215], v[8:11]\n"
    "s_mov_b32 s86, 0\n"
    "s_add_u32 s68, s68, 393216\n"
    "s_addc_u32 s69, s69, 0\n"
    "v_mfma_f32_16x16x32_bf16 v[16:19], a[128:131], a[200:203], v[16:19]\n"
    "s_and_b32 s69, s69, 0xffff\n"
    "s_add_u32 s72, s72, 392320\n"
    "s_addc_u32 s73, s73, 0\n"
    "v_mfma_f32_16x16x32_bf16 v[16:19], a[144:147], a[216:219], v[16:19]\n"
    "buffer_load_dwordx4 v231, s[36:39], s88 offen offset:2048 lds\n"
    "v_mfma_f32_16x16x32_bf16 v[24:27], a[128:131], a[204:207], v[24:27]\n"
    "s_and_b32 s73, s73, 0xffff\n"
    "s_add_u32 s76, s76, s22\n"
    "s_addc_u32 s77, s77, 0\n"
    "v_mfma_f32_16x16x32_bf16 v[24:27], a[144:147], a[220:223], v[24:27]\n"
    "s_mov_b32 s87, 0\n"
    "s_add_u32 s80, s80, s22\n"
    "s_addc_u32 s81, s81, 0\n"
    "v_mfma_f32_16x16x32_bf16 v[4:7], a[132:135], a[192:195], v[4:7]\n"
    "buffer_load_dwordx4 v231, s[32:35], s88 offen offset:1024 lds\n"
    "v_mfma_f32_16x16x32_bf16 v[4:7], a[148:151], a[208:211], v[4:7]\n"
    "s_mul_i32 s22, s96, 28\n"
    "s_add_u32 s76, s76, 391168\n"
    "s_addc_u32 s77, s77, 0\n"
    "v_mfma_f32_16x16x32_bf16 v[12:15], a[132:135], a[196:199], v[12:15]\n"
    "s_and_b32 s77, s77, 0xffff\n"
    "s_add_u32 s80, s80, 390272\n"
    "s_addc_u32 s81, s81, 0\n"
    "v_mfma_f32_16x16x32_bf16 v[12:15], a[148:151], a[212:215], v[12:15]\n"
    "buffer_load_dwordx4 v231, s[40:43], s88 offen offset:3072 lds\n"
    "v_mfma_f32_16x16x32_bf16 v[20:23], a[132:135], a[200:203], v[20:23]\n"
    "s_add_u32 m0, s94, 16384\n"
    "s_waitcnt vmcnt(13)\n"
    "ds_read_b64_tr_b16 a[160:161], v228 offset:0\n"
    "s_waitcnt vmcnt(12)\n"
    "ds_read_b64_tr_b16 a[162:163], v228 offset:2048\n"
    "v_mfma_f32_16x16x32_bf16 v[20:23], a[148:151], a[216:219], v[20:23]\n"
    "s_sub_u32 s88, s88, 262016\n"
    "s_waitcnt vmcnt(9)\n"
    "ds_read_b64_tr_b16 a[176:177], v228 offset:4096\n"
    "s_waitcnt vmcnt(8)\n"
    "ds_read_b64_tr_b16 a[178:179], v228 offset:6144\n"
    "v_mfma_f32_16x16x32_bf16 v[28:31], a[132:135], a[204:207], v[28:31]\n"
    "buffer_load_dwordx4 v231, s[28:31], s88 offen offset:0 lds\n"
    "v_mfma_f32_16x16x32_bf16 v[28:31], a[148:151], a[220:223], v[28:31]\n"
    "s_and_b32 s81, s81, 0xffff\n"
    "ds_read_b64_tr_b16 a[164:165], v229 offset:0\n"
    "ds_read_b64_tr_b16 a[166:167], v229 offset:2048\n"
    "v_mfma_f32_16x16x32_bf16 v[32:35], a[136:139], a[192:195], v[32:35]\n"
    "s_add_u32 s22, s22, s97\n"
    "ds_read_b64_tr_b16 a[180:181], v229 offset:4096\n"
    "ds_read_b64_tr_b16 a[182:183], v229 offset:6144\n"
    "v_mfma_f32_16x16x32_bf16 v[32:35], a[152:155], a[208:211], v[32:35]\n"
    "buffer_load_dwordx4 v231, s[36:39], s88 offen offset:2048 lds\n"
    "v_mfma_f32_16x16x32_bf16 v[40:43], a[136:139], a[196:199], v[40:43]\n"
    "s_lshr_b32 s92, s22, 3\n"
    "ds_read_b64_tr_b16 a[168:169], v228 offset:512\n"
    "ds_read_b64_tr_b16 a[170:171], v228 offset:2560\n"
    "v_mfma_f32_16x16x32_bf16 v[40:43], a[152:155], a[212:215], v[40:43]\n"
    "s_and_b32 s93, s22, 7\n"
    "ds_read_b64_tr_b16 a[184:185], v228 offset:4608\n"
    "ds_read_b64_tr_b16 a[186:187], v228 offset:6656\n"
    "v_mfma_f32_16x16x32_bf16 v[48:51], a[136:139], a[200:203], v[48:51]\n"
    "buffer_load_dwordx4 v231, s[32:35], s88 offen offset:1024 lds\n"
    "v_mfma_f32_16x16x32_bf16 v[48:51], a[152:155], a[216:219], v[48:51]\n"
    "s_lshl_b32 s22, s92, 7\n"
    "ds_read_b64_tr_b16 a[172:173], v229 offset:512\n"
    "ds_read_b64_tr_b16 a[174:175], v229 offset:2560\n"
    "v_mfma_f32_16x16x32_bf16 v[56:59], a[136:139], a[204:207], v[56:59]\n"
    "s_add_u32 s22, s84, s22\n"
    "ds_read_b64_tr_b16 a[188:189], v229 offset:4608\n"
    "s_waitcnt lgkmcnt(7)\n"
    "ds_read_b64_tr_b16 a[190:191], v229 offset:6656\n"
    "v_mfma_f32_16x16x32_bf16 v[56:59], a[152:155], a[220:223], v[56:59]\n"
    "buffer_load_dwordx4 v231, s[40:43], s88 offen offset:3072 lds\n"
    "v_mfma_f32_16x16x32_bf16 v[36:39], a[140:143], a[192:195], v[36:39]\n"
    "s_add_u32 m0, s94, 20480\n"
    "s_add_u32 s88, s88, 262144\n"
    "s_sub_u32 s23, 7340032, s22\n"
    "v_mfma_f32_16x16x32_bf16 v[36:39], a[156:159], a[208:211], v[36:39]\n"
    "s_and_b32 s6, s93, 7\n"
    "s_cmp_lt_u32 s97, 28\n"
    "s_cselect_b32 s85, -1, 0\n"
    "v_mfma_f32_16x16x32_bf16 v[44:47], a[140:143], a[196:199], v[44:47]\n"
    "buffer_load_dwordx4 v231, s[28:31], s88 offen offset:0 lds\n"
    "v_mfma_f32_16x16x32_bf16 v[44:47], a[156:159], a[212:215], v[44:47]\n"
    "s_and_b32 s23, s23, s85\n"
    "s_mov_b32 s46, s23\n"
    "s_sub_u32 s23, 7283712, s22\n"
    "v_mfma_f32_16x16x32_bf16 v[52:55], a[140:143], a[200:203], v[52:55]\n"
    "s_and_b32 s23, s23, s85\n"
    "s_mov_b32 s50, s23\n"
    "s_sub_u32 s23, 7313408, s22\n"
    "v_mfma_f32_16x16x32_bf16 v[52:55], a[156:159], a[216:219], v[52:55]\n"
    "buffer_load_dwordx4 v231, s[36:39], s88 offen offset:2048 lds\n"
    "v_mfma_f32_16x16x32_bf16 v[60:63], a[140:143], a[204:207], v[60:63]\n"
    "s_and_b32 s23, s23, s85\n"
    "s_mov_b32 s54, s23\n"
    "s_sub_u32 s23, 7257088, s22\n"
    "v_mfma_f32_16x16x32_bf16 v[60:63], a[156:159], a[220:223], v[60:63]\n"
    "s_and_b32 s23, s23, s85\n"
    "s_add_u32 s44, s44, s22\n"
    "s_addc_u32 s45, s45, 0\n"
    "s_barrier\n"
    "v_mfma_f32_16x16x32_bf16 v[64:67], a[160:163], a[192:195], v[64:67]\n"
    "buffer_load_dwordx4 v231, s[32:35], s88 offen offset:1024 lds\n"
    "v_mfma_f32_16x16x32_bf16 v[64:67], a[176:179], a[208:211], v[64:67]\n"
    "s_mov_b32 s58, s23\n"
    "ds_read_b128 a[224:227], v224 offset:16384\n"
    "ds_read_b128 a[240:243], v224 offset:20480\n"
    "v_mfma_f32_16x16x32_bf16 v[72:75], a[160:163], a[196:199], v[72:75]\n"
    "s_and_b32 s23, s93, 1\n"
    "ds_read_b128 a[228:231], v224 offset:17408\n"
    "ds_read_b128 a[244:247], v224 offset:21504\n"
    "v_mfma_f32_16x16x32_bf16 v[72:75], a[176:179], a[212:215], v[72:75]\n"
    "buffer_load_dwordx4 v231, s[40:43], s88 offen offset:3072 lds\n"
    "v_mfma_f32_16x16x32_bf16 v[80:83], a[160:163], a[200:203], v[80:83]\n"
    "s_add_u32 s88, s88, 262016\n"
    "ds_read_b128 a[232:235], v224 offset:18432\n"
    "ds_read_b128 a[248:251], v224 offset:22528\n"
    "v_mfma_f32_16x16x32_bf16 v[80:83], a[176:179], a[216:219], v[80:83]\n"
    "s_add_u32 m0, s94, 0\n"
    "ds_read_b128 a[236:239], v224 offset:19456\n"
    "s_and_b32 s88, s88, 4194303\n"
    "v_mfma_f32_16x16x32_bf16 v[88:91], a[160:163], a[204:207], v[88:91]\n"
    "s_waitcnt lgkmcnt(7)\n"
    "buffer_load_dwordx4 v231, s[28:31], s88 offen offset:0 lds\n"
    "v_mfma_f32_16x16x32_bf16 v[88:91], a[176:179], a[220:223], v[88:91]\n"
    "s_lshl_b32 s23, s23, 11\n"
    "s_add_u32 s48, s48, s22\n"
    "s_addc_u32 s49, s49, 0\n"
    "v_mfma_f32_16x16x32_bf16 v[68:71], a[164:167], a[192:195], v[68:71]\n"
    "s_mul_i32 s6, s6, 1\n"
    "s_add_u32 s52, s52, s22\n"
    "s_addc_u32 s53, s53, 0\n"
    "v_mfma_f32_16x16x32_bf16 v[68:71], a[180:183], a[208:211], v[68:71]\n"
    "buffer_load_dwordx4 v231, s[36:39], s88 offen offset:2048 lds\n"
    "v_mfma_f32_16x16x32_bf16 v[76:79], a[164:167], a[196:199], v[76:79]\n"
    "s_and_b32 s45, s45, 0xffff\n"
    "s_add_u32 s56, s56, s22\n"
    "s_addc_u32 s57, s57, 0\n"
    "v_mfma_f32_16x16x32_bf16 v[76:79], a[180:183], a[212:215], v[76:79]\n"
    "s_lshr_b32 s22, s93, 1\n"
    "s_lshl_b32 s22, s22, 13\n"
    "s_add_u32 s22, s22, s23\n"
    "v_mfma_f32_16x16x32_bf16 v[84:87], a[164:167], a[200:203], v[84:87]\n"
    "buffer_load_dwordx4 v231, s[32:35], s88 offen offset:1024 lds\n"
    "v_mfma_f32_16x16x32_bf16 v[84:87], a[180:183], a[216:219], v[84:87]\n"
    "s_add_u32 s84, s84, s22\n"
    "s_waitcnt vmcnt(16)\n"
    "ds_read_b64_tr_b16 a[128:129], v228 offset:8192\n"
    "s_waitcnt vmcnt(15)\n"
    "ds_read_b64_tr_b16 a[130:131], v228 offset:10240\n"
    "v_mfma_f32_16x16x32_bf16 v[92:95], a[164:167], a[204:207], v[92:95]\n"
    "s_and_b32 s22, %1, 1\n"
    "s_waitcnt vmcnt(12)\n"
    "ds_read_b64_tr_b16 a[144:145], v228 offset:12288\n"
    "s_waitcnt vmcnt(11)\n"
    "ds_read_b64_tr_b16 a[146:147], v228 offset:14336\n"
    "v_mfma_f32_16x16x32_bf16 v[92:95], a[180:183], a[220:223], v[92:95]\n"
    "buffer_load_dwordx4 v231, s[40:43], s88 offen offset:3072 lds\n"
    "v_mfma_f32_16x16x32_bf16 v[96:99], a[168:171], a[192:195], v[96:99]\n"
    "s_add_u32 m0, s94, 4096\n"
    "ds_read_b64_tr_b16 a[132:133], v229 offset:8192\n"
    "ds_read_b64_tr_b16 a[134:135], v229 offset:10240\n"
    "v_mfma_f32_16x16x32_bf16 v[96:99], a[184:187], a[208:211], v[96:99]\n"
    "s_add_u32 s88, s88, 262144\n"
    "ds_read_b64_tr_b16 a[148:149], v229 offset:12288\n"
    "ds_read_b64_tr_b16 a[150:151], v229 offset:14336\n"
    "v_mfma_f32_16x16x32_bf16 v[104:107], a[168:171], a[196:199], v[104:107]\n"
    "buffer_load_dwordx4 v231, s[28:31], s88 offen offset:0 lds\n"
    "v_mfma_f32_16x16x32_bf16 v[104:107], a[184:187], a[212:215], v[104:107]\n"
    "s_lshl_b32 s22, s22, 12\n"
    "s_waitcnt lgkmcnt(6)\n"
    "ds_read_b64_tr_b16 a[136:137], v228 offset:8704\n"
    "ds_read_b64_tr_b16 a[138:139], v228 offset:10752\n"
    "v_mfma_f32_16x16x32_bf16 v[112:115], a[168:171], a[200:203], v[112:115]\n"
    "s_mul_i32 s91, s6, 229376\n"
    "ds_read_b64_tr_b16 a[152:153], v228 offset:12800\n"
    "ds_read_b64_tr_b16 a[154:155], v228 offset:14848\n"
    "v_mfma_f32_16x16x32_bf16 v[112:115], a[184:187], a[216:219], v[112:115]\n"
    "buffer_load_dwordx4 v231, s[36:39], s88 offen offset:2048 lds\n"
    "v_mfma_f32_16x16x32_bf16 v[120:123], a[168:171], a[204:207], v[120:123]\n"
    "s_and_b32 s23, s85, 1835008\n"
    "ds_read_b64_tr_b16 a[140:141], v229 offset:8704\n"
    "ds_read_b64_tr_b16 a[142:143], v229 offset:10752\n"
    "v_mfma_f32_16x16x32_bf16 v[120:123], a[184:187], a[220:223], v[120:123]\n"
    "s_mov_b32 s62, s23\n"
    "ds_read_b64_tr_b16 a[156:157], v229 offset:12800\n"
    "ds_read_b64_tr_b16 a[158:159], v229 offset:14848\n"
    "v_mfma_f32_16x16x32_bf16 v[100:103], a[172:175], a[192:195], v[100:103]\n"
    "buffer_load_dwordx4 v231, s[32:35], s88 offen offset:1024 lds\n"
    "v_mfma_f32_16x16x32_bf16 v[100:103], a[188:191], a[208:211], v[100:103]\n"
    "s_mov_b32 s66, s23\n"
    "ds_read_b128 a[252:255], v224 offset:23552\n"
    "s_waitcnt lgkmcnt(7)\n"
    "ds_read_b128 a[192:195], v224 offset:24576\n"
    "v_mfma_f32_16x16x32_bf16 v[108:111], a[172:175], a[196:199], v[108:111]\n"
    "s_add_u32 s6, s6, 1\n"
    "ds_read_b128 a[208:211], v224 offset:28672\n"
    "ds_read_b128 a[196:199], v224 offset:25600\n"
    "v_mfma_f32_16x16x32_bf16 v[108:111], a[188:191], a[212:215], v[108:111]\n"
    "buffer_load_dwordx4 v231, s[40:43], s88 offen offset:3072 lds\n"
    "v_mfma_f32_16x16x32_bf16 v[116:119], a[172:175], a[200:203], v[116:119]\n"
    "s_add_u32 m0, s94, 8192\n"
    "ds_read_b128 a[212:215], v224 offset:29696\n"
    "ds_read_b128 a[200:203], v224 offset:26624\n"
    "v_mfma_f32_16x16x32_bf16 v[116:119], a[188:191], a[216:219], v[116:119]\n"
    "s_sub_u32 s88, s88, 262016\n"
    "s_add_u32 s60, s60, s84\n"
    "s_addc_u32 s61, s61, 0\n"
    "v_mfma_f32_16x16x32_bf16 v[124:127], a[172:175], a[204:207], v[124:127]\n"
    "s_waitcnt lgkmcnt(6)\n"
    "buffer_load_dwordx4 v231, s[28:31], s88 offen offset:0 lds\n"
    "v_mfma_f32_16x16x32_bf16 v[124:127], a[188:191], a[220:223], v[124:127]\n"
    "s_sub_u32 s84, s84, s22\n"
    "ds_read_b128 a[216:219], v224 offset:30720\n"
    "ds_read_b128 a[204:207], v224 offset:27648\n"
    "v_mfma_f32_16x16x32_bf16 v[0:3], a[128:131], a[224:227], v[0:3]\n"
    "s_add_u32 s84, s84, 4096\n"
    "s_add_u32 s52, s52, 26624\n"
    "s_addc_u32 s53, s53, 0\n"
    "v_mfma_f32_16x16x32_bf16 v[0:3], a[144:147], a[240:243], v[0:3]\n"
    "buffer_load_dwordx4 v231, s[36:39], s88 offen offset:2048 lds\n"
    "v_mfma_f32_16x16x32_bf16 v[8:11], a[128:131], a[228:231], v[8:11]\n"
    "s_and_b32 s53, s53, 0xffff\n"
    "s_add_u32 s48, s48, 56320\n"
    "s_addc_u32 s49, s49, 0\n"
    "v_mfma_f32_16x16x32_bf16 v[8:11], a[144:147], a[244:247], v[8:11]\n"
    "s_and_b32 s49, s49, 0xffff\n"
    "s_add_u32 s56, s56, 82944\n"
    "s_addc_u32 s57, s57, 0\n"
    "v_mfma_f32_16x16x32_bf16 v[16:19], a[128:131], a[232:235], v[16:19]\n"
    "buffer_load_dwordx4 v231, s[32:35], s88 offen offset:1024 lds\n"
    "v_mfma_f32_16x16x32_bf16 v[16:19], a[144:147], a[248:251], v[16:19]\n"
    "s_and_b32 s57, s57, 0xffff\n"
    "s_add_u32 s60, s60, s22\n"
    "s_addc_u32 s61, s61, 0\n"
    "v_mfma_f32_16x16x32_bf16 v[24:27], a[128:131], a[236:239], v[24:27]\n"
    "s_and_b32 s6, s6, 7\n"
    "s_add_u32 s64, s64, s84\n"
    "s_addc_u32 s65, s65, 0\n"
    "s_waitcnt lgkmcnt(0)\n"
    "v_mfma_f32_16x16x32_bf16 v[24:27], a[144:147], a[252:255], v[24:27]\n"
    "buffer_load_dwordx4 v231, s[40:43], s88 offen offset:3072 lds\n"
    "v_mfma_f32_16x16x32_bf16 v[4:7], a[132:135], a[224:227], v[4:7]\n"
    "s_add_u32 m0, s94, 12288\n"
    "s_add_u32 s88, s88, 262144\n"
    "s_lshl_b32 s84, s96, 19\n"
    "v_mfma_f32_16x16x32_bf16 v[4:7], a[148:151], a[240:243], v[4:7]\n"
    "s_lshl_b32 s22, %1, 8\n"
    "s_add_u32 s60, s60, 2097152\n"
    "s_addc_u32 s61, s61, 0\n"
    "v_mfma_f32_16x16x32_bf16 v[12:15], a[132:135], a[228:231], v[12:15]\n"
    "buffer_load_dwordx4 v231, s[28:31], s88 offen offset:0 lds\n"
    "v_mfma_f32_16x16x32_bf16 v[12:15], a[148:151], a[244:247], v[12:15]\n"
    "s_and_b32 s61, s61, 0xffff\n"
    "s_add_u32 s64, s64, 2095104\n"
    "s_addc_u32 s65, s65, 0\n"
    "v_mfma_f32_16x16x32_bf16 v[20:23], a[132:135], a[232:235], v[20:23]\n"
    "s_and_b32 s65, s65, 0xffff\n"
    "s_waitcnt vmcnt(18)\n"
    "ds_read_b64_tr_b16 a[160:161], v228 offset:16384\n"
    "s_waitcnt vmcnt(17)\n"
    "ds_read_b64_tr_b16 a[162:163], v228 offset:18432\n"
    "v_mfma_f32_16x16x32_bf16 v[20:23], a[148:151], a[248:251], v[20:23]\n"
    "buffer_load_dwordx4 v231, s[36:39], s88 offen offset:2048 lds\n"
    "v_mfma_f32_16x16x32_bf16 v[28:31], a[132:135], a[236:239], v[28:31]\n"
    "s_add_u32 s84, s84, s22\n"
    "s_waitcnt vmcnt(15)\n"
    "ds_read_b64_tr_b16 a[176:177], v228 offset:20480\n"
    "s_waitcnt vmcnt(14)\n"
    "ds_read_b64_tr_b16 a[178:179], v228 offset:22528\n"
    "v_mfma_f32_16x16x32_bf16 v[28:31], a[148:151], a[252:255], v[28:31]\n"
    "s_sub_u32 s85, 4194304, s84\n"
    "ds_read_b64_tr_b16 a[164:165], v229 offset:16384\n"
    "ds_read_b64_tr_b16 a[166:167], v229 offset:18432\n"
    "v_mfma_f32_16x16x32_bf16 v[32:35], a[136:139], a[224:227], v[32:35]\n"
    "buffer_load_dwordx4 v231, s[32:35], s88 offen offset:1024 lds\n"
    "v_mfma_f32_16x16x32_bf16 v[32:35], a[152:155], a[240:243], v[32:35]\n"
    "s_lshr_b32 s22, %1, 1\n"
    "ds_read_b64_tr_b16 a[180:181], v229 offset:20480\n"
    "ds_read_b64_tr_b16 a[182:183], v229 offset:22528\n"
    "v_mfma_f32_16x16x32_bf16 v[40:43], a[136:139], a[228:231], v[40:43]\n"
    "s_lshr_b32 s23, s96, 1\n"
    "ds_read_b64_tr_b16 a[168:169], v228 offset:16896\n"
    "ds_read_b64_tr_b16 a[170:171], v228 offset:18944\n"
    "v_mfma_f32_16x16x32_bf16 v[40:43], a[152:155], a[244:247], v[40:43]\n"
    "buffer_load_dwordx4 v231, s[40:43], s88 offen offset:3072 lds\n"
    "v_mfma_f32_16x16x32_bf16 v[48:51], a[136:139], a[232:235], v[48:51]\n"
    "s_add_u32 s88, s88, 262016\n"
    "ds_read_b64_tr_b16 a[184:185], v228 offset:20992\n"
    "ds_read_b64_tr_b16 a[186:187], v228 offset:23040\n"
    "v_mfma_f32_16x16x32_bf16 v[48:51], a[152:155], a[248:251], v[48:51]\n"
    "s_add_u32 m0, s94, 16384\n"
    "ds_read_b64_tr_b16 a[172:173], v229 offset:16896\n"
    "ds_read_b64_tr_b16 a[174:175], v229 offset:18944\n"
    "v_mfma_f32_16x16x32_bf16 v[56:59], a[136:139], a[236:239], v[56:59]\n"
    "s_and_b32 s88, s88, 4194303\n"
    "ds_read_b64_tr_b16 a[188:189], v229 offset:20992\n"
    "s_waitcnt lgkmcnt(9)\n"
    "ds_read_b64_tr_b16 a[190:191], v229 offset:23040\n"
    "v_mfma_f32_16x16x32_bf16 v[56:59], a[152:155], a[252:255], v[56:59]\n"
    "s_lshl_b32 s23, s23, 10\n"
    "v_mfma_f32_16x16x32_bf16 v[36:39], a[140:143], a[224:227], v[36:39]\n"
    "v_mfma_f32_16x16x32_bf16 v[36:39], a[156:159], a[240:243], v[36:39]\n"
    "v_mfma_f32_16x16x32_bf16 v[44:47], a[140:143], a[228:231], v[44:47]\n"
    "v_mfma_f32_16x16x32_bf16 v[44:47], a[156:159], a[244:247], v[44:47]\n"
    "v_mfma_f32_16x16x32_bf16 v[52:55], a[140:143], a[232:235], v[52:55]\n"
    "v_mfma_f32_16x16x32_bf16 v[52:55], a[156:159], a[248:251], v[52:55]\n"
    "s_waitcnt lgkmcnt(0)\n"
    "buffer_load_dwordx4 v231, s[28:31], s88 offen offset:0 lds\n"
    "v_mfma_f32_16x16x32_bf16 v[60:63], a[140:143], a[236:239], v[60:63]\n"
    "v_mfma_f32_16x16x32_bf16 v[60:63], a[156:159], a[252:255], v[60:63]\n"
    "v_mfma_f32_16x16x32_bf16 v[64:67], a[160:163], a[224:227], v[64:67]\n"
    "buffer_load_dwordx4 v231, s[36:39], s88 offen offset:2048 lds\n"
    "v_mfma_f32_16x16x32_bf16 v[64:67], a[176:179], a[240:243], v[64:67]\n"
    "v_mfma_f32_16x16x32_bf16 v[72:75], a[160:163], a[228:231], v[72:75]\n"
    "v_mfma_f32_16x16x32_bf16 v[72:75], a[176:179], a[244:247], v[72:75]\n"
    "buffer_load_dwordx4 v231, s[32:35], s88 offen offset:1024 lds\n"
    "v_mfma_f32_16x16x32_bf16 v[80:83], a[160:163], a[232:235], v[80:83]\n"
    "v_mfma_f32_16x16x32_bf16 v[80:83], a[176:179], a[248:251], v[80:83]\n"
    "v_mfma_f32_16x16x32_bf16 v[88:91], a[160:163], a[236:239], v[88:91]\n"
    "buffer_load_dwordx4 v231, s[40:43], s88 offen offset:3072 lds\n"
    "v_mfma_f32_16x16x32_bf16 v[88:91], a[176:179], a[252:255], v[88:91]\n"
    "s_add_u32 m0, s94, 20480\n"
    "s_add_u32 s88, s88, 262144\n"
    "v_mfma_f32_16x16x32_bf16 v[68:71], a[164:167], a[224:227], v[68:71]\n"
    "v_mfma_f32_16x16x32_bf16 v[68:71], a[180:183], a[240:243], v[68:71]\n"
    "buffer_load_dwordx4 v231, s[28:31], s88 offen offset:0 lds\n"
    "v_mfma_f32_16x16x32_bf16 v[76:79], a[164:167], a[228:231], v[76:79]\n"
    "v_mfma_f32_16x16x32_bf16 v[76:79], a[180:183], a[244:247], v[76:79]\n"
    "v_mfma_f32_16x16x32_bf16 v[84:87], a[164:167], a[232:235], v[84:87]\n"
    "buffer_load_dwordx4 v231, s[36:39], s88 offen offset:2048 lds\n"
    "v_mfma_f32_16x16x32_bf16 v[84:87], a[180:183], a[248:251], v[84:87]\n"
    "s_waitcnt vmcnt(19)\n"
    "ds_read_b64_tr_b16 a[128:129], v228 offset:0\n"
    "s_waitcnt vmcnt(18)\n"
    "ds_read_b64_tr_b16 a[130:131], v228 offset:2048\n"
    "s_waitcnt vmcnt(15)\n"
    "ds_read_b64_tr_b16 a[144:145], v228 offset:4096\n"
    "v_mfma_f32_16x16x32_bf16 v[92:95], a[164:167], a[236:239], v[92:95]\n"
    "s_waitcnt vmcnt(14)\n"
    "ds_read_b64_tr_b16 a[146:147], v228 offset:6144\n"
    "ds_read_b64_tr_b16 a[132:133], v229 offset:0\n"
    "ds_read_b64_tr_b16 a[134:135], v229 offset:2048\n"
    "v_mfma_f32_16x16x32_bf16 v[92:95], a[180:183], a[252:255], v[92:95]\n"
    "buffer_load_dwordx4 v231, s[32:35], s88 offen offset:1024 lds\n"
    "v_mfma_f32_16x16x32_bf16 v[96:99], a[168:171], a[224:227], v[96:99]\n"
    "ds_read_b64_tr_b16 a[148:149], v229 offset:4096\n"
    "ds_read_b64_tr_b16 a[150:151], v229 offset:6144\n"
    "ds_read_b64_tr_b16 a[136:137], v228 offset:512\n"
    "v_mfma_f32_16x16x32_bf16 v[96:99], a[184:187], a[240:243], v[96:99]\n"
    "ds_read_b64_tr_b16 a[138:139], v228 offset:2560\n"
    "ds_read_b64_tr_b16 a[152:153], v228 offset:4608\n"
    "ds_read_b64_tr_b16 a[154:155], v228 offset:6656\n"
    "v_mfma_f32_16x16x32_bf16 v[104:107], a[168:171], a[228:231], v[104:107]\n"
    "buffer_load_dwordx4 v231, s[40:43], s88 offen offset:3072 lds\n"
    "v_mfma_f32_16x16x32_bf16 v[104:107], a[184:187], a[244:247], v[104:107]\n"
    "s_add_u32 m0, s94, 0\n"
    "ds_read_b64_tr_b16 a[140:141], v229 offset:512\n"
    "ds_read_b64_tr_b16 a[142:143], v229 offset:2560\n"
    "v_mfma_f32_16x16x32_bf16 v[112:115], a[168:171], a[232:235], v[112:115]\n"
    "s_sub_u32 s88, s88, 262016\n"
    "ds_read_b64_tr_b16 a[156:157], v229 offset:4608\n"
    "s_waitcnt lgkmcnt(9)\n"
    "ds_read_b64_tr_b16 a[158:159], v229 offset:6656\n"
    "v_mfma_f32_16x16x32_bf16 v[112:115], a[184:187], a[248:251], v[112:115]\n"
    "v_mfma_f32_16x16x32_bf16 v[120:123], a[168:171], a[236:239], v[120:123]\n"
    "v_mfma_f32_16x16x32_bf16 v[120:123], a[184:187], a[252:255], v[120:123]\n"
    "v_mfma_f32_16x16x32_bf16 v[100:103], a[172:175], a[224:227], v[100:103]\n"
    "ds_read_b128 a[220:223], v224 offset:31744\n"
    "v_mfma_f32_16x16x32_bf16 v[100:103], a[188:191], a[240:243], v[100:103]\n"
    "v_mfma_f32_16x16x32_bf16 v[108:111], a[172:175], a[228:231], v[108:111]\n"
    "v_mfma_f32_16x16x32_bf16 v[108:111], a[188:191], a[244:247], v[108:111]\n"
    "s_waitcnt lgkmcnt(1)\n"
    "buffer_load_dwordx4 v231, s[28:31], s88 offen offset:0 lds\n"
    "v_mfma_f32_16x16x32_bf16 v[116:119], a[172:175], a[232:235], v[116:119]\n"
    "v_mfma_f32_16x16x32_bf16 v[116:119], a[188:191], a[248:251], v[116:119]\n"
    "v_mfma_f32_16x16x32_bf16 v[124:127], a[172:175], a[236:239], v[124:127]\n"
    "buffer_load_dwordx4 v231, s[36:39], s88 offen offset:2048 lds\n"
    "v_mfma_f32_16x16x32_bf16 v[124:127], a[188:191], a[252:255], v[124:127]\n"
    "v_mfma_f32_16x16x32_bf16 v[0:3], a[128:131], a[192:195], v[0:3]\n"
    "v_mfma_f32_16x16x32_bf16 v[0:3], a[144:147], a[208:211], v[0:3]\n"
    "buffer_load_dwordx4 v231, s[32:35], s88 offen offset:1024 lds\n"
    "v_mfma_f32_16x16x32_bf16 v[8:11], a[128:131], a[196:199], v[8:11]\n"
    "v_mfma_f32_16x16x32_bf16 v[8:11], a[144:147], a[212:215], v[8:11]\n"
    "v_mfma_f32_16x16x32_bf16 v[16:19], a[128:131], a[200:203], v[16:19]\n"
    "buffer_load_dwordx4 v231, s[40:43], s88 offen offset:3072 lds\n"
    "v_mfma_f32_16x16x32_bf16 v[16:19], a[144:147], a[216:219], v[16:19]\n"
    "s_add_u32 m0, s94, 4096\n"
    "s_add_u32 s88, s88, 262144\n"
    "v_mfma_f32_16x16x32_bf16 v[24:27], a[128:131], a[204:207], v[24:27]\n"
    "s_waitcnt lgkmcnt(0)\n"
    "v_mfma_f32_16x16x32_bf16 v[24:27], a[144:147], a[220:223], v[24:27]\n"
    "buffer_load_dwordx4 v231, s[28:31], s88 offen offset:0 lds\n"
    "v_mfma_f32_16x16x32_bf16 v[4:7], a[132:135], a[192:195], v[4:7]\n"
    "v_mfma_f32_16x16x32_bf16 v[4:7], a[148:151], a[208:211], v[4:7]\n"
    "v_mfma_f32_16x16x32_bf16 v[12:15], a[132:135], a[196:199], v[12:15]\n"
    "buffer_load_dwordx4 v231, s[36:39], s88 offen offset:2048 lds\n"
    "v_mfma_f32_16x16x32_bf16 v[12:15], a[148:151], a[212:215], v[12:15]\n"
    "v_mfma_f32_16x16x32_bf16 v[20:23], a[132:135], a[200:203], v[20:23]\n"
    "s_waitcnt vmcnt(19)\n"
    "ds_read_b64_tr_b16 a[160:161], v228 offset:8192\n"
    "s_waitcnt vmcnt(18)\n"
    "ds_read_b64_tr_b16 a[162:163], v228 offset:10240\n"
    "v_mfma_f32_16x16x32_bf16 v[20:23], a[148:151], a[216:219], v[20:23]\n"
    "buffer_load_dwordx4 v231, s[32:35], s88 offen offset:1024 lds\n"
    "v_mfma_f32_16x16x32_bf16 v[28:31], a[132:135], a[204:207], v[28:31]\n"
    "s_waitcnt vmcnt(16)\n"
    "ds_read_b64_tr_b16 a[176:177], v228 offset:12288\n"
    "s_waitcnt vmcnt(15)\n"
    "ds_read_b64_tr_b16 a[178:179], v228 offset:14336\n"
    "ds_read_b64_tr_b16 a[164:165], v229 offset:8192\n"
    "v_mfma_f32_16x16x32_bf16 v[28:31], a[148:151], a[220:223], v[28:31]\n"
    "ds_read_b64_tr_b16 a[166:167], v229 offset:10240\n"
    "ds_read_b64_tr_b16 a[180:181], v229 offset:12288\n"
    "ds_read_b64_tr_b16 a[182:183], v229 offset:14336\n"
    "v_mfma_f32_16x16x32_bf16 v[32:35], a[136:139], a[192:195], v[32:35]\n"
    "buffer_load_dwordx4 v231, s[40:43], s88 offen offset:3072 lds\n"
    "v_mfma_f32_16x16x32_bf16 v[32:35], a[152:155], a[208:211], v[32:35]\n"
    "s_add_u32 s88, s88, 262016\n"
    "ds_read_b64_tr_b16 a[168:169], v228 offset:8704\n"
    "ds_read_b64_tr_b16 a[170:171], v228 offset:10752\n"
    "v_mfma_f32_16x16x32_bf16 v[40:43], a[136:139], a[196:199], v[40:43]\n"
    "s_add_u32 m0, s94, 8192\n"
    "ds_read_b64_tr_b16 a[184:185], v228 offset:12800\n"
    "ds_read_b64_tr_b16 a[186:187], v228 offset:14848\n"
    "v_mfma_f32_16x16x32_bf16 v[40:43], a[152:155], a[212:215], v[40:43]\n"
    "s_and_b32 s88, s88, 4194303\n"
    "ds_read_b64_tr_b16 a[172:173], v229 offset:8704\n"
    "ds_read_b64_tr_b16 a[174:175], v229 offset:10752\n"
    "v_mfma_f32_16x16x32_bf16 v[48:51], a[136:139], a[200:203], v[48:51]\n"
    "ds_read_b64_tr_b16 a[188:189], v229 offset:12800\n"
    "s_waitcnt lgkmcnt(10)\n"
    "ds_read_b64_tr_b16 a[190:191], v229 offset:14848\n"
    "v_mfma_f32_16x16x32_bf16 v[48:51], a[152:155], a[216:219], v[48:51]\n"
    "v_mfma_f32_16x16x32_bf16 v[56:59], a[136:139], a[204:207], v[56:59]\n"
    "v_mfma_f32_16x16x32_bf16 v[56:59], a[152:155], a[220:223], v[56:59]\n"
    "v_mfma_f32_16x16x32_bf16 v[36:39], a[140:143], a[192:195], v[36:39]\n"
    "v_mfma_f32_16x16x32_bf16 v[36:39], a[156:159], a[208:211], v[36:39]\n"
    "v_mfma_f32_16x16x32_bf16 v[44:47], a[140:143], a[196:199], v[44:47]\n"
    "v_mfma_f32_16x16x32_bf16 v[44:47], a[156:159], a[212:215], v[44:47]\n"
    "s_waitcnt lgkmcnt(0)\n"
    "buffer_load_dwordx4 v231, s[28:31], s88 offen offset:0 lds\n"
    "v_mfma_f32_16x16x32_bf16 v[52:55], a[140:143], a[200:203], v[52:55]\n"
    "v_mfma_f32_16x16x32_bf16 v[52:55], a[156:159], a[216:219], v[52:55]\n"
    "v_mfma_f32_16x16x32_bf16 v[60:63], a[140:143], a[204:207], v[60:63]\n"
    "buffer_load_dwordx4 v231, s[36:39], s88 offen offset:2048 lds\n"
    "v_mfma_f32_16x16x32_bf16 v[60:63], a[156:159], a[220:223], v[60:63]\n"
    "s_barrier\n"
    "ds_read_b128 a[224:227], v224 offset:32768\n"
    "ds_read_b128 a[240:243], v224 offset:36864\n"
    "v_mfma_f32_16x16x32_bf16 v[64:67], a[160:163], a[192:195], v[64:67]\n"
    "ds_read_b128 a[228:231], v224 offset:33792\n"
    "ds_read_b128 a[244:247], v224 offset:37888\n"
    "v_mfma_f32_16x16x32_bf16 v[64:67], a[176:179], a[208:211], v[64:67]\n"
    "buffer_load_dwordx4 v231, s[32:35], s88 offen offset:1024 lds\n"
    "v_mfma_f32_16x16x32_bf16 v[72:75], a[160:163], a[196:199], v[72:75]\n"
    "ds_read_b128 a[232:235], v224 offset:34816\n"
    "ds_read_b128 a[248:251], v224 offset:38912\n"
    "v_mfma_f32_16x16x32_bf16 v[72:75], a[176:179], a[212:215], v[72:75]\n"
    "ds_read_b128 a[236:239], v224 offset:35840\n"
    "v_mfma_f32_16x16x32_bf16 v[80:83], a[160:163], a[200:203], v[80:83]\n"
    "buffer_load_dwordx4 v231, s[40:43], s88 offen offset:3072 lds\n"
    "v_mfma_f32_16x16x32_bf16 v[80:83], a[176:179], a[216:219], v[80:83]\n"
    "s_add_u32 m0, s94, 12288\n"
    "s_add_u32 s88, s88, 262144\n"
    "v_mfma_f32_16x16x32_bf16 v[88:91], a[160:163], a[204:207], v[88:91]\n"
    "v_mfma_f32_16x16x32_bf16 v[88:91], a[176:179], a[220:223], v[88:91]\n"
    "buffer_load_dwordx4 v231, s[28:31], s88 offen offset:0 lds\n"
    "v_mfma_f32_16x16x32_bf16 v[68:71], a[164:167], a[192:195], v[68:71]\n"
    "v_mfma_f32_16x16x32_bf16 v[68:71], a[180:183], a[208:211], v[68:71]\n"
    "v_mfma_f32_16x16x32_bf16 v[76:79], a[164:167], a[196:199], v[76:79]\n"
    "buffer_load_dwordx4 v231, s[36:39], s88 offen offset:2048 lds\n"
    "v_mfma_f32_16x16x32_bf16 v[76:79], a[180:183], a[212:215], v[76:79]\n"
    "v_mfma_f32_16x16x32_bf16 v[84:87], a[164:167], a[200:203], v[84:87]\n"
    "s_waitcnt vmcnt(19)\n"
    "ds_read_b64_tr_b16 a[128:129], v228 offset:16384\n"
    "s_waitcnt vmcnt(18)\n"
    "ds_read_b64_tr_b16 a[130:131], v228 offset:18432\n"
    "ds_read_b128 a[252:255], v224 offset:39936\n"
    "v_mfma_f32_16x16x32_bf16 v[84:87], a[180:183], a[216:219], v[84:87]\n"
    "buffer_load_dwordx4 v231, s[32:35], s88 offen offset:1024 lds\n"
    "v_mfma_f32_16x16x32_bf16 v[92:95], a[164:167], a[204:207], v[92:95]\n"
    "s_waitcnt vmcnt(16)\n"
    "ds_read_b64_tr_b16 a[144:145], v228 offset:20480\n"
    "s_waitcnt vmcnt(15)\n"
    "ds_read_b64_tr_b16 a[146:147], v228 offset:22528\n"
    "ds_read_b64_tr_b16 a[132:133], v229 offset:16384\n"
    "v_mfma_f32_16x16x32_bf16 v[92:95], a[180:183], a[220:223], v[92:95]\n"
    "ds_read_b64_tr_b16 a[134:135], v229 offset:18432\n"
    "ds_read_b64_tr_b16 a[148:149], v229 offset:20480\n"
    "s_waitcnt lgkmcnt(8)\n"
    "ds_read_b64_tr_b16 a[150:151], v229 offset:22528\n"
    "v_mfma_f32_16x16x32_bf16 v[96:99], a[168:171], a[192:195], v[96:99]\n"
    "buffer_load_dwordx4 v231, s[40:43], s88 offen offset:3072 lds\n"
    "v_mfma_f32_16x16x32_bf16 v[96:99], a[184:187], a[208:211], v[96:99]\n"
    "s_add_u32 m0, s94, 16384\n"
    "ds_read_b64_tr_b16 a[136:137], v228 offset:16896\n"
    "ds_read_b64_tr_b16 a[138:139], v228 offset:18944\n"
    "v_mfma_f32_16x16x32_bf16 v[104:107], a[168:171], a[196:199], v[104:107]\n"
    "s_sub_u32 s88, s88, 262016\n"
    "ds_read_b64_tr_b16 a[152:153], v228 offset:20992\n"
    "ds_read_b64_tr_b16 a[154:155], v228 offset:23040\n"
    "v_mfma_f32_16x16x32_bf16 v[104:107], a[184:187], a[212:215], v[104:107]\n"
    "ds_read_b64_tr_b16 a[140:141], v229 offset:16896\n"
    "ds_read_b64_tr_b16 a[142:143], v229 offset:18944\n"
    "s_waitcnt lgkmcnt(10)\n"
    "ds_read_b64_tr_b16 a[156:157], v229 offset:20992\n"
    "v_mfma_f32_16x16x32_bf16 v[112:115], a[168:171], a[200:203], v[112:115]\n"
    "v_mfma_f32_16x16x32_bf16 v[112:115], a[184:187], a[216:219], v[112:115]\n"
    "ds_read_b64_tr_b16 a[158:159], v229 offset:23040\n"
    "v_mfma_f32_16x16x32_bf16 v[120:123], a[168:171], a[204:207], v[120:123]\n"
    "v_mfma_f32_16x16x32_bf16 v[120:123], a[184:187], a[220:223], v[120:123]\n"
    "v_mfma_f32_16x16x32_bf16 v[100:103], a[172:175], a[192:195], v[100:103]\n"
    "v_mfma_f32_16x16x32_bf16 v[100:103], a[188:191], a[208:211], v[100:103]\n"
    "ds_read_b128 a[192:195], v224 offset:40960\n"
    "ds_read_b128 a[208:211], v224 offset:45056\n"
    "v_mfma_f32_16x16x32_bf16 v[108:111], a[172:175], a[196:199], v[108:111]\n"
    "v_mfma_f32_16x16x32_bf16 v[108:111], a[188:191], a[212:215], v[108:111]\n"
    "s_waitcnt lgkmcnt(2)\n"
    "buffer_load_dwordx4 v231, s[28:31], s88 offen offset:0 lds\n"
    "v_mfma_f32_16x16x32_bf16 v[116:119], a[172:175], a[200:203], v[116:119]\n"
    "ds_read_b128 a[196:199], v224 offset:41984\n"
    "ds_read_b128 a[212:215], v224 offset:46080\n"
    "ds_read_b128 a[200:203], v224 offset:43008\n"
    "v_mfma_f32_16x16x32_bf16 v[116:119], a[188:191], a[216:219], v[116:119]\n"
    "v_mfma_f32_16x16x32_bf16 v[124:127], a[172:175], a[204:207], v[124:127]\n"
    "buffer_load_dwordx4 v231, s[36:39], s88 offen offset:2048 lds\n"
    "v_mfma_f32_16x16x32_bf16 v[124:127], a[188:191], a[220:223], v[124:127]\n"
    "ds_read_b128 a[216:219], v224 offset:47104\n"
    "ds_read_b128 a[204:207], v224 offset:44032\n"
    "ds_read_b128 a[220:223], v224 offset:48128\n"
    "v_mfma_f32_16x16x32_bf16 v[0:3], a[128:131], a[224:227], v[0:3]\n"
    "v_mfma_f32_16x16x32_bf16 v[0:3], a[144:147], a[240:243], v[0:3]\n"
    "buffer_load_dwordx4 v231, s[32:35], s88 offen offset:1024 lds\n"
    "v_mfma_f32_16x16x32_bf16 v[8:11], a[128:131], a[228:231], v[8:11]\n"
    "v_mfma_f32_16x16x32_bf16 v[8:11], a[144:147], a[244:247], v[8:11]\n"
    "v_mfma_f32_16x16x32_bf16 v[16:19], a[128:131], a[232:235], v[16:19]\n"
    "buffer_load_dwordx4 v231, s[40:43], s88 offen offset:3072 lds\n"
    "v_mfma_f32_16x16x32_bf16 v[16:19], a[144:147], a[248:251], v[16:19]\n"
    "s_add_u32 m0, s94, 20480\n"
    "s_add_u32 s88, s88, 262144\n"
    "v_mfma_f32_16x16x32_bf16 v[24:27], a[128:131], a[236:239], v[24:27]\n"
    "v_mfma_f32_16x16x32_bf16 v[24:27], a[144:147], a[252:255], v[24:27]\n"
    "buffer_load_dwordx4 v231, s[28:31], s88 offen offset:0 lds\n"
    "v_mfma_f32_16x16x32_bf16 v[4:7], a[132:135], a[224:227], v[4:7]\n"
    "v_mfma_f32_16x16x32_bf16 v[4:7], a[148:151], a[240:243], v[4:7]\n"
    "v_mfma_f32_16x16x32_bf16 v[12:15], a[132:135], a[228:231], v[12:15]\n"
    "buffer_load_dwordx4 v231, s[36:39], s88 offen offset:2048 lds\n"
    "v_mfma_f32_16x16x32_bf16 v[12:15], a[148:151], a[244:247], v[12:15]\n"
    "v_mfma_f32_16x16x32_bf16 v[20:23], a[132:135], a[232:235], v[20:23]\n"
    "s_waitcnt vmcnt(19)\n"
    "ds_read_b64_tr_b16 a[160:161], v228 offset:0\n"
    "s_waitcnt vmcnt(18)\n"
    "ds_read_b64_tr_b16 a[162:163], v228 offset:2048\n"
    "v_mfma_f32_16x16x32_bf16 v[20:23], a[148:151], a[248:251], v[20:23]\n"
    "buffer_load_dwordx4 v231, s[32:35], s88 offen offset:1024 lds\n"
    "v_mfma_f32_16x16x32_bf16 v[28:31], a[132:135], a[236:239], v[28:31]\n"
    "s_waitcnt vmcnt(16)\n"
    "ds_read_b64_tr_b16 a[176:177], v228 offset:4096\n"
    "s_waitcnt vmcnt(15)\n"
    "ds_read_b64_tr_b16 a[178:179], v228 offset:6144\n"
    "ds_read_b64_tr_b16 a[164:165], v229 offset:0\n"
    "v_mfma_f32_16x16x32_bf16 v[28:31], a[148:151], a[252:255], v[28:31]\n"
    "ds_read_b64_tr_b16 a[166:167], v229 offset:2048\n"
    "ds_read_b64_tr_b16 a[180:181], v229 offset:4096\n"
    "s_waitcnt lgkmcnt(7)\n"
    "ds_read_b64_tr_b16 a[182:183], v229 offset:6144\n"
    "v_mfma_f32_16x16x32_bf16 v[32:35], a[136:139], a[224:227], v[32:35]\n"
    "buffer_load_dwordx4 v231, s[40:43], s88 offen offset:3072 lds\n"
    "v_mfma_f32_16x16x32_bf16 v[32:35], a[152:155], a[240:243], v[32:35]\n"
    "s_add_u32 m0, s95, 0\n"
    "ds_read_b64_tr_b16 a[168:169], v228 offset:512\n"
    "ds_read_b64_tr_b16 a[170:171], v228 offset:2560\n"
    "v_mfma_f32_16x16x32_bf16 v[40:43], a[136:139], a[228:231], v[40:43]\n"
    "s_add_u32 s88, s88, 262016\n"
    "ds_read_b64_tr_b16 a[184:185], v228 offset:4608\n"
    "ds_read_b64_tr_b16 a[186:187], v228 offset:6656\n"
    "v_mfma_f32_16x16x32_bf16 v[40:43], a[152:155], a[244:247], v[40:43]\n"
    "s_and_b32 s88, s88, 4194303\n"
    "ds_read_b64_tr_b16 a[172:173], v229 offset:512\n"
    "ds_read_b64_tr_b16 a[174:175], v229 offset:2560\n"
    "v_mfma_f32_16x16x32_bf16 v[48:51], a[136:139], a[232:235], v[48:51]\n"
    "ds_read_b64_tr_b16 a[188:189], v229 offset:4608\n"
    "s_waitcnt lgkmcnt(10)\n"
    "ds_read_b64_tr_b16 a[190:191], v229 offset:6656\n"
    "v_mfma_f32_16x16x32_bf16 v[48:51], a[152:155], a[248:251], v[48:51]\n"
    "v_mfma_f32_16x16x32_bf16 v[56:59], a[136:139], a[236:239], v[56:59]\n"
    "v_mfma_f32_16x16x32_bf16 v[56:59], a[152:155], a[252:255], v[56:59]\n"
    "v_mfma_f32_16x16x32_bf16 v[36:39], a[140:143], a[224:227], v[36:39]\n"
    "v_mfma_f32_16x16x32_bf16 v[36:39], a[156:159], a[240:243], v[36:39]\n"
    "v_mfma_f32_16x16x32_bf16 v[44:47], a[140:143], a[228:231], v[44:47]\n"
    "v_mfma_f32_16x16x32_bf16 v[44:47], a[156:159], a[244:247], v[44:47]\n"
    "v_mfma_f32_16x16x32_bf16 v[52:55], a[140:143], a[232:235], v[52:55]\n"
    "v_mfma_f32_16x16x32_bf16 v[52:55], a[156:159], a[248:251], v[52:55]\n"
    "v_mfma_f32_16x16x32_bf16 v[60:63], a[140:143], a[236:239], v[60:63]\n"
    "v_mfma_f32_16x16x32_bf16 v[60:63], a[156:159], a[252:255], v[60:63]\n"
    "s_barrier\n"
    "v_mfma_f32_16x16x32_bf16 v[64:67], a[160:163], a[224:227], v[64:67]\n"
    "buffer_load_dwordx4 v234, s[68:71], s89 offen offset:0 lds\n"
    "v_mfma_f32_16x16x32_bf16 v[64:67], a[176:179], a[240:243], v[64:67]\n"
    "v_mfma_f32_16x16x32_bf16 v[72:75], a[160:163], a[228:231], v[72:75]\n"
    "v_mfma_f32_16x16x32_bf16 v[72:75], a[176:179], a[244:247], v[72:75]\n"
    "v_mfma_f32_16x16x32_bf16 v[80:83], a[160:163], a[232:235], v[80:83]\n"
    "v_mfma_f32_16x16x32_bf16 v[80:83], a[176:179], a[248:251], v[80:83]\n"
    "buffer_load_dwordx4 v234, s[72:75], s89 offen offset:1024 lds\n"
    "v_mfma_f32_16x16x32_bf16 v[88:91], a[160:163], a[236:239], v[88:91]\n"
    "s_add_u32 m0, s95, 16384\n"
    "s_xor_b32 s95, s95, 32768\n"
    "v_mfma_f32_16x16x32_bf16 v[88:91], a[176:179], a[252:255], v[88:91]\n"
    "s_waitcnt lgkmcnt(0)\n"
    "v_mfma_f32_16x16x32_bf16 v[68:71], a[164:167], a[224:227], v[68:71]\n"
    "v_mfma_f32_16x16x32_bf16 v[68:71], a[180:183], a[240:243], v[68:71]\n"
    "v_mfma_f32_16x16x32_bf16 v[76:79], a[164:167], a[228:231], v[76:79]\n"
    "buffer_load_dwordx4 v234, s[68:71], s90 offen offset:0 lds\n"
    "v_mfma_f32_16x16x32_bf16 v[76:79], a[180:183], a[244:247], v[76:79]\n"
    "v_mfma_f32_16x16x32_bf16 v[84:87], a[164:167], a[232:235], v[84:87]\n"
    "s_waitcnt vmcnt(16)\n"
    "ds_read_b64_tr_b16 a[128:129], v228 offset:8192\n"
    "s_waitcnt vmcnt(15)\n"
    "ds_read_b64_tr_b16 a[130:131], v228 offset:10240\n"
    "v_mfma_f32_16x16x32_bf16 v[84:87], a[180:183], a[248:251], v[84:87]\n"
    "s_waitcnt vmcnt(12)\n"
    "ds_read_b64_tr_b16 a[144:145], v228 offset:12288\n"
    "s_waitcnt vmcnt(11)\n"
    "ds_read_b64_tr_b16 a[146:147], v228 offset:14336\n"
    "ds_read_b64_tr_b16 a[132:133], v229 offset:8192\n"
    "v_mfma_f32_16x16x32_bf16 v[92:95], a[164:167], a[236:239], v[92:95]\n"
    "ds_read_b64_tr_b16 a[134:135], v229 offset:10240\n"
    "ds_read_b64_tr_b16 a[148:149], v229 offset:12288\n"
    "ds_read_b64_tr_b16 a[150:151], v229 offset:14336\n"
    "v_mfma_f32_16x16x32_bf16 v[92:95], a[180:183], a[252:255], v[92:95]\n"
    "buffer_load_dwordx4 v234, s[72:75], s90 offen offset:1024 lds\n"
    "v_mfma_f32_16x16x32_bf16 v[96:99], a[168:171], a[224:227], v[96:99]\n"
    "ds_read_b64_tr_b16 a[136:137], v228 offset:8704\n"
    "ds_read_b64_tr_b16 a[138:139], v228 offset:10752\n"
    "ds_read_b64_tr_b16 a[152:153], v228 offset:12800\n"
    "v_mfma_f32_16x16x32_bf16 v[96:99], a[184:187], a[240:243], v[96:99]\n"
    "ds_read_b64_tr_b16 a[154:155], v228 offset:14848\n"
    "ds_read_b64_tr_b16 a[140:141], v229 offset:8704\n"
    "v_mfma_f32_16x16x32_bf16 v[104:107], a[168:171], a[228:231], v[104:107]\n"
    "ds_read_b64_tr_b16 a[142:143], v229 offset:10752\n"
    "ds_read_b64_tr_b16 a[156:157], v229 offset:12800\n"
    "v_mfma_f32_16x16x32_bf16 v[104:107], a[184:187], a[244:247], v[104:107]\n"
    "v_mfma_f32_16x16x32_bf16 v[112:115], a[168:171], a[232:235], v[112:115]\n"
    "buffer_load_dwordx4 v235, s[76:79], s89 offen offset:2048 lds\n"
    "v_mfma_f32_16x16x32_bf16 v[112:115], a[184:187], a[248:251], v[112:115]\n"
    "s_waitcnt lgkmcnt(7)\n"
    "ds_read_b64_tr_b16 a[158:159], v229 offset:14848\n"
    "v_mfma_f32_16x16x32_bf16 v[120:123], a[168:171], a[236:239], v[120:123]\n"
    "v_mfma_f32_16x16x32_bf16 v[120:123], a[184:187], a[252:255], v[120:123]\n"
    "v_mfma_f32_16x16x32_bf16 v[100:103], a[172:175], a[224:227], v[100:103]\n"
    "v_mfma_f32_16x16x32_bf16 v[100:103], a[188:191], a[240:243], v[100:103]\n"
    "buffer_load_dwordx4 v235, s[80:83], s89 offen offset:3072 lds\n"
    "v_mfma_f32_16x16x32_bf16 v[108:111], a[172:175], a[228:231], v[108:111]\n"
    "s_add_u32 m0, s94, 0\n"
    "ds_read_b128 a[224:227], v224 offset:49152\n"
    "ds_read_b128 a[240:243], v224 offset:53248\n"
    "v_mfma_f32_16x16x32_bf16 v[108:111], a[188:191], a[244:247], v[108:111]\n"
    "s_add_u32 s89, s90, 8192\n"
    "ds_read_b128 a[228:231], v224 offset:50176\n"
    "ds_read_b128 a[244:247], v224 offset:54272\n"
    "v_mfma_f32_16x16x32_bf16 v[116:119], a[172:175], a[232:235], v[116:119]\n"
    "buffer_load_dwordx4 v231, s[28:31], s88 offen offset:0 lds\n"
    "v_mfma_f32_16x16x32_bf16 v[116:119], a[188:191], a[248:251], v[116:119]\n"
    "s_and_b32 s89, s89, 131071\n"
    "ds_read_b128 a[232:235], v224 offset:51200\n"
    "ds_read_b128 a[248:251], v224 offset:55296\n"
    "v_mfma_f32_16x16x32_bf16 v[124:127], a[172:175], a[236:239], v[124:127]\n"
    "v_mfma_f32_16x16x32_bf16 v[124:127], a[188:191], a[252:255], v[124:127]\n"
    "buffer_load_dwordx4 v231, s[36:39], s88 offen offset:2048 lds\n"
    "v_mfma_f32_16x16x32_bf16 v[0:3], a[128:131], a[192:195], v[0:3]\n"
    "ds_read_b128 a[236:239], v224 offset:52224\n"
    "s_waitcnt lgkmcnt(5)\n"
    "ds_read_b128 a[252:255], v224 offset:56320\n"
    "v_mfma_f32_16x16x32_bf16 v[0:3], a[144:147], a[208:211], v[0:3]\n"
    "v_mfma_f32_16x16x32_bf16 v[8:11], a[128:131], a[196:199], v[8:11]\n"
    "buffer_load_dwordx4 v231, s[32:35], s88 offen offset:1024 lds\n"
    "v_mfma_f32_16x16x32_bf16 v[8:11], a[144:147], a[212:215], v[8:11]\n"
    "v_mfma_f32_16x16x32_bf16 v[16:19], a[128:131], a[200:203], v[16:19]\n"
    "v_mfma_f32_16x16x32_bf16 v[16:19], a[144:147], a[216:219], v[16:19]\n"
    "buffer_load_dwordx4 v231, s[40:43], s88 offen offset:3072 lds\n"
    "v_mfma_f32_16x16x32_bf16 v[24:27], a[128:131], a[204:207], v[24:27]\n"
    "s_add_u32 m0, s94, 4096\n"
    "s_add_u32 s88, s88, 262144\n"
    "v_mfma_f32_16x16x32_bf16 v[24:27], a[144:147], a[220:223], v[24:27]\n"
    "v_mfma_f32_16x16x32_bf16 v[4:7], a[132:135], a[192:195], v[4:7]\n"
    "buffer_load_dwordx4 v231, s[28:31], s88 offen offset:0 lds\n"
    "v_mfma_f32_16x16x32_bf16 v[4:7], a[148:151], a[208:211], v[4:7]\n"
    "v_mfma_f32_16x16x32_bf16 v[12:15], a[132:135], a[196:199], v[12:15]\n"
    "v_mfma_f32_16x16x32_bf16 v[12:15], a[148:151], a[212:215], v[12:15]\n"
    "buffer_load_dwordx4 v231, s[36:39], s88 offen offset:2048 lds\n"
    "v_mfma_f32_16x16x32_bf16 v[20:23], a[132:135], a[200:203], v[20:23]\n"
    "s_waitcnt vmcnt(17)\n"
    "ds_read_b64_tr_b16 a[160:161], v228 offset:16384\n"
    "s_waitcnt vmcnt(16)\n"
    "ds_read_b64_tr_b16 a[162:163], v228 offset:18432\n"
    "v_mfma_f32_16x16x32_bf16 v[20:23], a[148:151], a[216:219], v[20:23]\n"
    "s_waitcnt vmcnt(13)\n"
    "ds_read_b64_tr_b16 a[176:177], v228 offset:20480\n"
    "s_waitcnt vmcnt(12)\n"
    "ds_read_b64_tr_b16 a[178:179], v228 offset:22528\n"
    "ds_read_b64_tr_b16 a[164:165], v229 offset:16384\n"
    "v_mfma_f32_16x16x32_bf16 v[28:31], a[132:135], a[204:207], v[28:31]\n"
    "buffer_load_dwordx4 v231, s[32:35], s88 offen offset:1024 lds\n"
    "v_mfma_f32_16x16x32_bf16 v[28:31], a[148:151], a[220:223], v[28:31]\n"
    "ds_read_b64_tr_b16 a[166:167], v229 offset:18432\n"
    "ds_read_b64_tr_b16 a[180:181], v229 offset:20480\n"
    "ds_read_b64_tr_b16 a[182:183], v229 offset:22528\n"
    "v_mfma_f32_16x16x32_bf16 v[32:35], a[136:139], a[192:195], v[32:35]\n"
    "ds_read_b64_tr_b16 a[168:169], v228 offset:16896\n"
    "s_waitcnt lgkmcnt(9)\n"
    "ds_read_b64_tr_b16 a[170:171], v228 offset:18944\n"
    "ds_read_b64_tr_b16 a[184:185], v228 offset:20992\n"
    "v_mfma_f32_16x16x32_bf16 v[32:35], a[152:155], a[208:211], v[32:35]\n"
    "buffer_load_dwordx4 v231, s[40:43], s88 offen offset:3072 lds\n"
    "v_mfma_f32_16x16x32_bf16 v[40:43], a[136:139], a[196:199], v[40:43]\n"
    "s_add_u32 m0, s94, 8192\n"
    "ds_read_b64_tr_b16 a[186:187], v228 offset:23040\n"
    "ds_read_b64_tr_b16 a[172:173], v229 offset:16896\n"
    "v_mfma_f32_16x16x32_bf16 v[40:43], a[152:155], a[212:215], v[40:43]\n"
    "s_sub_u32 s88, s88, 262016\n"
    "ds_read_b64_tr_b16 a[174:175], v229 offset:18944\n"
    "ds_read_b64_tr_b16 a[188:189], v229 offset:20992\n"
    "v_mfma_f32_16x16x32_bf16 v[48:51], a[136:139], a[200:203], v[48:51]\n"
    "buffer_load_dwordx4 v231, s[28:31], s88 offen offset:0 lds\n"
    "v_mfma_f32_16x16x32_bf16 v[48:51], a[152:155], a[216:219], v[48:51]\n"
    "s_waitcnt lgkmcnt(7)\n"
    "ds_read_b64_tr_b16 a[190:191], v229 offset:23040\n"
    "v_mfma_f32_16x16x32_bf16 v[56:59], a[136:139], a[204:207], v[56:59]\n"
    "v_mfma_f32_16x16x32_bf16 v[56:59], a[152:155], a[220:223], v[56:59]\n"
    "buffer_load_dwordx4 v231, s[36:39], s88 offen offset:2048 lds\n"
    "v_mfma_f32_16x16x32_bf16 v[36:39], a[140:143], a[192:195], v[36:39]\n"
    "v_mfma_f32_16x16x32_bf16 v[36:39], a[156:159], a[208:211], v[36:39]\n"
    "v_mfma_f32_16x16x32_bf16 v[44:47], a[140:143], a[196:199], v[44:47]\n"
    "buffer_load_dwordx4 v231, s[32:35], s88 offen offset:1024 lds\n"
    "v_mfma_f32_16x16x32_bf16 v[44:47], a[156:159], a[212:215], v[44:47]\n"
    "v_mfma_f32_16x16x32_bf16 v[52:55], a[140:143], a[200:203], v[52:55]\n"
    "v_mfma_f32_16x16x32_bf16 v[52:55], a[156:159], a[216:219], v[52:55]\n"
    "buffer_load_dwordx4 v231, s[40:43], s88 offen offset:3072 lds\n"
    "v_mfma_f32_16x16x32_bf16 v[60:63], a[140:143], a[204:207], v[60:63]\n"
    "s_add_u32 m0, s94, 12288\n"
    "s_add_u32 s88, s88, 262144\n"
    "v_mfma_f32_16x16x32_bf16 v[60:63], a[156:159], a[220:223], v[60:63]\n"
    "v_mfma_f32_16x16x32_bf16 v[64:67], a[160:163], a[192:195], v[64:67]\n"
    "buffer_load_dwordx4 v231, s[28:31], s88 offen offset:0 lds\n"
    "v_mfma_f32_16x16x32_bf16 v[64:67], a[176:179], a[208:211], v[64:67]\n"
    "v_mfma_f32_16x16x32_bf16 v[72:75], a[160:163], a[196:199], v[72:75]\n"
    "v_mfma_f32_16x16x32_bf16 v[72:75], a[176:179], a[212:215], v[72:75]\n"
    "buffer_load_dwordx4 v231, s[36:39], s88 offen offset:2048 lds\n"
    "v_mfma_f32_16x16x32_bf16 v[80:83], a[160:163], a[200:203], v[80:83]\n"
    "v_mfma_f32_16x16x32_bf16 v[80:83], a[176:179], a[216:219], v[80:83]\n"
    "v_mfma_f32_16x16x32_bf16 v[88:91], a[160:163], a[204:207], v[88:91]\n"
    "buffer_load_dwordx4 v231, s[32:35], s88 offen offset:1024 lds\n"
    "v_mfma_f32_16x16x32_bf16 v[88:91], a[176:179], a[220:223], v[88:91]\n"
    "v_mfma_f32_16x16x32_bf16 v[68:71], a[164:167], a[192:195], v[68:71]\n"
    "v_mfma_f32_16x16x32_bf16 v[68:71], a[180:183], a[208:211], v[68:71]\n"
    "buffer_load_dwordx4 v231, s[40:43], s88 offen offset:3072 lds\n"
    "v_mfma_f32_16x16x32_bf16 v[76:79], a[164:167], a[196:199], v[76:79]\n"
    "s_add_u32 s88, s88, 262016\n"
    "s_add_u32 m0, s94, 16384\n"
    "s_and_b32 s88, s88, 4194303\n"
    "v_mfma_f32_16x16x32_bf16 v[76:79], a[180:183], a[212:215], v[76:79]\n"
    "v_mfma_f32_16x16x32_bf16 v[84:87], a[164:167], a[200:203], v[84:87]\n"
    "s_waitcnt lgkmcnt(0)\n"
    "buffer_load_dwordx4 v231, s[28:31], s88 offen offset:0 lds\n"
    "v_mfma_f32_16x16x32_bf16 v[84:87], a[180:183], a[216:219], v[84:87]\n"
    "s_waitcnt vmcnt(14)\n"
    "ds_read_b64_tr_b16 a[128:129], v228 offset:0\n"
    "s_waitcnt vmcnt(13)\n"
    "ds_read_b64_tr_b16 a[130:131], v228 offset:2048\n"
    "s_waitcnt vmcnt(10)\n"
    "ds_read_b64_tr_b16 a[144:145], v228 offset:4096\n"
    "v_mfma_f32_16x16x32_bf16 v[92:95], a[164:167], a[204:207], v[92:95]\n"
    "s_waitcnt vmcnt(9)\n"
    "ds_read_b64_tr_b16 a[146:147], v228 offset:6144\n"
    "ds_read_b64_tr_b16 a[132:133], v229 offset:0\n"
    "ds_read_b64_tr_b16 a[134:135], v229 offset:2048\n"
    "v_mfma_f32_16x16x32_bf16 v[92:95], a[180:183], a[220:223], v[92:95]\n"
    "buffer_load_dwordx4 v231, s[36:39], s88 offen offset:2048 lds\n"
    "v_mfma_f32_16x16x32_bf16 v[96:99], a[168:171], a[192:195], v[96:99]\n"
    "ds_read_b64_tr_b16 a[148:149], v229 offset:4096\n"
    "ds_read_b64_tr_b16 a[150:151], v229 offset:6144\n"
    "ds_read_b64_tr_b16 a[136:137], v228 offset:512\n"
    "v_mfma_f32_16x16x32_bf16 v[96:99], a[184:187], a[208:211], v[96:99]\n"
    "ds_read_b64_tr_b16 a[138:139], v228 offset:2560\n"
    "ds_read_b64_tr_b16 a[152:153], v228 offset:4608\n"
    "ds_read_b64_tr_b16 a[154:155], v228 offset:6656\n"
    "v_mfma_f32_16x16x32_bf16 v[104:107], a[168:171], a[196:199], v[104:107]\n"
    "buffer_load_dwordx4 v231, s[32:35], s88 offen offset:1024 lds\n"
    "v_mfma_f32_16x16x32_bf16 v[104:107], a[184:187], a[212:215], v[104:107]\n"
    "ds_read_b64_tr_b16 a[140:141], v229 offset:512\n"
    "ds_read_b64_tr_b16 a[142:143], v229 offset:2560\n"
    "ds_read_b64_tr_b16 a[156:157], v229 offset:4608\n"
    "v_mfma_f32_16x16x32_bf16 v[112:115], a[168:171], a[200:203], v[112:115]\n"
    "v_mfma_f32_16x16x32_bf16 v[112:115], a[184:187], a[216:219], v[112:115]\n"
    "buffer_load_dwordx4 v231, s[40:43], s88 offen offset:3072 lds\n"
    "v_mfma_f32_16x16x32_bf16 v[120:123], a[168:171], a[204:207], v[120:123]\n"
    "s_add_u32 s88, s88, 262144\n"
    "s_waitcnt lgkmcnt(6)\n"
    "ds_read_b64_tr_b16 a[158:159], v229 offset:6656\n"
    "s_add_u32 m0, s94, 20480\n"
    "v_mfma_f32_16x16x32_bf16 v[120:123], a[184:187], a[220:223], v[120:123]\n"
    "v_mfma_f32_16x16x32_bf16 v[100:103], a[172:175], a[192:195], v[100:103]\n"
    "buffer_load_dwordx4 v231, s[28:31], s88 offen offset:0 lds\n"
    "v_mfma_f32_16x16x32_bf16 v[100:103], a[188:191], a[208:211], v[100:103]\n"
    "ds_read_b128 a[192:195], v224 offset:57344\n"
    "ds_read_b128 a[208:211], v224 offset:61440\n"
    "v_mfma_f32_16x16x32_bf16 v[108:111], a[172:175], a[196:199], v[108:111]\n"
    "v_mfma_f32_16x16x32_bf16 v[108:111], a[188:191], a[212:215], v[108:111]\n"
    "buffer_load_dwordx4 v231, s[36:39], s88 offen offset:2048 lds\n"
    "v_mfma_f32_16x16x32_bf16 v[116:119], a[172:175], a[200:203], v[116:119]\n"
    "ds_read_b128 a[196:199], v224 offset:58368\n"
    "ds_read_b128 a[212:215], v224 offset:62464\n"
    "ds_read_b128 a[200:203], v224 offset:59392\n"
    "v_mfma_f32_16x16x32_bf16 v[116:119], a[188:191], a[216:219], v[116:119]\n"
    "v_mfma_f32_16x16x32_bf16 v[124:127], a[172:175], a[204:207], v[124:127]\n"
    "buffer_load_dwordx4 v231, s[32:35], s88 offen offset:1024 lds\n"
    "v_mfma_f32_16x16x32_bf16 v[124:127], a[188:191], a[220:223], v[124:127]\n"
    "ds_read_b128 a[216:219], v224 offset:63488\n"
    "ds_read_b128 a[204:207], v224 offset:60416\n"
    "ds_read_b128 a[220:223], v224 offset:64512\n"
    "v_mfma_f32_16x16x32_bf16 v[0:3], a[128:131], a[224:227], v[0:3]\n"
    "v_mfma_f32_16x16x32_bf16 v[0:3], a[144:147], a[240:243], v[0:3]\n"
    "buffer_load_dwordx4 v231, s[40:43], s88 offen offset:3072 lds\n"
    "v_mfma_f32_16x16x32_bf16 v[8:11], a[128:131], a[228:231], v[8:11]\n"
    "s_add_u32 m0, s94, 0\n"
    "s_sub_u32 s88, s88, 262016\n"
    "v_mfma_f32_16x16x32_bf16 v[8:11], a[144:147], a[244:247], v[8:11]\n"
    "v_mfma_f32_16x16x32_bf16 v[16:19], a[128:131], a[232:235], v[16:19]\n"
    "s_waitcnt lgkmcnt(3)\n"
    "buffer_load_dwordx4 v231, s[28:31], s88 offen offset:0 lds\n"
    "v_mfma_f32_16x16x32_bf16 v[16:19], a[144:147], a[248:251], v[16:19]\n"
    "v_mfma_f32_16x16x32_bf16 v[24:27], a[128:131], a[236:239], v[24:27]\n"
    "v_mfma_f32_16x16x32_bf16 v[24:27], a[144:147], a[252:255], v[24:27]\n"
    "buffer_load_dwordx4 v231, s[36:39], s88 offen offset:2048 lds\n"
    "v_mfma_f32_16x16x32_bf16 v[4:7], a[132:135], a[224:227], v[4:7]\n"
    "v_mfma_f32_16x16x32_bf16 v[4:7], a[148:151], a[240:243], v[4:7]\n"
    "v_mfma_f32_16x16x32_bf16 v[12:15], a[132:135], a[228:231], v[12:15]\n"
    "buffer_load_dwordx4 v231, s[32:35], s88 offen offset:1024 lds\n"
    "v_mfma_f32_16x16x32_bf16 v[12:15], a[148:151], a[244:247], v[12:15]\n"
    "v_mfma_f32_16x16x32_bf16 v[20:23], a[132:135], a[232:235], v[20:23]\n"
    "s_waitcnt vmcnt(16)\n"
    "ds_read_b64_tr_b16 a[160:161], v228 offset:8192\n"
    "s_waitcnt vmcnt(15)\n"
    "ds_read_b64_tr_b16 a[162:163], v228 offset:10240\n"
    "v_mfma_f32_16x16x32_bf16 v[20:23], a[148:151], a[248:251], v[20:23]\n"
    "buffer_load_dwordx4 v231, s[40:43], s88 offen offset:3072 lds\n"
    "v_mfma_f32_16x16x32_bf16 v[28:31], a[132:135], a[236:239], v[28:31]\n"
    "s_add_u32 m0, s94, 4096\n"
    "s_waitcnt vmcnt(13)\n"
    "ds_read_b64_tr_b16 a[176:177], v228 offset:12288\n"
    "s_waitcnt vmcnt(12)\n"
    "ds_read_b64_tr_b16 a[178:179], v228 offset:14336\n"
    "v_mfma_f32_16x16x32_bf16 v[28:31], a[148:151], a[252:255], v[28:31]\n"
    "s_add_u32 s88, s88, 262144\n"
    "ds_read_b64_tr_b16 a[164:165], v229 offset:8192\n"
    "ds_read_b64_tr_b16 a[166:167], v229 offset:10240\n"
    "v_mfma_f32_16x16x32_bf16 v[32:35], a[136:139], a[224:227], v[32:35]\n"
    "buffer_load_dwordx4 v231, s[28:31], s88 offen offset:0 lds\n"
    "v_mfma_f32_16x16x32_bf16 v[32:35], a[152:155], a[240:243], v[32:35]\n"
    "s_mov_b32 s30, s85\n"
    "ds_read_b64_tr_b16 a[180:181], v229 offset:12288\n"
    "ds_read_b64_tr_b16 a[182:183], v229 offset:14336\n"
    "v_mfma_f32_16x16x32_bf16 v[40:43], a[136:139], a[228:231], v[40:43]\n"
    "s_sub_u32 s85, 4187136, s84\n"
    "ds_read_b64_tr_b16 a[168:169], v228 offset:8704\n"
    "ds_read_b64_tr_b16 a[170:171], v228 offset:10752\n"
    "v_mfma_f32_16x16x32_bf16 v[40:43], a[152:155], a[244:247], v[40:43]\n"
    "buffer_load_dwordx4 v231, s[36:39], s88 offen offset:2048 lds\n"
    "v_mfma_f32_16x16x32_bf16 v[48:51], a[136:139], a[232:235], v[48:51]\n"
    "s_mov_b64 s[28:29], s[8:9]\n"
    "ds_read_b64_tr_b16 a[184:185], v228 offset:12800\n"
    "ds_read_b64_tr_b16 a[186:187], v228 offset:14848\n"
    "v_mfma_f32_16x16x32_bf16 v[48:51], a[152:155], a[248:251], v[48:51]\n"
    "s_mov_b64 s[36:37], s[8:9]\n"
    "s_waitcnt lgkmcnt(6)\n"
    "ds_read_b64_tr_b16 a[172:173], v229 offset:8704\n"
    "ds_read_b64_tr_b16 a[174:175], v229 offset:10752\n"
    "v_mfma_f32_16x16x32_bf16 v[56:59], a[136:139], a[236:239], v[56:59]\n"
    "buffer_load_dwordx4 v231, s[32:35], s88 offen offset:1024 lds\n"
    "v_mfma_f32_16x16x32_bf16 v[56:59], a[152:155], a[252:255], v[56:59]\n"
    "s_mov_b32 s34, s85\n"
    "ds_read_b64_tr_b16 a[188:189], v229 offset:12800\n"
    "ds_read_b64_tr_b16 a[190:191], v229 offset:14848\n"
    "v_mfma_f32_16x16x32_bf16 v[36:39], a[140:143], a[224:227], v[36:39]\n"
    "s_sub_u32 s85, 4179968, s84\n"
    "s_mov_b64 s[32:33], s[8:9]\n"
    "s_mov_b32 s38, s85\n"
    "v_mfma_f32_16x16x32_bf16 v[36:39], a[156:159], a[240:243], v[36:39]\n"
    "buffer_load_dwordx4 v231, s[40:43], s88 offen offset:3072 lds\n"
    "v_mfma_f32_16x16x32_bf16 v[44:47], a[140:143], a[228:231], v[44:47]\n"
    "s_add_u32 m0, s94, 8192\n"
    "s_mov_b64 s[40:41], s[8:9]\n"
    "s_sub_u32 s85, 4172800, s84\n"
    "v_mfma_f32_16x16x32_bf16 v[44:47], a[156:159], a[244:247], v[44:47]\n"
    "s_mov_b32 s42, s85\n"
    "s_add_u32 s28, s28, s84\n"
    "s_addc_u32 s29, s29, 0\n"
    "v_mfma_f32_16x16x32_bf16 v[52:55], a[140:143], a[232:235], v[52:55]\n"
    "s_and_b32 s29, s29, 0xffff\n"
    "s_add_u32 s32, s32, s84\n"
    "s_addc_u32 s33, s33, 0\n"
    "v_mfma_f32_16x16x32_bf16 v[52:55], a[156:159], a[248:251], v[52:55]\n"
    "s_mov_b32 s31, 0x20000\n"
    "s_add_u32 s36, s36, s84\n"
    "s_addc_u32 s37, s37, 0\n"
    "v_mfma_f32_16x16x32_bf16 v[60:63], a[140:143], a[236:239], v[60:63]\n"
    "s_waitcnt lgkmcnt(0)\n"
    "buffer_load_dwordx4 v232, s[44:47], s91 offen offset:0 lds\n"
    "v_mfma_f32_16x16x32_bf16 v[60:63], a[156:159], a[252:255], v[60:63]\n"
    "s_lshl_b32 s88, %1, 17\n"
    "s_add_u32 s40, s40, s84\n"
    "s_addc_u32 s41, s41, 0\n"
    "v_mfma_f32_16x16x32_bf16 v[64:67], a[160:163], a[224:227], v[64:67]\n"
    "s_lshl_b32 s84, s92, 1\n"
    "s_add_u32 s84, s84, s22\n"
    "s_lshl_b32 s22, s84, 6\n"
    "v_mfma_f32_16x16x32_bf16 v[64:67], a[176:179], a[240:243], v[64:67]\n"
    "s_mov_b32 s39, 0x20000\n"
    "s_cmp_lt_u32 s97, 28\n"
    "s_cselect_b32 s85, 64, 0\n"
    "v_mfma_f32_16x16x32_bf16 v[72:75], a[160:163], a[228:231], v[72:75]\n"
    "s_mov_b32 s26, s85\n"
    "s_add_u32 s24, s24, s22\n"
    "s_addc_u32 s25, s25, 0\n"
    "v_mfma_f32_16x16x32_bf16 v[72:75], a[176:179], a[244:247], v[72:75]\n"
    "buffer_load_dwordx4 v232, s[52:55], s91 offen offset:2048 lds\n"
    "v_mfma_f32_16x16x32_bf16 v[80:83], a[160:163], a[232:235], v[80:83]\n"
    "s_and_b32 s25, s25, 0xffff\n"
    "s_lshl_b32 s22, s97, 4\n"
    "s_mov_b32 s35, 0x20000\n"
    "v_mfma_f32_16x16x32_bf16 v[80:83], a[176:179], a[248:251], v[80:83]\n"
    "s_mov_b32 s43, 0x20000\n"
    "s_add_u32 s36, s36, 14336\n"
    "s_addc_u32 s37, s37, 0\n"
    "v_mfma_f32_16x16x32_bf16 v[88:91], a[160:163], a[236:239], v[88:91]\n"
    "s_and_b32 s37, s37, 0xffff\n"
    "s_add_u32 s32, s32, 7168\n"
    "s_addc_u32 s33, s33, 0\n"
    "v_mfma_f32_16x16x32_bf16 v[88:91], a[176:179], a[252:255], v[88:91]\n"
    "s_and_b32 s33, s33, 0xffff\n"
    "s_add_u32 s40, s40, 21504\n"
    "s_addc_u32 s41, s41, 0\n"
    "v_mfma_f32_16x16x32_bf16 v[68:71], a[164:167], a[224:227], v[68:71]\n"
    "buffer_load_dwordx4 v232, s[48:51], s91 offen offset:1024 lds\n"
    "v_mfma_f32_16x16x32_bf16 v[68:71], a[180:183], a[240:243], v[68:71]\n"
    "s_and_b32 s41, s41, 0xffff\n"
    "s_lshl_b32 s84, s92, 1\n"
    "v_mfma_f32_16x16x32_bf16 v[76:79], a[164:167], a[228:231], v[76:79]\n"
    "s_cmp_lt_u32 s97, 28\n"
    "s_cselect_b32 s85, -1, 0\n"
    "v_mfma_f32_16x16x32_bf16 v[76:79], a[180:183], a[244:247], v[76:79]\n"
    "s_cmp_eq_u32 %1, 0\n"
    "s_cselect_b32 s8, 1, 0\n"
    "s_mov_b32 s9, 0\n"
    "v_mfma_f32_16x16x32_bf16 v[84:87], a[164:167], a[232:235], v[84:87]\n"
    "s_waitcnt vmcnt(16)\n"
    "ds_read_b64_tr_b16 a[128:129], v228 offset:16384\n"
    "s_waitcnt vmcnt(15)\n"
    "ds_read_b64_tr_b16 a[130:131], v228 offset:18432\n"
    "v_mfma_f32_16x16x32_bf16 v[84:87], a[180:183], a[248:251], v[84:87]\n"
    "s_waitcnt vmcnt(12)\n"
    "ds_read_b64_tr_b16 a[144:145], v228 offset:20480\n"
    "s_waitcnt vmcnt(11)\n"
    "ds_read_b64_tr_b16 a[146:147], v228 offset:22528\n"
    "ds_read_b64_tr_b16 a[132:133], v229 offset:16384\n"
    "v_mfma_f32_16x16x32_bf16 v[92:95], a[164:167], a[236:239], v[92:95]\n"
    "ds_read_b64_tr_b16 a[134:135], v229 offset:18432\n"
    "ds_read_b64_tr_b16 a[148:149], v229 offset:20480\n"
    "ds_read_b64_tr_b16 a[150:151], v229 offset:22528\n"
    "v_mfma_f32_16x16x32_bf16 v[92:95], a[180:183], a[252:255], v[92:95]\n"
    "buffer_load_dwordx4 v232, s[56:59], s91 offen offset:3072 lds\n"
    "v_mfma_f32_16x16x32_bf16 v[96:99], a[168:171], a[224:227], v[96:99]\n"
    "s_add_u32 m0, s94, 12288\n"
    "ds_read_b64_tr_b16 a[136:137], v228 offset:16896\n"
    "ds_read_b64_tr_b16 a[138:139], v228 offset:18944\n"
    "v_mfma_f32_16x16x32_bf16 v[96:99], a[184:187], a[240:243], v[96:99]\n"
    "ds_read_b64_tr_b16 a[152:153], v228 offset:20992\n"
    "ds_read_b64_tr_b16 a[154:155], v228 offset:23040\n"
    "ds_read_b64_tr_b16 a[140:141], v229 offset:16896\n"
    "v_mfma_f32_16x16x32_bf16 v[104:107], a[168:171], a[228:231], v[104:107]\n"
    "ds_read_b64_tr_b16 a[142:143], v229 offset:18944\n"
    "ds_read_b64_tr_b16 a[156:157], v229 offset:20992\n"
    "v_mfma_f32_16x16x32_bf16 v[104:107], a[184:187], a[244:247], v[104:107]\n"
    "v_mfma_f32_16x16x32_bf16 v[112:115], a[168:171], a[232:235], v[112:115]\n"
    "s_waitcnt lgkmcnt(7)\n"
    "ds_read_b64_tr_b16 a[158:159], v229 offset:23040\n"
    "v_mfma_f32_16x16x32_bf16 v[112:115], a[184:187], a[248:251], v[112:115]\n"
    "buffer_load_dwordx4 v224, s[60:63], s91 offen offset:0 lds\n"
    "v_mfma_f32_16x16x32_bf16 v[120:123], a[168:171], a[236:239], v[120:123]\n"
    "v_mfma_f32_16x16x32_bf16 v[120:123], a[184:187], a[252:255], v[120:123]\n"
    "v_mfma_f32_16x16x32_bf16 v[100:103], a[172:175], a[224:227], v[100:103]\n"
    "v_mfma_f32_16x16x32_bf16 v[100:103], a[188:191], a[240:243], v[100:103]\n"
    "v_mfma_f32_16x16x32_bf16 v[108:111], a[172:175], a[228:231], v[108:111]\n"
    "buffer_load_dwordx4 v224, s[64:67], s91 offen offset:2048 lds\n"
    "v_mfma_f32_16x16x32_bf16 v[108:111], a[188:191], a[244:247], v[108:111]\n"
    "v_mfma_f32_16x16x32_bf16 v[116:119], a[172:175], a[232:235], v[116:119]\n"
    "v_mfma_f32_16x16x32_bf16 v[116:119], a[188:191], a[248:251], v[116:119]\n"
    "v_mfma_f32_16x16x32_bf16 v[124:127], a[172:175], a[236:239], v[124:127]\n"
    "v_mfma_f32_16x16x32_bf16 v[124:127], a[188:191], a[252:255], v[124:127]\n"
    "buffer_load_dwordx4 v224, s[60:63], s91 offen offset:1024 lds\n"
    "v_mfma_f32_16x16x32_bf16 v[0:3], a[128:131], a[192:195], v[0:3]\n"
    "v_mfma_f32_16x16x32_bf16 v[0:3], a[144:147], a[208:211], v[0:3]\n"
    "v_mfma_f32_16x16x32_bf16 v[8:11], a[128:131], a[196:199], v[8:11]\n"
    "v_mfma_f32_16x16x32_bf16 v[8:11], a[144:147], a[212:215], v[8:11]\n"
    "v_mfma_f32_16x16x32_bf16 v[16:19], a[128:131], a[200:203], v[16:19]\n"
    "buffer_load_dwordx4 v224, s[64:67], s91 offen offset:3072 lds\n"
    "v_mfma_f32_16x16x32_bf16 v[16:19], a[144:147], a[216:219], v[16:19]\n"
    "s_add_u32 m0, s95, 0\n"
    "s_nop 1\n"
    "v_mul_f32 v0, s99, v0\n"
    "v_mul_f32 v1, s99, v1\n"
    "v_mfma_f32_16x16x32_bf16 v[24:27], a[128:131], a[204:207], v[24:27]\n"
    "s_add_u32 s91, s91, 114688\n"
    "v_cvt_pk_bf16_f32 v0, v0, v1\n"
    "v_mul_f32 v2, s99, v2\n"
    "v_mfma_f32_16x16x32_bf16 v[24:27], a[144:147], a[220:223], v[24:27]\n"
    "v_mul_f32 v8, s99, v8\n"
    "v_mul_f32 v9, s99, v9\n"
    "v_mfma_f32_16x16x32_bf16 v[4:7], a[132:135], a[192:195], v[4:7]\n"
    "v_mul_f32 v3, s99, v3\n"
    "v_cvt_pk_bf16_f32 v8, v8, v9\n"
    "v_mfma_f32_16x16x32_bf16 v[4:7], a[148:151], a[208:211], v[4:7]\n"
    "v_mul_f32 v16, s99, v16\n"
    "v_mul_f32 v17, s99, v17\n"
    "v_mfma_f32_16x16x32_bf16 v[12:15], a[132:135], a[196:199], v[12:15]\n"
    "v_mul_f32 v10, s99, v10\n"
    "v_mul_f32 v11, s99, v11\n"
    "v_mfma_f32_16x16x32_bf16 v[12:15], a[148:151], a[212:215], v[12:15]\n"
    "v_cvt_pk_bf16_f32 v1, v2, v3\n"
    "v_cvt_pk_bf16_f32 v16, v16, v17\n"
    "v_mfma_f32_16x16x32_bf16 v[20:23], a[132:135], a[200:203], v[20:23]\n"
    "s_waitcnt vmcnt(13)\n"
    "ds_read_b64_tr_b16 a[160:161], v228 offset:0\n"
    "s_waitcnt vmcnt(12)\n"
    "ds_read_b64_tr_b16 a[162:163], v228 offset:2048\n"
    "v_mfma_f32_16x16x32_bf16 v[20:23], a[148:151], a[216:219], v[20:23]\n"
    "s_waitcnt vmcnt(9)\n"
    "ds_read_b64_tr_b16 a[176:177], v228 offset:4096\n"
    "s_waitcnt vmcnt(8)\n"
    "ds_read_b64_tr_b16 a[178:179], v228 offset:6144\n"
    "ds_read_b64_tr_b16 a[164:165], v229 offset:0\n"
    "v_mfma_f32_16x16x32_bf16 v[28:31], a[132:135], a[204:207], v[28:31]\n"
    "ds_read_b64_tr_b16 a[166:167], v229 offset:2048\n"
    "ds_read_b64_tr_b16 a[180:181], v229 offset:4096\n"
    "s_waitcnt lgkmcnt(7)\n"
    "ds_read_b64_tr_b16 a[182:183], v229 offset:6144\n"
    "v_mfma_f32_16x16x32_bf16 v[28:31], a[148:151], a[220:223], v[28:31]\n"
    "ds_read_b64_tr_b16 a[168:169], v228 offset:512\n"
    "v_mul_f32 v4, s99, v4\n"
    "v_mfma_f32_16x16x32_bf16 v[32:35], a[136:139], a[192:195], v[32:35]\n"
    "ds_read_b64_tr_b16 a[170:171], v228 offset:2560\n"
    "ds_read_b64_tr_b16 a[184:185], v228 offset:4608\n"
    "v_mfma_f32_16x16x32_bf16 v[32:35], a[152:155], a[208:211], v[32:35]\n"
    "ds_read_b64_tr_b16 a[186:187], v228 offset:6656\n"
    "ds_read_b64_tr_b16 a[172:173], v229 offset:512\n"
    "v_mfma_f32_16x16x32_bf16 v[40:43], a[136:139], a[196:199], v[40:43]\n"
    "ds_read_b64_tr_b16 a[174:175], v229 offset:2560\n"
    "v_mul_f32 v5, s99, v5\n"
    "v_mfma_f32_16x16x32_bf16 v[40:43], a[152:155], a[212:215], v[40:43]\n"
    "ds_read_b64_tr_b16 a[188:189], v229 offset:4608\n"
    "s_waitcnt lgkmcnt(10)\n"
    "ds_read_b64_tr_b16 a[190:191], v229 offset:6656\n"
    "v_mfma_f32_16x16x32_bf16 v[48:51], a[136:139], a[200:203], v[48:51]\n"
    "v_mul_f32 v6, s99, v6\n"
    "v_mul_f32 v7, s99, v7\n"
    "v_mul_f32 v32, s99, v32\n"
    "v_mul_f32 v33, s99, v33\n"
    "v_mul_f32 v34, s99, v34\n"
    "v_mfma_f32_16x16x32_bf16 v[48:51], a[152:155], a[216:219], v[48:51]\n"
    "v_cvt_pk_bf16_f32 v32, v32, v33\n"
    "v_mul_f32 v35, s99, v35\n"
    "v_mfma_f32_16x16x32_bf16 v[56:59], a[136:139], a[204:207], v[56:59]\n"
    "v_cvt_pk_bf16_f32 v33, v34, v35\n"
    "v_mul_f32 v43, s99, v43\n"
    "v_mfma_f32_16x16x32_bf16 v[56:59], a[152:155], a[220:223], v[56:59]\n"
    "v_mul_f32 v40, s99, v40\n"
    "v_mul_f32 v18, s99, v18\n"
    "v_mul_f32 v19, s99, v19\n"
    "v_mfma_f32_16x16x32_bf16 v[36:39], a[140:143], a[192:195], v[36:39]\n"
    "v_mul_f32 v41, s99, v41\n"
    "v_mul_f32 v48, s99, v48\n"
    "v_mul_f32 v49, s99, v49\n"
    "v_cvt_pk_bf16_f32 v48, v48, v49\n"
    "v_mul_f32 v50, s99, v50\n"
    "v_mul_f32 v51, s99, v51\n"
    "v_mfma_f32_16x16x32_bf16 v[36:39], a[156:159], a[208:211], v[36:39]\n"
    "v_cvt_pk_bf16_f32 v9, v10, v11\n"
    "v_cvt_pk_bf16_f32 v40, v40, v41\n"
    "v_mul_f32 v24, s99, v24\n"
    "v_mul_f32 v25, s99, v25\n"
    "v_mfma_f32_16x16x32_bf16 v[44:47], a[140:143], a[196:199], v[44:47]\n"
    "v_mul_f32 v12, s99, v12\n"
    "v_mul_f32 v13, s99, v13\n"
    "v_mul_f32 v14, s99, v14\n"
    "v_mul_f32 v42, s99, v42\n"
    "v_mfma_f32_16x16x32_bf16 v[44:47], a[156:159], a[212:215], v[44:47]\n"
    "v_mul_f32 v15, s99, v15\n"
    "v_mul_f32 v56, s99, v56\n"
    "v_mul_f32 v57, s99, v57\n"
    "v_mfma_f32_16x16x32_bf16 v[52:55], a[140:143], a[200:203], v[52:55]\n"
    "v_cvt_pk_bf16_f32 v2, v4, v5\n"
    "v_cvt_pk_bf16_f32 v3, v6, v7\n"
    "v_mul_f32 v36, s99, v36\n"
    "v_mul_f32 v37, s99, v37\n"
    "v_mul_f32 v38, s99, v38\n"
    "v_mul_f32 v39, s99, v39\n"
    "v_cvt_pk_bf16_f32 v41, v42, v43\n"
    "v_mfma_f32_16x16x32_bf16 v[52:55], a[156:159], a[216:219], v[52:55]\n"
    "v_cvt_pk_bf16_f32 v34, v36, v37\n"
    "v_cvt_pk_bf16_f32 v35, v38, v39\n"
    "v_cvt_pk_bf16_f32 v17, v18, v19\n"
    "v_mfma_f32_16x16x32_bf16 v[60:63], a[140:143], a[204:207], v[60:63]\n"
    "v_mul_f32 v45, s99, v45\n"
    "v_mul_f32 v20, s99, v20\n"
    "v_mul_f32 v21, s99, v21\n"
    "v_mul_f32 v22, s99, v22\n"
    "v_mfma_f32_16x16x32_bf16 v[60:63], a[156:159], a[220:223], v[60:63]\n"
    "v_mul_f32 v23, s99, v23\n"
    "v_cvt_pk_bf16_f32 v49, v50, v51\n"
    "s_barrier\n"
    "buffer_load_dwordx4 v234, s[68:71], s89 offen offset:0 lds\n"
    "v_mfma_f32_16x16x32_bf16 v[64:67], a[160:163], a[192:195], v[64:67]\n"
    "v_mul_f32 v46, s99, v46\n"
    "buffer_load_dwordx4 v234, s[72:75], s89 offen offset:1024 lds\n"
    "v_mul_f32 v53, s99, v53\n"
    "v_cvt_pk_bf16_f32 v24, v24, v25\n"
    "buffer_load_dwordx4 v235, s[76:79], s90 offen offset:2048 lds\n"
    "v_mul_f32 v26, s99, v26\n"
    "v_mfma_f32_16x16x32_bf16 v[64:67], a[176:179], a[208:211], v[64:67]\n"
    "v_cvt_pk_bf16_f32 v10, v12, v13\n"
    "buffer_load_dwordx4 v235, s[80:83], s90 offen offset:3072 lds\n"
    "s_add_u32 m0, s94, 0\n"
    "v_cvt_pk_bf16_f32 v11, v14, v15\n"
    "v_mul_f32 v44, s99, v44\n"
    "v_mul_f32 v47, s99, v47\n"
    "v_mul_f32 v27, s99, v27\n"
    "v_mfma_f32_16x16x32_bf16 v[72:75], a[160:163], a[196:199], v[72:75]\n"
    "s_waitcnt lgkmcnt(0)\n"
    "buffer_load_dwordx4 v232, s[44:47], s91 offen offset:0 lds\n"
    "v_mul_f32 v54, s99, v54\n"
    "v_mul_f32 v55, s99, v55\n"
    "v_cvt_pk_bf16_f32 v56, v56, v57\n"
    "v_mul_f32 v64, s99, v64\n"
    "v_mul_f32 v65, s99, v65\n"
    "v_mfma_f32_16x16x32_bf16 v[72:75], a[176:179], a[212:215], v[72:75]\n"
    "buffer_load_dwordx4 v232, s[52:55], s91 offen offset:2048 lds\n"
    "v_cvt_pk_bf16_f32 v42, v44, v45\n"
    "v_mul_f32 v58, s99, v58\n"
    "v_mul_f32 v59, s99, v59\n"
    "v_cvt_pk_bf16_f32 v64, v64, v65\n"
    "v_mul_f32 v66, s99, v66\n"
    "v_mul_f32 v67, s99, v67\n"
    "v_mfma_f32_16x16x32_bf16 v[80:83], a[160:163], a[200:203], v[80:83]\n"
    "buffer_load_dwordx4 v232, s[48:51], s91 offen offset:1024 lds\n"
    "v_permlane16_swap_b32 v0, v2\n"
    "v_cvt_pk_bf16_f32 v43, v46, v47\n"
    "v_cvt_pk_bf16_f32 v65, v66, v67\n"
    "v_mfma_f32_16x16x32_bf16 v[80:83], a[176:179], a[216:219], v[80:83]\n"
    "buffer_load_dwordx4 v232, s[56:59], s91 offen offset:3072 lds\n"
    "v_permlane16_swap_b32 v1, v3\n"
    "v_permlane16_swap_b32 v32, v34\n"
    "v_permlane16_swap_b32 v33, v35\n"
    "v_mul_f32 v52, s99, v52\n"
    "v_mfma_f32_16x16x32_bf16 v[88:91], a[160:163], a[204:207], v[88:91]\n"
    "s_add_u32 s90, s89, 8192\n"
    "s_add_u32 m0, s94, 4096\n"
    "v_cvt_pk_bf16_f32 v18, v20, v21\n"
    "v_mul_f32 v72, s99, v72\n"
    "v_mul_f32 v73, s99, v73\n"
    "v_mul_f32 v74, s99, v74\n"
    "v_mfma_f32_16x16x32_bf16 v[88:91], a[176:179], a[220:223], v[88:91]\n"
    "s_and_b32 s90, s90, 131071\n"
    "ds_read_b128 a[128:131], v225 offset:0\n"
    "ds_read_b128 a[132:135], v225 offset:512\n"
    "v_cvt_pk_bf16_f32 v19, v22, v23\n"
    "v_cvt_pk_bf16_f32 v50, v52, v53\n"
    "v_cvt_pk_bf16_f32 v72, v72, v73\n"
    "v_mul_f32 v75, s99, v75\n"
    "v_mfma_f32_16x16x32_bf16 v[68:71], a[164:167], a[192:195], v[68:71]\n"
    "v_cvt_pk_bf16_f32 v25, v26, v27\n"
    "v_mul_f32 v28, s99, v28\n"
    "v_cvt_pk_bf16_f32 v73, v74, v75\n"
    "v_mul_f32 v80, s99, v80\n"
    "v_mul_f32 v81, s99, v81\n"
    "v_mfma_f32_16x16x32_bf16 v[68:71], a[180:183], a[208:211], v[68:71]\n"
    "v_mul_f32 v29, s99, v29\n"
    "v_mul_f32 v30, s99, v30\n"
    "v_mul_f32 v31, s99, v31\n"
    "v_cvt_pk_bf16_f32 v80, v80, v81\n"
    "v_mfma_f32_16x16x32_bf16 v[76:79], a[164:167], a[196:199], v[76:79]\n"
    "v_permlane16_swap_b32 v8, v10\n"
    "v_permlane16_swap_b32 v9, v11\n"
    "v_mul_f32 v82, s99, v82\n"
    "v_mul_f32 v88, s99, v88\n"
    "v_mfma_f32_16x16x32_bf16 v[76:79], a[180:183], a[212:215], v[76:79]\n"
    "v_cvt_pk_bf16_f32 v51, v54, v55\n"
    "v_cvt_pk_bf16_f32 v57, v58, v59\n"
    "v_mul_f32 v68, s99, v68\n"
    "v_mul_f32 v69, s99, v69\n"
    "v_mul_f32 v70, s99, v70\n"
    "v_mul_f32 v71, s99, v71\n"
    "v_mul_f32 v83, s99, v83\n"
    "v_mul_f32 v89, s99, v89\n"
    "v_mfma_f32_16x16x32_bf16 v[84:87], a[164:167], a[200:203], v[84:87]\n"
    "v_mul_f32 v60, s99, v60\n"
    "v_mul_f32 v61, s99, v61\n"
    "v_cvt_pk_bf16_f32 v66, v68, v69\n"
    "v_cvt_pk_bf16_f32 v67, v70, v71\n"
    "v_mul_f32 v78, s99, v78\n"
    "v_mul_f32 v79, s99, v79\n"
    "v_cvt_pk_bf16_f32 v88, v88, v89\n"
    "ds_read_b128 a[136:139], v225 offset:1024\n"
    "ds_read_b128 a[140:143], v225 offset:1536\n"
    "ds_read_b128 a[144:147], v225 offset:8192\n"
    "ds_read_b128 a[148:151], v225 offset:8704\n"
    "v_mfma_f32_16x16x32_bf16 v[84:87], a[180:183], a[216:219], v[84:87]\n"
    "v_permlane16_swap_b32 v40, v42\n"
    "v_permlane16_swap_b32 v41, v43\n"
    "v_mul_f32 v62, s99, v62\n"
    "v_cvt_pk_bf16_f32 v75, v78, v79\n"
    "v_mul_f32 v90, s99, v90\n"
    "v_mfma_f32_16x16x32_bf16 v[92:95], a[164:167], a[204:207], v[92:95]\n"
    "v_mul_f32 v63, s99, v63\n"
    "v_permlane16_swap_b32 v64, v66\n"
    "v_mul_f32 v91, s99, v91\n"
    "v_mfma_f32_16x16x32_bf16 v[92:95], a[180:183], a[220:223], v[92:95]\n"
    "v_accvgpr_write_b32 a0, v0\n"
    "v_accvgpr_write_b32 a1, v1\n"
    "v_permlane16_swap_b32 v65, v67\n"
    "v_permlane16_swap_b32 v73, v75\n"
    "v_cvt_pk_bf16_f32 v81, v82, v83\n"
    "v_mfma_f32_16x16x32_bf16 v[96:99], a[168:171], a[192:195], v[96:99]\n"
    "v_accvgpr_write_b32 a2, v2\n"
    "v_accvgpr_write_b32 a3, v3\n"
    "v_accvgpr_write_b32 a4, v32\n"
    "v_mul_f32 v76, s99, v76\n"
    "v_mul_f32 v84, s99, v84\n"
    "v_mfma_f32_16x16x32_bf16 v[96:99], a[184:187], a[208:211], v[96:99]\n"
    "v_accvgpr_write_b32 a5, v33\n"
    "v_accvgpr_write_b32 a6, v34\n"
    "v_accvgpr_write_b32 a7, v35\n"
    "v_mul_f32 v77, s99, v77\n"
    "v_mul_f32 v85, s99, v85\n"
    "v_mfma_f32_16x16x32_bf16 v[104:107], a[168:171], a[196:199], v[104:107]\n"
    "v_accvgpr_write_b32 a8, v64\n"
    "v_accvgpr_write_b32 a9, v65\n"
    "v_accvgpr_write_b32 a10, v66\n"
    "v_cvt_pk_bf16_f32 v74, v76, v77\n"
    "ds_read_b128 a[152:155], v225 offset:9216\n"
    "ds_read_b128 a[156:159], v225 offset:9728\n"
    "v_mfma_f32_16x16x32_bf16 v[104:107], a[184:187], a[212:215], v[104:107]\n"
    "v_accvgpr_write_b32 a11, v67\n"
    "v_mul_f32 v96, s99, v96\n"
    "v_mul_f32 v97, s99, v97\n"
    "v_mul_f32 v86, s99, v86\n"
    "v_cvt_pk_bf16_f32 v89, v90, v91\n"
    "v_mul_f32 v92, s99, v92\n"
    "v_mfma_f32_16x16x32_bf16 v[112:115], a[168:171], a[200:203], v[112:115]\n"
    "v_cvt_pk_bf16_f32 v96, v96, v97\n"
    "v_mul_f32 v98, s99, v98\n"
    "v_mul_f32 v99, s99, v99\n"
    "v_mul_f32 v87, s99, v87\n"
    "v_mul_f32 v93, s99, v93\n"
    "v_mul_f32 v94, s99, v94\n"
    "v_mul_f32 v95, s99, v95\n"
    "v_mfma_f32_16x16x32_bf16 v[112:115], a[184:187], a[216:219], v[112:115]\n"
    "buffer_load_dwordx4 v224, s[60:63], s91 offen offset:0 lds\n"
    "v_cvt_pk_bf16_f32 v97, v98, v99\n"
    "v_mul_f32 v104, s99, v104\n"
    "v_mul_f32 v105, s99, v105\n"
    "v_mfma_f32_16x16x32_bf16 v[120:123], a[168:171], a[204:207], v[120:123]\n"
    "buffer_load_dwordx4 v224, s[64:67], s91 offen offset:2048 lds\n"
    "v_cvt_pk_bf16_f32 v104, v104, v105\n"
    "v_mul_f32 v106, s99, v106\n"
    "v_mul_f32 v107, s99, v107\n"
    "v_cvt_pk_bf16_f32 v82, v84, v85\n"
    "v_mfma_f32_16x16x32_bf16 v[120:123], a[184:187], a[220:223], v[120:123]\n"
    "v_permlane16_swap_b32 v16, v18\n"
    "v_permlane16_swap_b32 v17, v19\n"
    "v_cvt_pk_bf16_f32 v105, v106, v107\n"
    "v_mul_f32 v112, s99, v112\n"
    "v_mul_f32 v113, s99, v113\n"
    "v_mfma_f32_16x16x32_bf16 v[100:103], a[172:175], a[192:195], v[100:103]\n"
    "v_permlane16_swap_b32 v48, v50\n"
    "v_cvt_pk_bf16_f32 v26, v28, v29\n"
    "v_cvt_pk_bf16_f32 v27, v30, v31\n"
    "v_permlane16_swap_b32 v72, v74\n"
    "v_cvt_pk_bf16_f32 v112, v112, v113\n"
    "v_mfma_f32_16x16x32_bf16 v[100:103], a[188:191], a[208:211], v[100:103]\n"
    "v_accvgpr_write_b32 a16, v8\n"
    "v_accvgpr_write_b32 a17, v9\n"
    "v_accvgpr_write_b32 a18, v10\n"
    "v_cvt_pk_bf16_f32 v83, v86, v87\n"
    "v_mul_f32 v114, s99, v114\n"
    "v_mfma_f32_16x16x32_bf16 v[108:111], a[172:175], a[196:199], v[108:111]\n"
    "v_accvgpr_write_b32 a19, v11\n"
    "v_permlane16_swap_b32 v49, v51\n"
    "v_cvt_pk_bf16_f32 v58, v60, v61\n"
    "v_mul_f32 v115, s99, v115\n"
    "v_mul_f32 v122, s99, v122\n"
    "v_mfma_f32_16x16x32_bf16 v[108:111], a[188:191], a[212:215], v[108:111]\n"
    "v_mul_f32 v102, s99, v102\n"
    "v_mul_f32 v103, s99, v103\n"
    "v_mul_f32 v100, s99, v100\n"
    "v_mul_f32 v101, s99, v101\n"
    "v_cvt_pk_bf16_f32 v99, v102, v103\n"
    "v_accvgpr_write_b32 a20, v40\n"
    "v_accvgpr_write_b32 a21, v41\n"
    "v_cvt_pk_bf16_f32 v59, v62, v63\n"
    "v_cvt_pk_bf16_f32 v98, v100, v101\n"
    "v_mul_f32 v120, s99, v120\n"
    "v_mul_f32 v123, s99, v123\n"
    "v_mfma_f32_16x16x32_bf16 v[116:119], a[172:175], a[200:203], v[116:119]\n"
    "v_accvgpr_write_b32 a22, v42\n"
    "v_mul_f32 v108, s99, v108\n"
    "v_mul_f32 v109, s99, v109\n"
    "v_mul_f32 v110, s99, v110\n"
    "v_mul_f32 v111, s99, v111\n"
    "v_accvgpr_write_b32 a23, v43\n"
    "v_permlane16_swap_b32 v97, v99\n"
    "v_cvt_pk_bf16_f32 v106, v108, v109\n"
    "v_cvt_pk_bf16_f32 v107, v110, v111\n"
    "v_permlane16_swap_b32 v80, v82\n"
    "v_cvt_pk_bf16_f32 v113, v114, v115\n"
    "v_mul_f32 v121, s99, v121\n"
    "s_waitcnt vmcnt(15)\n"
    "ds_read_b64_tr_b16 a[200:201], v228 offset:8704\n"
    "s_waitcnt vmcnt(14)\n"
    "ds_read_b64_tr_b16 a[202:203], v228 offset:10752\n"
    "v_mfma_f32_16x16x32_bf16 v[116:119], a[188:191], a[216:219], v[116:119]\n"
    "v_permlane16_swap_b32 v96, v98\n"
    "v_accvgpr_write_b32 a13, v97\n"
    "v_accvgpr_write_b32 a24, v72\n"
    "v_accvgpr_write_b32 a25, v73\n"
    "v_accvgpr_write_b32 a26, v74\n"
    "v_cvt_pk_bf16_f32 v120, v120, v121\n"
    "v_mfma_f32_16x16x32_bf16 v[124:127], a[172:175], a[204:207], v[124:127]\n"
    "v_accvgpr_write_b32 a14, v98\n"
    "v_accvgpr_write_b32 a27, v75\n"
    "v_permlane16_swap_b32 v104, v106\n"
    "v_permlane16_swap_b32 v105, v107\n"
    "v_permlane16_swap_b32 v81, v83\n"
    "v_cvt_pk_bf16_f32 v121, v122, v123\n"
    "ds_read_b64_tr_b16 a[204:205], v229 offset:8704\n"
    "ds_read_b64_tr_b16 a[206:207], v229 offset:10752\n"
    "v_mfma_f32_16x16x32_bf16 v[124:127], a[188:191], a[220:223], v[124:127]\n"
    "v_accvgpr_write_b32 a12, v96\n"
    "v_accvgpr_write_b32 a15, v99\n"
    "v_accvgpr_write_b32 a29, v105\n"
    "v_accvgpr_write_b32 a30, v106\n"
    "v_accvgpr_write_b32 a31, v107\n"
    "v_mul_f32 v116, s99, v116\n"
    "v_mul_f32 v117, s99, v117\n"
    "s_waitcnt lgkmcnt(4)\n"
    "v_mfma_f32_16x16x32_bf16 v[128:131], a[128:131], a[0:3], 0\n"
    "v_cvt_pk_bf16_f32 v114, v116, v117\n"
    "v_mul_f32 v118, s99, v118\n"
    "v_mul_f32 v119, s99, v119\n"
    "v_cvt_pk_bf16_f32 v90, v92, v93\n"
    "v_cvt_pk_bf16_f32 v91, v94, v95\n"
    "v_mfma_f32_16x16x32_bf16 v[128:131], a[132:135], a[4:7], v[128:131]\n"
    "buffer_load_dwordx4 v224, s[60:63], s91 offen offset:1024 lds\n"
    "v_cvt_pk_bf16_f32 v115, v118, v119\n"
    "v_mfma_f32_16x16x32_bf16 v[128:131], a[136:139], a[8:11], v[128:131]\n"
    "v_mul_f32 v125, s99, v125\n"
    "buffer_load_dwordx4 v224, s[64:67], s91 offen offset:3072 lds\n"
    "v_mfma_f32_16x16x32_bf16 v[128:131], a[140:143], a[12:15], v[128:131]\n"
    "s_mul_i32 s91, s6, 229376\n"
    "v_accvgpr_write_b32 a32, v16\n"
    "v_accvgpr_write_b32 a28, v104\n"
    "v_mfma_f32_16x16x32_bf16 v[132:135], a[144:147], a[0:3], 0\n"
    "s_add_u32 m0, s94, 8192\n"
    "v_accvgpr_write_b32 a33, v17\n"
    "v_accvgpr_write_b32 a34, v18\n"
    "v_accvgpr_write_b32 a35, v19\n"
    "v_permlane16_swap_b32 v112, v114\n"
    "v_mfma_f32_16x16x32_bf16 v[132:135], a[148:151], a[4:7], v[132:135]\n"
    "s_add_u32 s6, s6, 1\n"
    "v_accvgpr_write_b32 a36, v48\n"
    "v_permlane16_swap_b32 v24, v26\n"
    "v_permlane16_swap_b32 v25, v27\n"
    "v_permlane16_swap_b32 v113, v115\n"
    "v_mfma_f32_16x16x32_bf16 v[132:135], a[152:155], a[8:11], v[132:135]\n"
    "s_and_b32 s6, s6, 7\n"
    "v_accvgpr_write_b32 a37, v49\n"
    "v_accvgpr_write_b32 a38, v50\n"
    "v_accvgpr_write_b32 a39, v51\n"
    "v_mul_f32 v126, s99, v126\n"
    "v_mfma_f32_16x16x32_bf16 v[132:135], a[156:159], a[12:15], v[132:135]\n"
    "v_permlane16_swap_b32 v56, v58\n"
    "v_permlane16_swap_b32 v57, v59\n"
    "v_accvgpr_write_b32 a40, v80\n"
    "v_mul_f32 v124, s99, v124\n"
    "v_mul_f32 v127, s99, v127\n"
    "v_mfma_f32_16x16x32_bf16 v[136:139], a[128:131], a[16:19], 0\n"
    "v_accvgpr_write_b32 a41, v81\n"
    "v_accvgpr_write_b32 a42, v82\n"
    "v_accvgpr_write_b32 a43, v83\n"
    "v_cvt_pk_bf16_f32 v122, v124, v125\n"
    "v_mfma_f32_16x16x32_bf16 v[136:139], a[132:135], a[20:23], v[136:139]\n"
    "v_accvgpr_write_b32 a44, v112\n"
    "v_accvgpr_write_b32 a45, v113\n"
    "v_accvgpr_write_b32 a46, v114\n"
    "v_cvt_pk_bf16_f32 v123, v126, v127\n"
    "v_mfma_f32_16x16x32_bf16 v[136:139], a[136:139], a[24:27], v[136:139]\n"
    "v_accvgpr_write_b32 a47, v115\n"
    "v_permlane16_swap_b32 v88, v90\n"
    "v_permlane16_swap_b32 v89, v91\n"
    "v_max3_f32 v240, v128, v129, v130\n"
    "v_mfma_f32_16x16x32_bf16 v[136:139], a[140:143], a[28:31], v[136:139]\n"
    "v_permlane16_swap_b32 v120, v122\n"
    "v_max3_f32 v241, v131, v132, v133\n"
    "v_max3_f32 v240, v240, v134, v135\n"
    "v_mfma_f32_16x16x32_bf16 v[140:143], a[144:147], a[16:19], 0\n"
    "v_permlane16_swap_b32 v121, v123\n"
    "v_accvgpr_write_b32 a48, v24\n"
    "v_mfma_f32_16x16x32_bf16 v[140:143], a[148:151], a[20:23], v[140:143]\n"
    "ds_read_b64_tr_b16 a[192:193], v228 offset:8192\n"
    "ds_read_b64_tr_b16 a[194:195], v228 offset:10240\n"
    "s_waitcnt vmcnt(13)\n"
    "ds_read_b128 a[208:211], v230 offset:12288\n"
    "v_mfma_f32_16x16x32_bf16 v[140:143], a[152:155], a[24:27], v[140:143]\n"
    "v_accvgpr_write_b32 a49, v25\n"
    "v_accvgpr_write_b32 a50, v26\n"
    "v_mfma_f32_16x16x32_bf16 v[140:143], a[156:159], a[28:31], v[140:143]\n"
    "v_accvgpr_write_b32 a51, v27\n"
    "v_accvgpr_write_b32 a52, v56\n"
    "v_mfma_f32_16x16x32_bf16 v[144:147], a[128:131], a[32:35], 0\n"
    "v_accvgpr_write_b32 a53, v57\n"
    "v_accvgpr_write_b32 a54, v58\n"
    "v_accvgpr_write_b32 a55, v59\n"
    "v_mfma_f32_16x16x32_bf16 v[144:147], a[132:135], a[36:39], v[144:147]\n"
    "v_accvgpr_write_b32 a56, v88\n"
    "ds_read_b128 a[212:215], v230 offset:13312\n"
    "s_waitcnt vmcnt(12)\n"
    "ds_read_b128 a[216:219], v230 offset:14336\n"
    "v_mfma_f32_16x16x32_bf16 v[144:147], a[136:139], a[40:43], v[144:147]\n"
    "v_accvgpr_write_b32 a57, v89\n"
    "v_accvgpr_write_b32 a58, v90\n"
    "v_accvgpr_write_b32 a59, v91\n"
    "v_mfma_f32_16x16x32_bf16 v[144:147], a[140:143], a[44:47], v[144:147]\n"
    "v_max_f32 v240, v240, v241\n"
    "v_max3_f32 v242, v136, v137, v138\n"
    "v_mfma_f32_16x16x32_bf16 v[148:151], a[144:147], a[32:35], 0\n"
    "v_accvgpr_write_b32 a60, v120\n"
    "v_accvgpr_write_b32 a61, v121\n"
    "v_accvgpr_write_b32 a62, v122\n"
    "v_mfma_f32_16x16x32_bf16 v[148:151], a[148:151], a[36:39], v[148:151]\n"
    "ds_read_b128 a[220:223], v230 offset:15360\n"
    "ds_read_b64_tr_b16 a[196:197], v229 offset:8192\n"
    "v_mfma_f32_16x16x32_bf16 v[148:151], a[152:155], a[40:43], v[148:151]\n"
    "v_accvgpr_write_b32 a63, v123\n"
    "ds_read_b64_tr_b16 a[198:199], v229 offset:10240\n"
    "v_mfma_f32_16x16x32_bf16 v[148:151], a[156:159], a[44:47], v[148:151]\n"
    "v_mov_b32 v241, v240\n"
    "v_max3_f32 v243, v139, v140, v141\n"
    "v_mfma_f32_16x16x32_bf16 v[152:155], a[128:131], a[48:51], 0\n"
    "v_max3_f32 v242, v242, v142, v143\n"
    "v_permlane32_swap_b32 v240, v241\n"
    "v_mfma_f32_16x16x32_bf16 v[152:155], a[132:135], a[52:55], v[152:155]\n"
    "v_max_f32 v242, v242, v243\n"
    "v_max_f32 v240, v240, v241\n"
    "v_mfma_f32_16x16x32_bf16 v[152:155], a[136:139], a[56:59], v[152:155]\n"
    "v_mov_b32 v243, v242\n"
    "v_mov_b32 v241, v240\n"
    "v_mfma_f32_16x16x32_bf16 v[152:155], a[140:143], a[60:63], v[152:155]\n"
    "v_max3_f32 v244, v144, v145, v146\n"
    "v_max3_f32 v245, v147, v148, v149\n"
    "v_mfma_f32_16x16x32_bf16 v[156:159], a[144:147], a[48:51], 0\n"
    "v_permlane32_swap_b32 v242, v243\n"
    "s_waitcnt lgkmcnt(0)\n"
    "buffer_load_dwordx4 v232, s[44:47], s91 offen offset:0 lds\n"
    "v_mfma_f32_16x16x32_bf16 v[156:159], a[148:151], a[52:55], v[156:159]\n"
    "v_max3_f32 v244, v244, v150, v151\n"
    "buffer_load_dwordx4 v232, s[52:55], s91 offen offset:2048 lds\n"
    "v_mfma_f32_16x16x32_bf16 v[156:159], a[152:155], a[56:59], v[156:159]\n"
    "v_max3_f32 v246, v152, v153, v154\n"
    "buffer_load_dwordx4 v232, s[48:51], s91 offen offset:1024 lds\n"
    "v_mfma_f32_16x16x32_bf16 v[156:159], a[156:159], a[60:63], v[156:159]\n"
    "v_max_f32 v244, v244, v245\n"
    "buffer_load_dwordx4 v232, s[56:59], s91 offen offset:3072 lds\n"
    "s_add_u32 m0, s94, 12288\n"
    "v_mfma_f32_16x16x32_bf16 a[64:67], a[192:195], a[208:211], 0\n"
    "ds_read_b128 a[128:131], v225 offset:16384\n"
    "ds_read_b128 a[132:135], v225 offset:16896\n"
    "ds_read_b128 a[136:139], v225 offset:17408\n"
    "buffer_load_dwordx4 v224, s[60:63], s91 offen offset:0 lds\n"
    "v_mfma_f32_16x16x32_bf16 a[72:75], a[192:195], a[212:215], 0\n"
    "v_max3_f32 v247, v155, v156, v157\n"
    "buffer_load_dwordx4 v224, s[64:67], s91 offen offset:2048 lds\n"
    "v_mfma_f32_16x16x32_bf16 a[80:83], a[192:195], a[216:219], 0\n"
    "v_permlane16_swap_b32 v240, v241\n"
    "buffer_load_dwordx4 v224, s[60:63], s91 offen offset:1024 lds\n"
    "v_mfma_f32_16x16x32_bf16 a[88:91], a[192:195], a[220:223], 0\n"
    "v_max3_f32 v246, v246, v158, v159\n"
    "buffer_load_dwordx4 v224, s[64:67], s91 offen offset:3072 lds\n"
    "v_mfma_f32_16x16x32_bf16 a[68:71], a[196:199], a[208:211], 0\n"
    "s_add_u32 m0, s95, 16384\n"
    "v_max_f32 v242, v242, v243\n"
    "v_max_f32 v246, v246, v247\n"
    "v_mfma_f32_16x16x32_bf16 a[76:79], a[196:199], a[212:215], 0\n"
    "s_add_u32 s91, s91, 114688\n"
    "v_mov_b32 v245, v244\n"
    "v_max_f32 v240, v240, v241\n"
    "v_mfma_f32_16x16x32_bf16 a[84:87], a[196:199], a[216:219], 0\n"
    "s_xor_b32 s95, s95, 32768\n"
    "ds_read_b128 a[140:143], v225 offset:17920\n"
    "ds_read_b128 a[144:147], v225 offset:24576\n"
    "v_mfma_f32_16x16x32_bf16 a[92:95], a[196:199], a[220:223], 0\n"
    "ds_read_b128 a[148:151], v225 offset:25088\n"
    "ds_read_b128 a[152:155], v225 offset:25600\n"
    "v_mfma_f32_16x16x32_bf16 a[96:99], a[200:203], a[208:211], 0\n"
    "v_mov_b32 v243, v242\n"
    "v_mov_b32 v247, v246\n"
    "v_mfma_f32_16x16x32_bf16 a[104:107], a[200:203], a[212:215], 0\n"
    "buffer_load_dwordx4 v234, s[68:71], s90 offen offset:0 lds\n"
    "v_mfma_f32_16x16x32_bf16 a[112:115], a[200:203], a[216:219], 0\n"
    "v_sub_f32 v192, 0, v240\n"
    "v_permlane32_swap_b32 v244, v245\n"
    "v_mfma_f32_16x16x32_bf16 a[120:123], a[200:203], a[220:223], 0\n"
    "v_sub_f32 v193, 0, v240\n"
    "v_sub_f32 v194, 0, v240\n"
    "v_sub_f32 v195, 0, v240\n"
    "v_mfma_f32_16x16x32_bf16 a[100:103], a[204:207], a[208:211], 0\n"
    "v_permlane16_swap_b32 v242, v243\n"
    "v_max_f32 v244, v244, v245\n"
    "v_permlane32_swap_b32 v246, v247\n"
    "v_mfma_f32_16x16x32_bf16 a[108:111], a[204:207], a[212:215], 0\n"
    "buffer_load_dwordx4 v234, s[72:75], s90 offen offset:1024 lds\n"
    "ds_read_b128 a[156:159], v225 offset:26112\n"
    "v_mfma_f32_16x16x32_bf16 a[116:119], a[204:207], a[216:219], 0\n"
    "v_max_f32 v242, v242, v243\n"
    "v_mov_b32 v245, v244\n"
    "v_max_f32 v246, v246, v247\n"
    "v_mfma_f32_16x16x32_bf16 a[124:127], a[204:207], a[220:223], 0\n"
    "v_mov_b32 v247, v246\n"
    "s_waitcnt vmcnt(15)\n"
    "ds_read_b64_tr_b16 a[224:225], v228 offset:0\n"
    "s_waitcnt vmcnt(14)\n"
    "ds_read_b64_tr_b16 a[226:227], v228 offset:2048\n"
    "s_waitcnt lgkmcnt(3)\n"
    "v_mfma_f32_16x16x32_bf16 v[160:163], a[128:131], a[0:3], v[192:195]\n"
    "v_sub_f32 v196, 0, v242\n"
    "v_sub_f32 v197, 0, v242\n"
    "v_mfma_f32_16x16x32_bf16 v[160:163], a[132:135], a[4:7], v[160:163]\n"
    "buffer_load_dwordx4 v235, s[76:79], s89 offen offset:2048 lds\n"
    "s_waitcnt vmcnt(12)\n"
    "ds_read_b128 a[240:243], v230 offset:4096\n"
    "v_mfma_f32_16x16x32_bf16 v[160:163], a[136:139], a[8:11], v[160:163]\n"
    "v_sub_f32 v198, 0, v242\n"
    "v_sub_f32 v199, 0, v242\n"
    "v_mfma_f32_16x16x32_bf16 v[160:163], a[140:143], a[12:15], v[160:163]\n"
    "ds_read_b128 a[244:247], v230 offset:5120\n"
    "ds_read_b64_tr_b16 a[232:233], v228 offset:512\n"
    "ds_read_b64_tr_b16 a[234:235], v228 offset:2560\n"
    "s_waitcnt vmcnt(11)\n"
    "ds_read_b128 a[248:251], v230 offset:6144\n"
    "v_mfma_f32_16x16x32_bf16 v[164:167], a[144:147], a[0:3], v[192:195]\n"
    "v_permlane16_swap_b32 v244, v245\n"
    "ds_read_b64_tr_b16 a[228:229], v229 offset:0\n"
    "ds_read_b128 a[252:255], v230 offset:7168\n"
    "v_mfma_f32_16x16x32_bf16 v[164:167], a[148:151], a[4:7], v[164:167]\n"
    "v_sub_f32 v128, v128, v240\n"
    "v_permlane16_swap_b32 v246, v247\n"
    "v_mfma_f32_16x16x32_bf16 v[164:167], a[152:155], a[8:11], v[164:167]\n"
    "buffer_load_dwordx4 v235, s[80:83], s89 offen offset:3072 lds\n"
    "ds_read_b64_tr_b16 a[230:231], v229 offset:2048\n"
    "s_waitcnt lgkmcnt(8)\n"
    "v_mfma_f32_16x16x32_bf16 v[164:167], a[156:159], a[12:15], v[164:167]\n"
    "s_add_u32 m0, s94, 0\n"
    "v_sub_f32 v129, v129, v240\n"
    "ds_read_b64_tr_b16 a[236:237], v229 offset:512\n"
    "ds_read_b64_tr_b16 a[238:239], v229 offset:2560\n"
    "v_mfma_f32_16x16x32_bf16 v[168:171], a[128:131], a[16:19], v[196:199]\n"
    "s_add_u32 s89, s90, 8192\n"
    "v_sub_f32 v136, v136, v242\n"
    "v_sub_f32 v137, v137, v242\n"
    "v_max_f32 v244, v244, v245\n"
    "v_mfma_f32_16x16x32_bf16 v[168:171], a[132:135], a[20:23], v[168:171]\n"
    "s_and_b32 s89, s89, 131071\n"
    "v_sub_f32 v130, v130, v240\n"
    "v_sub_f32 v131, v131, v240\n"
    "v_sub_f32 v132, v132, v240\n"
    "v_mfma_f32_16x16x32_bf16 v[168:171], a[136:139], a[24:27], v[168:171]\n"
    "v_sub_f32 v133, v133, v240\n"
    "v_sub_f32 v134, v134, v240\n"
    "v_sub_f32 v135, v135, v240\n"
    "v_mfma_f32_16x16x32_bf16 v[168:171], a[140:143], a[28:31], v[168:171]\n"
    "v_max_f32 v246, v246, v247\n"
    "v_exp_f32 v128, v128\n"
    "v_mfma_f32_16x16x32_bf16 v[172:175], a[144:147], a[16:19], v[196:199]\n"
    "v_sub_f32 v138, v138, v242\n"
    "v_exp_f32 v129, v129\n"
    "v_mfma_f32_16x16x32_bf16 v[172:175], a[148:151], a[20:23], v[172:175]\n"
    "v_sub_f32 v139, v139, v242\n"
    "v_exp_f32 v136, v136\n"
    "v_mfma_f32_16x16x32_bf16 v[172:175], a[152:155], a[24:27], v[172:175]\n"
    "v_sub_f32 v200, 0, v244\n"
    "v_exp_f32 v137, v137\n"
    "v_mfma_f32_16x16x32_bf16 v[172:175], a[156:159], a[28:31], v[172:175]\n"
    "s_waitcnt lgkmcnt(0)\n"
    "v_sub_f32 v201, 0, v244\n"
    "v_sub_f32 v202, 0, v244\n"
    "v_sub_f32 v203, 0, v244\n"
    "v_mfma_f32_16x16x32_bf16 a[64:67], a[224:227], a[240:243], a[64:67]\n"
    "v_sub_f32 v204, 0, v246\n"
    "v_max3_f32 v240, v160, v161, v162\n"
    "v_mfma_f32_16x16x32_bf16 a[72:75], a[224:227], a[244:247], a[72:75]\n"
    "v_sub_f32 v205, 0, v246\n"
    "v_exp_f32 v130, v130\n"
    "v_mfma_f32_16x16x32_bf16 a[80:83], a[224:227], a[248:251], a[80:83]\n"
    "v_sub_f32 v206, 0, v246\n"
    "v_exp_f32 v131, v131\n"
    "v_mfma_f32_16x16x32_bf16 a[88:91], a[224:227], a[252:255], a[88:91]\n"
    "v_sub_f32 v207, 0, v246\n"
    "v_exp_f32 v138, v138\n"
    "v_mfma_f32_16x16x32_bf16 a[68:71], a[228:231], a[240:243], a[68:71]\n"
    "v_sub_f32 v140, v140, v242\n"
    "v_exp_f32 v139, v139\n"
    "v_mfma_f32_16x16x32_bf16 a[76:79], a[228:231], a[244:247], a[76:79]\n"
    "v_cvt_pk_bf16_f32 v128, v128, v129\n"
    "ds_read_b64_tr_b16 a[160:161], v226 offset:18432\n"
    "ds_read_b64_tr_b16 a[162:163], v226 offset:26624\n"
    "v_mfma_f32_16x16x32_bf16 a[84:87], a[228:231], a[248:251], a[84:87]\n"
    "v_sub_f32 v141, v141, v242\n"
    "v_sub_f32 v142, v142, v242\n"
    "v_cvt_pk_bf16_f32 v136, v136, v137\n"
    "v_mfma_f32_16x16x32_bf16 a[92:95], a[228:231], a[252:255], a[92:95]\n"
    "v_exp_f32 v132, v132\n"
    "v_max3_f32 v240, v240, v163, v164\n"
    "v_mfma_f32_16x16x32_bf16 v[176:179], a[128:131], a[32:35], v[200:203]\n"
    "v_exp_f32 v133, v133\n"
    "v_mfma_f32_16x16x32_bf16 v[176:179], a[132:135], a[36:39], v[176:179]\n"
    "v_exp_f32 v134, v134\n"
    "v_mfma_f32_16x16x32_bf16 v[176:179], a[136:139], a[40:43], v[176:179]\n"
    "v_sub_f32 v143, v143, v242\n"
    "v_exp_f32 v135, v135\n"
    "v_mfma_f32_16x16x32_bf16 v[176:179], a[140:143], a[44:47], v[176:179]\n"
    "ds_read_b64_tr_b16 a[164:165], v227 offset:18432\n"
    "ds_read_b64_tr_b16 a[166:167], v227 offset:26624\n"
    "ds_read_b64_tr_b16 a[168:169], v226 offset:18944\n"
    "ds_read_b64_tr_b16 a[170:171], v226 offset:27136\n"
    "ds_read_b64_tr_b16 a[172:173], v227 offset:18944\n"
    "v_mfma_f32_16x16x32_bf16 v[180:183], a[144:147], a[32:35], v[200:203]\n"
    "ds_read_b64_tr_b16 a[174:175], v227 offset:27136\n"
    "ds_read_b64_tr_b16 a[176:177], v226 offset:19456\n"
    "ds_read_b64_tr_b16 a[178:179], v226 offset:27648\n"
    "v_mfma_f32_16x16x32_bf16 v[180:183], a[148:151], a[36:39], v[180:183]\n"
    "ds_read_b64_tr_b16 a[180:181], v227 offset:19456\n"
    "ds_read_b64_tr_b16 a[182:183], v227 offset:27648\n"
    "ds_read_b64_tr_b16 a[184:185], v226 offset:19968\n"
    "v_mfma_f32_16x16x32_bf16 v[180:183], a[152:155], a[40:43], v[180:183]\n"
    "ds_read_b64_tr_b16 a[186:187], v226 offset:28160\n"
    "ds_read_b64_tr_b16 a[188:189], v227 offset:19968\n"
    "s_waitcnt lgkmcnt(10)\n"
    "ds_read_b64_tr_b16 a[190:191], v227 offset:28160\n"
    "v_mfma_f32_16x16x32_bf16 v[180:183], a[156:159], a[44:47], v[180:183]\n"
    "v_cvt_pk_bf16_f32 v129, v130, v131\n"
    "v_exp_f32 v140, v140\n"
    "v_mfma_f32_16x16x32_bf16 v[184:187], a[128:131], a[48:51], v[204:207]\n"
    "v_cvt_pk_bf16_f32 v137, v138, v139\n"
    "v_exp_f32 v141, v141\n"
    "v_mfma_f32_16x16x32_bf16 v[184:187], a[132:135], a[52:55], v[184:187]\n"
    "v_exp_f32 v142, v142\n"
    "v_max3_f32 v240, v240, v165, v166\n"
    "v_max3_f32 v241, v176, v177, v178\n"
    "v_mfma_f32_16x16x32_bf16 v[184:187], a[136:139], a[56:59], v[184:187]\n"
    "v_exp_f32 v143, v143\n"
    "v_max3_f32 v241, v241, v179, v180\n"
    "v_mfma_f32_16x16x32_bf16 v[184:187], a[140:143], a[60:63], v[184:187]\n"
    "buffer_load_dwordx4 v232, s[44:47], s91 offen offset:0 lds\n"
    "v_mfma_f32_16x16x32_bf16 v[188:191], a[144:147], a[48:51], v[204:207]\n"
    "v_cvt_pk_bf16_f32 v130, v132, v133\n"
    "v_cvt_pk_bf16_f32 v131, v134, v135\n"
    "v_mfma_f32_16x16x32_bf16 v[188:191], a[148:151], a[52:55], v[188:191]\n"
    "v_cvt_pk_bf16_f32 v138, v140, v141\n"
    "v_max3_f32 v240, v240, v167, v168\n"
    "v_max3_f32 v241, v241, v181, v182\n"
    "v_mfma_f32_16x16x32_bf16 v[188:191], a[152:155], a[56:59], v[188:191]\n"
    "v_cvt_pk_bf16_f32 v139, v142, v143\n"
    "v_max3_f32 v240, v240, v169, v170\n"
    "v_max3_f32 v241, v241, v183, v184\n"
    "v_mfma_f32_16x16x32_bf16 v[188:191], a[156:159], a[60:63], v[188:191]\n"
    "v_max3_f32 v240, v240, v171, v172\n"
    "v_max3_f32 v241, v241, v185, v186\n"
    "v_mfma_f32_16x16x32_bf16 v[208:211], v[248:251], v[128:131], 0\n"
    "v_max3_f32 v240, v240, v173, v174\n"
    "v_sub_f32 v144, v144, v244\n"
    "v_mfma_f32_16x16x32_bf16 v[212:215], v[248:251], v[136:139], 0\n"
    "buffer_load_dwordx4 v232, s[52:55], s91 offen offset:2048 lds\n"
    "v_mfma_f32_16x16x32_bf16 v[0:3], a[160:163], v[128:131], 0\n"
    "v_sub_f32 v145, v145, v244\n"
    "v_sub_f32 v146, v146, v244\n"
    "v_max3_f32 v241, v241, v187, v188\n"
    "v_max3_f32 v241, v241, v189, v190\n"
    "v_max3_f32 v240, v240, v175, v191\n"
    "v_max_f32 v240, v240, v241\n"
    "v_mfma_f32_16x16x32_bf16 v[8:11], a[160:163], v[136:139], 0\n"
    "v_cmp_lt_f32 vcc, 0x41a00000, v240\n"
    "v_sub_f32 v147, v147, v244\n"
    "s_waitcnt vmcnt(2) lgkmcnt(0)\n"
    "s_barrier\n"
    "v_mfma_f32_16x16x32_bf16 v[4:7], a[164:167], v[128:131], 0\n"
    "v_exp_f32 v144, v144\n"
    "v_mfma_f32_16x16x32_bf16 v[12:15], a[164:167], v[136:139], 0\n"
    "buffer_load_dwordx4 v232, s[48:51], s91 offen offset:1024 lds\n"
    "s_cbranch_vccnz K2_S1_1\n"
    "K2_R1_1:\n"
    "v_mfma_f32_16x16x32_bf16 v[32:35], a[168:171], v[128:131], 0\n"
    "v_sub_f32 v152, v152, v246\n"
    "v_sub_f32 v153, v153, v246\n"
    "v_mfma_f32_16x16x32_bf16 v[40:43], a[168:171], v[136:139], 0\n"
    "v_exp_f32 v145, v145\n"
    "v_mfma_f32_16x16x32_bf16 v[36:39], a[172:175], v[128:131], 0\n"
    "v_exp_f32 v146, v146\n"
    "v_mfma_f32_16x16x32_bf16 v[44:47], a[172:175], v[136:139], 0\n"
    "v_sub_f32 v148, v148, v244\n"
    "v_sub_f32 v149, v149, v244\n"
    "v_mfma_f32_16x16x32_bf16 v[64:67], a[176:179], v[128:131], 0\n"
    "v_exp_f32 v147, v147\n"
    "v_mfma_f32_16x16x32_bf16 v[72:75], a[176:179], v[136:139], 0\n"
    "v_exp_f32 v152, v152\n"
    "v_mfma_f32_16x16x32_bf16 v[68:71], a[180:183], v[128:131], 0\n"
    "v_sub_f32 v150, v150, v244\n"
    "v_exp_f32 v153, v153\n"
    "v_mfma_f32_16x16x32_bf16 v[76:79], a[180:183], v[136:139], 0\n"
    "v_sub_f32 v151, v151, v244\n"
    "v_exp_f32 v148, v148\n"
    "v_mfma_f32_16x16x32_bf16 v[96:99], a[184:187], v[128:131], 0\n"
    "v_sub_f32 v154, v154, v246\n"
    "v_exp_f32 v149, v149\n"
    "v_mfma_f32_16x16x32_bf16 v[104:107], a[184:187], v[136:139], 0\n"
    "v_sub_f32 v155, v155, v246\n"
    "v_exp_f32 v150, v150\n"
    "v_mfma_f32_16x16x32_bf16 v[100:103], a[188:191], v[128:131], 0\n"
    "v_sub_f32 v156, v156, v246\n"
    "v_exp_f32 v151, v151\n"
    "v_mfma_f32_16x16x32_bf16 v[108:111], a[188:191], v[136:139], 0\n"
    "v_sub_f32 v157, v157, v246\n"
    "v_exp_f32 v154, v154\n"
    "v_mfma_f32_16x16x32_bf16 a[96:99], a[232:235], a[240:243], a[96:99]\n"
    "v_sub_f32 v158, v158, v246\n"
    "v_exp_f32 v155, v155\n"
    "v_mfma_f32_16x16x32_bf16 a[104:107], a[232:235], a[244:247], a[104:107]\n"
    "v_cvt_pk_bf16_f32 v144, v144, v145\n"
    "v_exp_f32 v156, v156\n"
    "v_mfma_f32_16x16x32_bf16 a[112:115], a[232:235], a[248:251], a[112:115]\n"
    "v_sub_f32 v159, v159, v246\n"
    "v_exp_f32 v157, v157\n"
    "v_mfma_f32_16x16x32_bf16 a[120:123], a[232:235], a[252:255], a[120:123]\n"
    "v_cvt_pk_bf16_f32 v145, v146, v147\n"
    "v_exp_f32 v158, v158\n"
    "v_mfma_f32_16x16x32_bf16 a[100:103], a[236:239], a[240:243], a[100:103]\n"
    "v_cvt_pk_bf16_f32 v152, v152, v153\n"
    "v_exp_f32 v159, v159\n"
    "v_mfma_f32_16x16x32_bf16 a[108:111], a[236:239], a[244:247], a[108:111]\n"
    "v_cvt_pk_bf16_f32 v146, v148, v149\n"
    "v_cvt_pk_bf16_f32 v147, v150, v151\n"
    "v_cvt_pk_bf16_f32 v153, v154, v155\n"
    "v_mfma_f32_16x16x32_bf16 a[116:119], a[236:239], a[248:251], a[116:119]\n"
    "v_cvt_pk_bf16_f32 v154, v156, v157\n"
    "v_cvt_pk_bf16_f32 v155, v158, v159\n"
    "v_mfma_f32_16x16x32_bf16 a[124:127], a[236:239], a[252:255], a[124:127]\n"
    "buffer_load_dwordx4 v232, s[56:59], s91 offen offset:3072 lds\n"
    "v_mfma_f32_16x16x32_bf16 v[216:219], v[248:251], v[144:147], 0\n"
    "s_add_u32 m0, s94, 4096\n"
    "v_xor_b32 v225, 0x8000, v225\n"
    "v_xor_b32 v226, 0x8000, v226\n"
    "v_mfma_f32_16x16x32_bf16 v[220:223], v[248:251], v[152:155], 0\n"
    "v_exp_f32 v160, v160\n"
    "v_mfma_f32_16x16x32_bf16 v[16:19], a[160:163], v[144:147], 0\n"
    "v_exp_f32 v161, v161\n"
    "v_mfma_f32_16x16x32_bf16 v[24:27], a[160:163], v[152:155], 0\n"
    "v_exp_f32 v168, v168\n"
    "v_mfma_f32_16x16x32_bf16 v[20:23], a[164:167], v[144:147], 0\n"
    "buffer_load_dwordx4 v224, s[60:63], s91 offen offset:0 lds\n"
    "v_mfma_f32_16x16x32_bf16 v[28:31], a[164:167], v[152:155], 0\n"
    "ds_read_b128 a[128:131], v225 offset:0\n"
    "ds_read_b128 a[132:135], v225 offset:512\n"
    "ds_read_b128 a[136:139], v225 offset:1024\n"
    "v_mfma_f32_16x16x32_bf16 v[48:51], a[168:171], v[144:147], 0\n"
    "ds_read_b128 a[140:143], v225 offset:1536\n"
    "v_cvt_pk_bf16_f32 v160, v160, v161\n"
    "v_mfma_f32_16x16x32_bf16 v[56:59], a[168:171], v[152:155], 0\n"
    "v_exp_f32 v169, v169\n"
    "v_mfma_f32_16x16x32_bf16 v[52:55], a[172:175], v[144:147], 0\n"
    "v_exp_f32 v162, v162\n"
    "v_mfma_f32_16x16x32_bf16 v[60:63], a[172:175], v[152:155], 0\n"
    "buffer_load_dwordx4 v224, s[64:67], s91 offen offset:2048 lds\n"
    "v_mfma_f32_16x16x32_bf16 v[80:83], a[176:179], v[144:147], 0\n"
    "ds_read_b128 a[144:147], v225 offset:8192\n"
    "ds_read_b128 a[148:151], v225 offset:8704\n"
    "ds_read_b128 a[152:155], v225 offset:9216\n"
    "v_mfma_f32_16x16x32_bf16 v[88:91], a[176:179], v[152:155], 0\n"
    "ds_read_b128 a[156:159], v225 offset:9728\n"
    "v_xor_b32 v227, 0x8000, v227\n"
    "v_mfma_f32_16x16x32_bf16 v[84:87], a[180:183], v[144:147], 0\n"
    "v_exp_f32 v163, v163\n"
    "v_mfma_f32_16x16x32_bf16 v[92:95], a[180:183], v[152:155], 0\n"
    "v_exp_f32 v170, v170\n"
    "v_mfma_f32_16x16x32_bf16 v[112:115], a[184:187], v[144:147], 0\n"
    "buffer_load_dwordx4 v224, s[60:63], s91 offen offset:1024 lds\n"
    "v_mfma_f32_16x16x32_bf16 v[120:123], a[184:187], v[152:155], 0\n"
    "ds_read_b64_tr_b16 a[192:193], v228 offset:8192\n"
    "ds_read_b64_tr_b16 a[194:195], v228 offset:10240\n"
    "ds_read_b128 a[208:211], v230 offset:12288\n"
    "v_mfma_f32_16x16x32_bf16 v[116:119], a[188:191], v[144:147], 0\n"
    "ds_read_b128 a[212:215], v230 offset:13312\n"
    "ds_read_b64_tr_b16 a[200:201], v228 offset:8704\n"
    "ds_read_b64_tr_b16 a[202:203], v228 offset:10752\n"
    "v_mfma_f32_16x16x32_bf16 v[124:127], a[188:191], v[152:155], 0\n"
    "buffer_load_dwordx2 v[244:245], v254, s[24:27], 0 offen offset:0\n"
    "buffer_load_dwordx2 v[246:247], v254, s[24:27], 0 offen offset:32\n"
    "v_lshrrev_b32 v254, 4, v224\n"
    "v_lshrrev_b32 v242, 1, v254\n"
    "v_add_u32 v242, s22, v242\n"
    "v_lshlrev_b32 v242, 10, v242\n"
    "v_and_b32 v243, 1, v254\n"
    "v_lshl_add_u32 v242, v243, 7, v242\n"
    "v_lshrrev_b32 v243, 3, v254\n"
    "v_lshl_add_u32 v252, v243, 26, v242\n"
    "s_lshl_b32 s22, s97, 2\n"
    "s_add_u32 s22, s22, %1\n"
    "s_mul_i32 s22, s22, 14\n"
    "v_lshrrev_b32 v242, 3, v254\n"
    "v_mul_u32_u24 v243, 86, v242\n"
    "v_lshrrev_b32 v243, 8, v243\n"
    "v_add_u32 v242, s22, v242\n"
    "v_lshlrev_b32 v242, 12, v242\n"
    "v_lshl_add_u32 v242, v243, 26, v242\n"
    "v_and_b32 v243, 7, v254\n"
    "v_lshl_add_u32 v253, v243, 7, v242\n"
    "s_mov_b64 s[24:25], s[12:13]\n"
    "s_add_u32 s24, s24, s23\n"
    "s_addc_u32 s25, s25, 0\n"
    "s_and_b32 s25, s25, 0xffff\n"
    "s_mov_b32 s26, 7336960\n"
    "s_mov_b32 s27, 0x20000\n"
    "buffer_load_dwordx4 v224, s[64:67], s91 offen offset:3072 lds\n"
    "s_add_u32 m0, s95, 0\n"
    "s_nop 0\n"
    "buffer_load_dwordx4 v234, s[68:71], s89 offen offset:0 lds\n"
    "buffer_load_dwordx4 v234, s[72:75], s89 offen offset:1024 lds\n"
    "buffer_load_dwordx4 v235, s[76:79], s90 offen offset:2048 lds\n"
    "buffer_load_dwordx4 v235, s[80:83], s90 offen offset:3072 lds\n"
    "v_exp_f32 v184, v184\n"
    "v_exp_f32 v185, v185\n"
    "s_nop 0\n"
    "v_cvt_pk_bf16_f32 v184, v184, v185\n"
    "v_exp_f32 v186, v186\n"
    "v_exp_f32 v187, v187\n"
    "s_nop 0\n"
    "v_cvt_pk_bf16_f32 v185, v186, v187\n"
    "v_exp_f32 v190, v190\n"
    "v_exp_f32 v191, v191\n"
    "s_nop 0\n"
    "v_cvt_pk_bf16_f32 v187, v190, v191\n"
    "s_add_u32 m0, s95, 16384\n"
    "s_add_u32 s90, s89, 8192\n"
    "s_and_b32 s90, s90, 131071\n"
    "buffer_load_dwordx4 v234, s[68:71], s90 offen offset:0 lds\n"
    "buffer_load_dwordx4 v234, s[72:75], s90 offen offset:1024 lds\n"
    "buffer_load_dwordx4 v235, s[76:79], s89 offen offset:2048 lds\n"
    "buffer_load_dwordx4 v235, s[80:83], s89 offen offset:3072 lds\n"
    "s_mov_b32 s99, 5\n"
    "K2_LOOP:\n"
    "s_cbranch_vccnz K2_S2_1\n"
    "K2_R2_1:\n"
    "s_waitcnt lgkmcnt(6)\n"
    "v_mfma_f32_16x16x32_bf16 v[128:131], a[128:131], a[0:3], v[192:195]\n"
    "s_add_u32 m0, s94, 8192\n"
    "s_waitcnt vmcnt(17)\n"
    "ds_read_b128 a[216:219], v230 offset:14336\n"
    "ds_read_b128 a[220:223], v230 offset:15360\n"
    "v_mfma_f32_16x16x32_bf16 v[128:131], a[132:135], a[4:7], v[128:131]\n"
    "s_mul_i32 s91, s6, 229376\n"
    "ds_read_b64_tr_b16 a[196:197], v229 offset:8192\n"
    "ds_read_b64_tr_b16 a[198:199], v229 offset:10240\n"
    "v_mfma_f32_16x16x32_bf16 v[128:131], a[136:139], a[8:11], v[128:131]\n"
    "s_xor_b32 s95, s95, 32768\n"
    "ds_read_b64_tr_b16 a[204:205], v229 offset:8704\n"
    "ds_read_b64_tr_b16 a[206:207], v229 offset:10752\n"
    "v_mfma_f32_16x16x32_bf16 v[128:131], a[140:143], a[12:15], v[128:131]\n"
    "s_add_u32 s89, s90, 8192\n"
    "v_exp_f32 v171, v171\n"
    "v_mfma_f32_16x16x32_bf16 v[132:135], a[144:147], a[0:3], v[192:195]\n"
    "s_nop 0\n"
    "v_exp_f32 v164, v164\n"
    "v_mfma_f32_16x16x32_bf16 v[132:135], a[148:151], a[4:7], v[132:135]\n"
    "buffer_load_dword v242, v252, s[28:31], s86 offen offset:0\n"
    "v_mfma_f32_16x16x32_bf16 v[132:135], a[152:155], a[8:11], v[132:135]\n"
    "s_and_b32 s89, s89, 131071\n"
    "v_exp_f32 v165, v165\n"
    "v_mfma_f32_16x16x32_bf16 v[132:135], a[156:159], a[12:15], v[132:135]\n"
    "s_add_u32 s6, s6, 1\n"
    "v_max3_f32 v240, v128, v129, v130\n"
    "v_cvt_pk_bf16_f32 v168, v168, v169\n"
    "v_mfma_f32_16x16x32_bf16 v[136:139], a[128:131], a[16:19], v[196:199]\n"
    "s_and_b32 s6, s6, 7\n"
    "ds_read_b64_tr_b16 a[160:161], v226 offset:2048\n"
    "ds_read_b64_tr_b16 a[162:163], v226 offset:10240\n"
    "v_mfma_f32_16x16x32_bf16 v[136:139], a[132:135], a[20:23], v[136:139]\n"
    "s_add_u32 s86, s86, 4096\n"
    "v_exp_f32 v166, v166\n"
    "v_mfma_f32_16x16x32_bf16 v[136:139], a[136:139], a[24:27], v[136:139]\n"
    "s_waitcnt lgkmcnt(2)\n"
    "buffer_load_dwordx4 v232, s[44:47], s91 offen offset:0 lds\n"
    "v_mfma_f32_16x16x32_bf16 v[136:139], a[140:143], a[28:31], v[136:139]\n"
    "v_max3_f32 v240, v240, v131, v132\n"
    "v_max3_f32 v240, v240, v133, v134\n"
    "v_mfma_f32_16x16x32_bf16 v[140:143], a[144:147], a[16:19], v[196:199]\n"
    "v_exp_f32 v167, v167\n"
    "v_mfma_f32_16x16x32_bf16 v[140:143], a[148:151], a[20:23], v[140:143]\n"
    "v_cvt_pk_bf16_f32 v161, v162, v163\n"
    "v_cvt_pk_bf16_f32 v169, v170, v171\n"
    "v_mfma_f32_16x16x32_bf16 v[140:143], a[152:155], a[24:27], v[140:143]\n"
    "v_exp_f32 v172, v172\n"
    "v_mfma_f32_16x16x32_bf16 v[140:143], a[156:159], a[28:31], v[140:143]\n"
    "buffer_load_dwordx4 v232, s[52:55], s91 offen offset:2048 lds\n"
    "v_mfma_f32_16x16x32_bf16 a[64:67], a[192:195], a[208:211], a[64:67]\n"
    "v_max3_f32 v240, v240, v135, v136\n"
    "v_max3_f32 v240, v240, v137, v138\n"
    "v_mfma_f32_16x16x32_bf16 a[72:75], a[192:195], a[212:215], a[72:75]\n"
    "v_exp_f32 v173, v173\n"
    "v_mfma_f32_16x16x32_bf16 a[80:83], a[192:195], a[216:219], a[80:83]\n"
    "v_exp_f32 v174, v174\n"
    "v_mfma_f32_16x16x32_bf16 a[88:91], a[192:195], a[220:223], a[88:91]\n"
    "v_exp_f32 v175, v175\n"
    "v_mfma_f32_16x16x32_bf16 a[68:71], a[196:199], a[208:211], a[68:71]\n"
    "buffer_load_dwordx4 v232, s[48:51], s91 offen offset:1024 lds\n"
    "v_mfma_f32_16x16x32_bf16 a[76:79], a[196:199], a[212:215], a[76:79]\n"
    "ds_read_b64_tr_b16 a[164:165], v227 offset:2048\n"
    "ds_read_b64_tr_b16 a[166:167], v227 offset:10240\n"
    "ds_read_b64_tr_b16 a[168:169], v226 offset:2560\n"
    "v_mfma_f32_16x16x32_bf16 a[84:87], a[196:199], a[216:219], a[84:87]\n"
    "ds_read_b64_tr_b16 a[170:171], v226 offset:10752\n"
    "ds_read_b64_tr_b16 a[172:173], v227 offset:2560\n"
    "ds_read_b64_tr_b16 a[174:175], v227 offset:10752\n"
    "v_mfma_f32_16x16x32_bf16 a[92:95], a[196:199], a[220:223], a[92:95]\n"
    "v_cvt_pk_bf16_f32 v162, v164, v165\n"
    "v_cvt_pk_bf16_f32 v163, v166, v167\n"
    "v_mfma_f32_16x16x32_bf16 v[144:147], a[128:131], a[32:35], v[200:203]\n"
    "v_max3_f32 v240, v240, v139, v140\n"
    "v_cvt_pk_bf16_f32 v170, v172, v173\n"
    "v_mfma_f32_16x16x32_bf16 v[144:147], a[132:135], a[36:39], v[144:147]\n"
    "buffer_load_dwordx4 v232, s[56:59], s91 offen offset:3072 lds\n"
    "v_mfma_f32_16x16x32_bf16 v[144:147], a[136:139], a[40:43], v[144:147]\n"
    "s_add_u32 m0, s94, 12288\n"
    "v_cvt_pk_bf16_f32 v171, v174, v175\n"
    "v_max3_f32 v240, v240, v141, v142\n"
    "v_mfma_f32_16x16x32_bf16 v[144:147], a[140:143], a[44:47], v[144:147]\n"
    "v_exp_f32 v176, v176\n"
    "v_mfma_f32_16x16x32_bf16 v[148:151], a[144:147], a[32:35], v[200:203]\n"
    "v_exp_f32 v177, v177\n"
    "v_mfma_f32_16x16x32_bf16 v[148:151], a[148:151], a[36:39], v[148:151]\n"
    "v_exp_f32 v178, v178\n"
    "v_mfma_f32_16x16x32_bf16 v[148:151], a[152:155], a[40:43], v[148:151]\n"
    "buffer_load_dwordx4 v224, s[60:63], s91 offen offset:0 lds\n"
    "s_nop 0\n"
    "v_max3_f32 v241, v144, v145, v146\n"
    "v_mfma_f32_16x16x32_bf16 v[148:151], a[156:159], a[44:47], v[148:151]\n"
    "v_exp_f32 v179, v179\n"
    "v_mfma_f32_16x16x32_bf16 v[152:155], a[128:131], a[48:51], v[204:207]\n"
    "v_exp_f32 v180, v180\n"
    "v_mfma_f32_16x16x32_bf16 v[152:155], a[132:135], a[52:55], v[152:155]\n"
    "v_exp_f32 v181, v181\n"
    "v_mfma_f32_16x16x32_bf16 v[152:155], a[136:139], a[56:59], v[152:155]\n"
    "v_exp_f32 v182, v182\n"
    "v_mfma_f32_16x16x32_bf16 v[152:155], a[140:143], a[60:63], v[152:155]\n"
    "v_max3_f32 v241, v241, v147, v148\n"
    "v_max3_f32 v241, v241, v149, v150\n"
    "v_mfma_f32_16x16x32_bf16 v[156:159], a[144:147], a[48:51], v[204:207]\n"
    "buffer_load_dwordx4 v224, s[64:67], s91 offen offset:2048 lds\n"
    "v_mfma_f32_16x16x32_bf16 v[156:159], a[148:151], a[52:55], v[156:159]\n"
    "ds_read_b64_tr_b16 a[176:177], v226 offset:3072\n"
    "ds_read_b64_tr_b16 a[178:179], v226 offset:11264\n"
    "ds_read_b64_tr_b16 a[180:181], v227 offset:3072\n"
    "v_mfma_f32_16x16x32_bf16 v[156:159], a[152:155], a[56:59], v[156:159]\n"
    "ds_read_b64_tr_b16 a[182:183], v227 offset:11264\n"
    "ds_read_b64_tr_b16 a[184:185], v226 offset:3584\n"
    "v_max3_f32 v241, v241, v151, v152\n"
    "v_max3_f32 v241, v241, v153, v154\n"
    "v_mfma_f32_16x16x32_bf16 v[156:159], a[156:159], a[60:63], v[156:159]\n"
    "v_exp_f32 v183, v183\n"
    "v_mfma_f32_16x16x32_bf16 v[208:211], v[248:251], v[160:163], v[208:211]\n"
    "ds_read_b64_tr_b16 a[186:187], v226 offset:11776\n"
    "ds_read_b64_tr_b16 a[188:189], v227 offset:3584\n"
    "v_mfma_f32_16x16x32_bf16 v[212:215], v[248:251], v[168:171], v[212:215]\n"
    "buffer_load_dwordx4 v224, s[60:63], s91 offen offset:1024 lds\n"
    "s_waitcnt lgkmcnt(7)\n"
    "v_mfma_f32_16x16x32_bf16 v[0:3], a[160:163], v[160:163], v[0:3]\n"
    "ds_read_b64_tr_b16 a[190:191], v227 offset:11776\n"
    "ds_read_b128 a[128:131], v225 offset:16384\n"
    "v_max3_f32 v241, v241, v155, v156\n"
    "v_max3_f32 v241, v241, v157, v158\n"
    "v_max3_f32 v240, v240, v143, v159\n"
    "v_max_f32 v240, v240, v241\n"
    "v_cmp_lt_f32 vcc, 0x41a00000, v240\n"
    "v_mfma_f32_16x16x32_bf16 v[8:11], a[160:163], v[168:171], v[8:11]\n"
    "v_exp_f32 v188, v188\n"
    "v_mfma_f32_16x16x32_bf16 v[4:7], a[164:167], v[160:163], v[4:7]\n"
    "v_cvt_pk_bf16_f32 v176, v176, v177\n"
    "v_cvt_pk_bf16_f32 v177, v178, v179\n"
    "v_mfma_f32_16x16x32_bf16 v[12:15], a[164:167], v[168:171], v[12:15]\n"
    "buffer_load_dwordx4 v224, s[64:67], s91 offen offset:3072 lds\n"
    "s_cbranch_vccnz K2_S1_2\n"
    "K2_R1_2:\n"
    "v_mfma_f32_16x16x32_bf16 v[32:35], a[168:171], v[160:163], v[32:35]\n"
    "s_add_u32 m0, s94, 0\n"
    "v_exp_f32 v189, v189\n"
    "v_mfma_f32_16x16x32_bf16 v[40:43], a[168:171], v[168:171], v[40:43]\n"
    "s_add_u32 s91, s91, 114688\n"
    "v_cvt_pk_bf16_f32 v178, v180, v181\n"
    "v_cvt_pk_bf16_f32 v179, v182, v183\n"
    "v_mfma_f32_16x16x32_bf16 v[36:39], a[172:175], v[160:163], v[36:39]\n"
    "v_cvt_pk_bf16_f32 v186, v188, v189\n"
    "v_mfma_f32_16x16x32_bf16 v[44:47], a[172:175], v[168:171], v[44:47]\n"
    "ds_read_b128 a[132:135], v225 offset:16896\n"
    "ds_read_b128 a[136:139], v225 offset:17408\n"
    "ds_read_b128 a[140:143], v225 offset:17920\n"
    "s_waitcnt lgkmcnt(3)\n"
    "v_mfma_f32_16x16x32_bf16 v[64:67], a[176:179], v[160:163], v[64:67]\n"
    "s_waitcnt vmcnt(25)\n"
    "buffer_load_dword v243, v253, s[24:27], s87 offen offset:0\n"
    "v_mfma_f32_16x16x32_bf16 v[72:75], a[176:179], v[168:171], v[72:75]\n"
    "s_add_u32 s87, s87, 12288\n"
    "ds_read_b128 a[144:147], v225 offset:24576\n"
    "ds_read_b128 a[148:151], v225 offset:25088\n"
    "v_mfma_f32_16x16x32_bf16 v[68:71], a[180:183], v[160:163], v[68:71]\n"
    "ds_read_b128 a[152:155], v225 offset:25600\n"
    "v_mfma_f32_16x16x32_bf16 v[76:79], a[180:183], v[168:171], v[76:79]\n"
    "v_exp_f32 v128, v128\n"
    "v_mfma_f32_16x16x32_bf16 v[96:99], a[184:187], v[160:163], v[96:99]\n"
    "v_exp_f32 v129, v129\n"
    "v_mfma_f32_16x16x32_bf16 v[104:107], a[184:187], v[168:171], v[104:107]\n"
    "v_exp_f32 v136, v136\n"
    "v_mfma_f32_16x16x32_bf16 v[100:103], a[188:191], v[160:163], v[100:103]\n"
    "v_exp_f32 v137, v137\n"
    "v_mfma_f32_16x16x32_bf16 v[108:111], a[188:191], v[168:171], v[108:111]\n"
    "v_exp_f32 v130, v130\n"
    "v_mfma_f32_16x16x32_bf16 a[96:99], a[200:203], a[208:211], a[96:99]\n"
    "v_exp_f32 v131, v131\n"
    "v_mfma_f32_16x16x32_bf16 a[104:107], a[200:203], a[212:215], a[104:107]\n"
    "v_cvt_pk_bf16_f32 v128, v128, v129\n"
    "v_cvt_pk_bf16_f32 v136, v136, v137\n"
    "v_mfma_f32_16x16x32_bf16 a[112:115], a[200:203], a[216:219], a[112:115]\n"
    "v_exp_f32 v138, v138\n"
    "v_mfma_f32_16x16x32_bf16 a[120:123], a[200:203], a[220:223], a[120:123]\n"
    "v_exp_f32 v139, v139\n"
    "v_mfma_f32_16x16x32_bf16 a[100:103], a[204:207], a[208:211], a[100:103]\n"
    "v_exp_f32 v132, v132\n"
    "v_mfma_f32_16x16x32_bf16 a[108:111], a[204:207], a[212:215], a[108:111]\n"
    "v_exp_f32 v133, v133\n"
    "v_mfma_f32_16x16x32_bf16 a[116:119], a[204:207], a[216:219], a[116:119]\n"
    "v_exp_f32 v134, v134\n"
    "v_mfma_f32_16x16x32_bf16 a[124:127], a[204:207], a[220:223], a[124:127]\n"
    "v_exp_f32 v135, v135\n"
    "v_mfma_f32_16x16x32_bf16 v[216:219], v[248:251], v[176:179], v[216:219]\n"
    "v_cvt_pk_bf16_f32 v129, v130, v131\n"
    "v_cvt_pk_bf16_f32 v137, v138, v139\n"
    "v_mfma_f32_16x16x32_bf16 v[220:223], v[248:251], v[184:187], v[220:223]\n"
    "v_exp_f32 v140, v140\n"
    "v_mfma_f32_16x16x32_bf16 v[16:19], a[160:163], v[176:179], v[16:19]\n"
    "v_exp_f32 v141, v141\n"
    "v_mfma_f32_16x16x32_bf16 v[24:27], a[160:163], v[184:187], v[24:27]\n"
    "v_exp_f32 v142, v142\n"
    "v_mfma_f32_16x16x32_bf16 v[20:23], a[164:167], v[176:179], v[20:23]\n"
    "v_exp_f32 v143, v143\n"
    "v_mfma_f32_16x16x32_bf16 v[28:31], a[164:167], v[184:187], v[28:31]\n"
    "v_cvt_pk_bf16_f32 v130, v132, v133\n"
    "v_cvt_pk_bf16_f32 v131, v134, v135\n"
    "v_mfma_f32_16x16x32_bf16 v[48:51], a[168:171], v[176:179], v[48:51]\n"
    "v_cvt_pk_bf16_f32 v138, v140, v141\n"
    "v_cvt_pk_bf16_f32 v139, v142, v143\n"
    "v_mfma_f32_16x16x32_bf16 v[56:59], a[168:171], v[184:187], v[56:59]\n"
    "v_exp_f32 v144, v144\n"
    "v_mfma_f32_16x16x32_bf16 v[52:55], a[172:175], v[176:179], v[52:55]\n"
    "v_exp_f32 v145, v145\n"
    "v_mfma_f32_16x16x32_bf16 v[60:63], a[172:175], v[184:187], v[60:63]\n"
    "v_exp_f32 v146, v146\n"
    "v_mfma_f32_16x16x32_bf16 v[80:83], a[176:179], v[176:179], v[80:83]\n"
    "v_exp_f32 v147, v147\n"
    "v_mfma_f32_16x16x32_bf16 v[88:91], a[176:179], v[184:187], v[88:91]\n"
    "v_exp_f32 v152, v152\n"
    "v_mfma_f32_16x16x32_bf16 v[84:87], a[180:183], v[176:179], v[84:87]\n"
    "v_exp_f32 v153, v153\n"
    "v_mfma_f32_16x16x32_bf16 v[92:95], a[180:183], v[184:187], v[92:95]\n"
    "ds_read_b128 a[156:159], v225 offset:26112\n"
    "ds_read_b64_tr_b16 a[160:161], v226 offset:18432\n"
    "v_mfma_f32_16x16x32_bf16 v[112:115], a[184:187], v[176:179], v[112:115]\n"
    "s_waitcnt vmcnt(24)\n"
    "ds_read_b64_tr_b16 a[224:225], v228 offset:0\n"
    "s_waitcnt vmcnt(22)\n"
    "ds_read_b64_tr_b16 a[226:227], v228 offset:2048\n"
    "s_waitcnt vmcnt(19)\n"
    "ds_read_b128 a[240:243], v230 offset:4096\n"
    "v_mfma_f32_16x16x32_bf16 v[120:123], a[184:187], v[184:187], v[120:123]\n"
    "ds_read_b128 a[244:247], v230 offset:5120\n"
    "v_cvt_pk_bf16_f32 v144, v144, v145\n"
    "v_mfma_f32_16x16x32_bf16 v[116:119], a[188:191], v[176:179], v[116:119]\n"
    "v_exp_f32 v148, v148\n"
    "v_mfma_f32_16x16x32_bf16 v[124:127], a[188:191], v[184:187], v[124:127]\n"
    "v_exp_f32 v149, v149\n"
    "s_cbranch_vccnz K2_S2_2\n"
    "K2_R2_2:\n"
    "v_mfma_f32_16x16x32_bf16 v[160:163], a[128:131], a[0:3], v[192:195]\n"
    "ds_read_b64_tr_b16 a[232:233], v228 offset:512\n"
    "ds_read_b64_tr_b16 a[234:235], v228 offset:2560\n"
    "s_waitcnt vmcnt(18)\n"
    "ds_read_b128 a[248:251], v230 offset:6144\n"
    "s_waitcnt lgkmcnt(9)\n"
    "v_mfma_f32_16x16x32_bf16 v[160:163], a[132:135], a[4:7], v[160:163]\n"
    "ds_read_b64_tr_b16 a[228:229], v229 offset:0\n"
    "ds_read_b64_tr_b16 a[230:231], v229 offset:2048\n"
    "ds_read_b128 a[252:255], v230 offset:7168\n"
    "v_mfma_f32_16x16x32_bf16 v[160:163], a[136:139], a[8:11], v[160:163]\n"
    "ds_read_b64_tr_b16 a[236:237], v229 offset:512\n"
    "v_cvt_pk_bf16_f32 v145, v146, v147\n"
    "v_mfma_f32_16x16x32_bf16 v[160:163], a[140:143], a[12:15], v[160:163]\n"
    "v_exp_f32 v150, v150\n"
    "v_mfma_f32_16x16x32_bf16 v[164:167], a[144:147], a[0:3], v[192:195]\n"
    "v_exp_f32 v151, v151\n"
    "v_mfma_f32_16x16x32_bf16 v[164:167], a[148:151], a[4:7], v[164:167]\n"
    "v_exp_f32 v154, v154\n"
    "v_mfma_f32_16x16x32_bf16 v[164:167], a[152:155], a[8:11], v[164:167]\n"
    "s_waitcnt lgkmcnt(4)\n"
    "v_exp_f32 v155, v155\n"
    "v_mfma_f32_16x16x32_bf16 v[164:167], a[156:159], a[12:15], v[164:167]\n"
    "v_max3_f32 v240, v160, v161, v162\n"
    "v_cvt_pk_bf16_f32 v152, v152, v153\n"
    "v_mfma_f32_16x16x32_bf16 v[168:171], a[128:131], a[16:19], v[196:199]\n"
    "v_exp_f32 v156, v156\n"
    "v_mfma_f32_16x16x32_bf16 v[168:171], a[132:135], a[20:23], v[168:171]\n"
    "ds_read_b64_tr_b16 a[238:239], v229 offset:2560\n"
    "ds_read_b64_tr_b16 a[162:163], v226 offset:26624\n"
    "v_mfma_f32_16x16x32_bf16 v[168:171], a[136:139], a[24:27], v[168:171]\n"
    "s_waitcnt lgkmcnt(2)\n"
    "buffer_load_dwordx4 v232, s[44:47], s91 offen offset:0 lds\n"
    "v_mfma_f32_16x16x32_bf16 v[168:171], a[140:143], a[28:31], v[168:171]\n"
    "v_max3_f32 v240, v240, v163, v164\n"
    "v_max3_f32 v240, v240, v165, v166\n"
    "v_mfma_f32_16x16x32_bf16 v[172:175], a[144:147], a[16:19], v[196:199]\n"
    "v_exp_f32 v157, v157\n"
    "v_mfma_f32_16x16x32_bf16 v[172:175], a[148:151], a[20:23], v[172:175]\n"
    "v_exp_f32 v158, v158\n"
    "v_mfma_f32_16x16x32_bf16 v[172:175], a[152:155], a[24:27], v[172:175]\n"
    "v_exp_f32 v159, v159\n"
    "v_mfma_f32_16x16x32_bf16 v[172:175], a[156:159], a[28:31], v[172:175]\n"
    "buffer_load_dwordx4 v232, s[48:51], s91 offen offset:1024 lds\n"
    "v_mfma_f32_16x16x32_bf16 a[64:67], a[224:227], a[240:243], a[64:67]\n"
    "ds_read_b64_tr_b16 a[164:165], v227 offset:18432\n"
    "ds_read_b64_tr_b16 a[166:167], v227 offset:26624\n"
    "ds_read_b64_tr_b16 a[168:169], v226 offset:18944\n"
    "v_mfma_f32_16x16x32_bf16 a[72:75], a[224:227], a[244:247], a[72:75]\n"
    "ds_read_b64_tr_b16 a[170:171], v226 offset:27136\n"
    "ds_read_b64_tr_b16 a[172:173], v227 offset:18944\n"
    "ds_read_b64_tr_b16 a[174:175], v227 offset:27136\n"
    "v_mfma_f32_16x16x32_bf16 a[80:83], a[224:227], a[248:251], a[80:83]\n"
    "ds_read_b64_tr_b16 a[176:177], v226 offset:19456\n"
    "ds_read_b64_tr_b16 a[178:179], v226 offset:27648\n"
    "ds_read_b64_tr_b16 a[180:181], v227 offset:19456\n"
    "v_mfma_f32_16x16x32_bf16 a[88:91], a[224:227], a[252:255], a[88:91]\n"
    "v_max3_f32 v240, v240, v167, v168\n"
    "v_max3_f32 v240, v240, v169, v170\n"
    "v_mfma_f32_16x16x32_bf16 a[68:71], a[228:231], a[240:243], a[68:71]\n"
    "s_waitcnt lgkmcnt(9)\n"
    "buffer_load_dwordx4 v232, s[52:55], s91 offen offset:2048 lds\n"
    "v_mfma_f32_16x16x32_bf16 a[76:79], a[228:231], a[244:247], a[76:79]\n"
    "ds_read_b64_tr_b16 a[182:183], v227 offset:27648\n"
    "ds_read_b64_tr_b16 a[184:185], v226 offset:19968\n"
    "ds_read_b64_tr_b16 a[186:187], v226 offset:28160\n"
    "v_mfma_f32_16x16x32_bf16 a[84:87], a[228:231], a[248:251], a[84:87]\n"
    "ds_read_b64_tr_b16 a[188:189], v227 offset:19968\n"
    "ds_read_b64_tr_b16 a[190:191], v227 offset:28160\n"
    "v_mfma_f32_16x16x32_bf16 a[92:95], a[228:231], a[252:255], a[92:95]\n"
    "v_max3_f32 v240, v240, v171, v172\n"
    "v_max3_f32 v240, v240, v173, v174\n"
    "v_mfma_f32_16x16x32_bf16 v[176:179], a[128:131], a[32:35], v[200:203]\n"
    "v_cvt_pk_bf16_f32 v146, v148, v149\n"
    "v_cvt_pk_bf16_f32 v147, v150, v151\n"
    "v_mfma_f32_16x16x32_bf16 v[176:179], a[132:135], a[36:39], v[176:179]\n"
    "buffer_load_dwordx4 v232, s[56:59], s91 offen offset:3072 lds\n"
    "v_mfma_f32_16x16x32_bf16 v[176:179], a[136:139], a[40:43], v[176:179]\n"
    "s_add_u32 m0, s94, 4096\n"
    "v_cvt_pk_bf16_f32 v153, v154, v155\n"
    "v_cvt_pk_bf16_f32 v154, v156, v157\n"
    "v_mfma_f32_16x16x32_bf16 v[176:179], a[140:143], a[44:47], v[176:179]\n"
    "v_cvt_pk_bf16_f32 v155, v158, v159\n"
    "v_xor_b32 v225, 0x8000, v225\n"
    "v_mfma_f32_16x16x32_bf16 v[180:183], a[144:147], a[32:35], v[200:203]\n"
    "v_xor_b32 v226, 0x8000, v226\n"
    "v_xor_b32 v227, 0x8000, v227\n"
    "v_mfma_f32_16x16x32_bf16 v[180:183], a[148:151], a[36:39], v[180:183]\n"
    "v_mfma_f32_16x16x32_bf16 v[180:183], a[152:155], a[40:43], v[180:183]\n"
    "buffer_load_dwordx4 v224, s[60:63], s91 offen offset:0 lds\n"
    "v_mfma_f32_16x16x32_bf16 v[180:183], a[156:159], a[44:47], v[180:183]\n"
    "v_max3_f32 v241, v176, v177, v178\n"
    "v_mfma_f32_16x16x32_bf16 v[184:187], a[128:131], a[48:51], v[204:207]\n"
    "v_mfma_f32_16x16x32_bf16 v[184:187], a[132:135], a[52:55], v[184:187]\n"
    "v_mfma_f32_16x16x32_bf16 v[184:187], a[136:139], a[56:59], v[184:187]\n"
    "v_mfma_f32_16x16x32_bf16 v[184:187], a[140:143], a[60:63], v[184:187]\n"
    "s_nop 2\n"
    "v_max3_f32 v241, v241, v179, v180\n"
    "v_max3_f32 v241, v241, v181, v182\n"
    "v_mfma_f32_16x16x32_bf16 v[188:191], a[144:147], a[48:51], v[204:207]\n"
    "buffer_load_dwordx4 v224, s[64:67], s91 offen offset:2048 lds\n"
    "v_mfma_f32_16x16x32_bf16 v[188:191], a[148:151], a[52:55], v[188:191]\n"
    "v_mfma_f32_16x16x32_bf16 v[188:191], a[152:155], a[56:59], v[188:191]\n"
    "v_mfma_f32_16x16x32_bf16 v[188:191], a[156:159], a[60:63], v[188:191]\n"
    "v_max3_f32 v241, v241, v183, v184\n"
    "v_max3_f32 v241, v241, v185, v186\n"
    "v_mfma_f32_16x16x32_bf16 v[208:211], v[248:251], v[128:131], v[208:211]\n"
    "v_mfma_f32_16x16x32_bf16 v[212:215], v[248:251], v[136:139], v[212:215]\n"
    "buffer_load_dwordx4 v224, s[60:63], s91 offen offset:1024 lds\n"
    "v_mfma_f32_16x16x32_bf16 v[0:3], a[160:163], v[128:131], v[0:3]\n"
    "v_mfma_f32_16x16x32_bf16 v[8:11], a[160:163], v[136:139], v[8:11]\n"
    "s_nop 0\n"
    "v_max3_f32 v241, v241, v187, v188\n"
    "v_max3_f32 v241, v241, v189, v190\n"
    "v_max3_f32 v240, v240, v175, v191\n"
    "v_max_f32 v240, v240, v241\n"
    "v_cmp_lt_f32 vcc, 0x41a00000, v240\n"
    "s_waitcnt vmcnt(17) lgkmcnt(0)\n"
    "s_barrier\n"
    "v_mfma_f32_16x16x32_bf16 v[4:7], a[164:167], v[128:131], v[4:7]\n"
    "ds_read_b128 a[128:131], v225 offset:0\n"
    "ds_read_b128 a[132:135], v225 offset:512\n"
    "ds_read_b128 a[136:139], v225 offset:1024\n"
    "v_mfma_f32_16x16x32_bf16 v[12:15], a[164:167], v[136:139], v[12:15]\n"
    "buffer_load_dwordx4 v224, s[64:67], s91 offen offset:3072 lds\n"
    "s_cbranch_vccnz K2_S1_3\n"
    "K2_R1_3:\n"
    "v_mfma_f32_16x16x32_bf16 v[32:35], a[168:171], v[128:131], v[32:35]\n"
    "s_add_u32 m0, s95, 0\n"
    "ds_read_b128 a[140:143], v225 offset:1536\n"
    "ds_read_b128 a[144:147], v225 offset:8192\n"
    "v_mfma_f32_16x16x32_bf16 v[40:43], a[168:171], v[136:139], v[40:43]\n"
    "ds_read_b128 a[148:151], v225 offset:8704\n"
    "ds_read_b128 a[152:155], v225 offset:9216\n"
    "v_mfma_f32_16x16x32_bf16 v[36:39], a[172:175], v[128:131], v[36:39]\n"
    "v_exp_f32 v160, v160\n"
    "v_mfma_f32_16x16x32_bf16 v[44:47], a[172:175], v[136:139], v[44:47]\n"
    "v_exp_f32 v161, v161\n"
    "v_mfma_f32_16x16x32_bf16 v[64:67], a[176:179], v[128:131], v[64:67]\n"
    "buffer_load_dwordx4 v234, s[68:71], s89 offen offset:0 lds\n"
    "v_mfma_f32_16x16x32_bf16 v[72:75], a[176:179], v[136:139], v[72:75]\n"
    "v_exp_f32 v168, v168\n"
    "v_mfma_f32_16x16x32_bf16 v[68:71], a[180:183], v[128:131], v[68:71]\n"
    "v_exp_f32 v169, v169\n"
    "v_mfma_f32_16x16x32_bf16 v[76:79], a[180:183], v[136:139], v[76:79]\n"
    "v_exp_f32 v162, v162\n"
    "v_mfma_f32_16x16x32_bf16 v[96:99], a[184:187], v[128:131], v[96:99]\n"
    "v_exp_f32 v163, v163\n"
    "v_mfma_f32_16x16x32_bf16 v[104:107], a[184:187], v[136:139], v[104:107]\n"
    "buffer_load_dwordx4 v234, s[72:75], s89 offen offset:1024 lds\n"
    "v_mfma_f32_16x16x32_bf16 v[100:103], a[188:191], v[128:131], v[100:103]\n"
    "v_cvt_pk_bf16_f32 v160, v160, v161\n"
    "v_mfma_f32_16x16x32_bf16 v[108:111], a[188:191], v[136:139], v[108:111]\n"
    "v_exp_f32 v170, v170\n"
    "v_mfma_f32_16x16x32_bf16 a[96:99], a[232:235], a[240:243], a[96:99]\n"
    "v_exp_f32 v184, v184\n"
    "v_mfma_f32_16x16x32_bf16 a[104:107], a[232:235], a[244:247], a[104:107]\n"
    "v_exp_f32 v185, v185\n"
    "v_mfma_f32_16x16x32_bf16 a[112:115], a[232:235], a[248:251], a[112:115]\n"
    "buffer_load_dwordx4 v235, s[76:79], s90 offen offset:2048 lds\n"
    "v_mfma_f32_16x16x32_bf16 a[120:123], a[232:235], a[252:255], a[120:123]\n"
    "v_exp_f32 v186, v186\n"
    "v_mfma_f32_16x16x32_bf16 a[100:103], a[236:239], a[240:243], a[100:103]\n"
    "v_exp_f32 v187, v187\n"
    "v_mfma_f32_16x16x32_bf16 a[108:111], a[236:239], a[244:247], a[108:111]\n"
    "v_exp_f32 v190, v190\n"
    "v_mfma_f32_16x16x32_bf16 a[116:119], a[236:239], a[248:251], a[116:119]\n"
    "v_cvt_pk_bf16_f32 v184, v184, v185\n"
    "v_cvt_pk_bf16_f32 v185, v186, v187\n"
    "v_mfma_f32_16x16x32_bf16 a[124:127], a[236:239], a[252:255], a[124:127]\n"
    "buffer_load_dwordx4 v235, s[80:83], s90 offen offset:3072 lds\n"
    "v_mfma_f32_16x16x32_bf16 v[216:219], v[248:251], v[144:147], v[216:219]\n"
    "s_add_u32 m0, s95, 16384\n"
    "v_exp_f32 v191, v191\n"
    "v_mfma_f32_16x16x32_bf16 v[220:223], v[248:251], v[152:155], v[220:223]\n"
    "s_add_u32 s90, s89, 8192\n"
    "v_cvt_pk_bf16_f32 v187, v190, v191\n"
    "s_and_b32 s90, s90, 131071\n"
    "v_mfma_f32_16x16x32_bf16 v[16:19], a[160:163], v[144:147], v[16:19]\n"
    "v_mfma_f32_16x16x32_bf16 v[24:27], a[160:163], v[152:155], v[24:27]\n"
    "v_mfma_f32_16x16x32_bf16 v[20:23], a[164:167], v[144:147], v[20:23]\n"
    "buffer_load_dwordx4 v234, s[68:71], s90 offen offset:0 lds\n"
    "v_mfma_f32_16x16x32_bf16 v[28:31], a[164:167], v[152:155], v[28:31]\n"
    "v_mfma_f32_16x16x32_bf16 v[48:51], a[168:171], v[144:147], v[48:51]\n"
    "v_mfma_f32_16x16x32_bf16 v[56:59], a[168:171], v[152:155], v[56:59]\n"
    "v_mfma_f32_16x16x32_bf16 v[52:55], a[172:175], v[144:147], v[52:55]\n"
    "v_mfma_f32_16x16x32_bf16 v[60:63], a[172:175], v[152:155], v[60:63]\n"
    "buffer_load_dwordx4 v234, s[72:75], s90 offen offset:1024 lds\n"
    "v_mfma_f32_16x16x32_bf16 v[80:83], a[176:179], v[144:147], v[80:83]\n"
    "ds_read_b128 a[156:159], v225 offset:9728\n"
    "v_mfma_f32_16x16x32_bf16 v[88:91], a[176:179], v[152:155], v[88:91]\n"
    "v_mfma_f32_16x16x32_bf16 v[84:87], a[180:183], v[144:147], v[84:87]\n"
    "v_mfma_f32_16x16x32_bf16 v[92:95], a[180:183], v[152:155], v[92:95]\n"
    "v_mfma_f32_16x16x32_bf16 v[112:115], a[184:187], v[144:147], v[112:115]\n"
    "buffer_load_dwordx4 v235, s[76:79], s89 offen offset:2048 lds\n"
    "v_mfma_f32_16x16x32_bf16 v[120:123], a[184:187], v[152:155], v[120:123]\n"
    "s_waitcnt vmcnt(21)\n"
    "ds_read_b64_tr_b16 a[192:193], v228 offset:8192\n"
    "s_waitcnt vmcnt(20)\n"
    "ds_read_b64_tr_b16 a[194:195], v228 offset:10240\n"
    "s_waitcnt vmcnt(17)\n"
    "ds_read_b128 a[208:211], v230 offset:12288\n"
    "v_mfma_f32_16x16x32_bf16 v[116:119], a[188:191], v[144:147], v[116:119]\n"
    "ds_read_b128 a[212:215], v230 offset:13312\n"
    "ds_read_b64_tr_b16 a[200:201], v228 offset:8704\n"
    "ds_read_b64_tr_b16 a[202:203], v228 offset:10752\n"
    "v_mfma_f32_16x16x32_bf16 v[124:127], a[188:191], v[152:155], v[124:127]\n"
    "buffer_load_dwordx4 v235, s[80:83], s89 offen offset:3072 lds\n"
    "s_sub_u32 s99, s99, 1\n"
    "s_cmp_lg_u32 s99, 0\n"
    "s_cbranch_scc1 K2_LOOP\n"
    "s_cbranch_vccnz K2_S2_11\n"
    "K2_R2_11:\n"
    "s_waitcnt lgkmcnt(6)\n"
    "v_mfma_f32_16x16x32_bf16 v[128:131], a[128:131], a[0:3], v[192:195]\n"
    "s_add_u32 m0, s94, 8192\n"
    "s_waitcnt vmcnt(17)\n"
    "ds_read_b128 a[216:219], v230 offset:14336\n"
    "ds_read_b128 a[220:223], v230 offset:15360\n"
    "v_mfma_f32_16x16x32_bf16 v[128:131], a[132:135], a[4:7], v[128:131]\n"
    "s_mul_i32 s91, s6, 229376\n"
    "ds_read_b64_tr_b16 a[196:197], v229 offset:8192\n"
    "ds_read_b64_tr_b16 a[198:199], v229 offset:10240\n"
    "v_mfma_f32_16x16x32_bf16 v[128:131], a[136:139], a[8:11], v[128:131]\n"
    "s_xor_b32 s95, s95, 32768\n"
    "ds_read_b64_tr_b16 a[204:205], v229 offset:8704\n"
    "s_add_u32 s6, s6, 1\n"
    "v_mfma_f32_16x16x32_bf16 v[128:131], a[140:143], a[12:15], v[128:131]\n"
    "s_nop 0\n"
    "ds_read_b64_tr_b16 a[206:207], v229 offset:10752\n"
    "ds_read_b64_tr_b16 a[160:161], v226 offset:2048\n"
    "v_mfma_f32_16x16x32_bf16 v[132:135], a[144:147], a[0:3], v[192:195]\n"
    "buffer_load_dword v242, v252, s[28:31], s86 offen offset:0\n"
    "v_mfma_f32_16x16x32_bf16 v[132:135], a[148:151], a[4:7], v[132:135]\n"
    "s_lshl_b32 s22, %1, 12\n"
    "v_exp_f32 v171, v171\n"
    "v_mfma_f32_16x16x32_bf16 v[132:135], a[152:155], a[8:11], v[132:135]\n"
    "s_and_b32 s23, s93, 1\n"
    "v_exp_f32 v164, v164\n"
    "v_mfma_f32_16x16x32_bf16 v[132:135], a[156:159], a[12:15], v[132:135]\n"
    "s_lshl_b32 s23, s23, 11\n"
    "v_max3_f32 v240, v128, v129, v130\n"
    "v_cvt_pk_bf16_f32 v168, v168, v169\n"
    "v_mfma_f32_16x16x32_bf16 v[136:139], a[128:131], a[16:19], v[196:199]\n"
    "s_mov_b64 s[72:73], s[4:5]\n"
    "v_exp_f32 v165, v165\n"
    "v_mfma_f32_16x16x32_bf16 v[136:139], a[132:135], a[20:23], v[136:139]\n"
    "s_waitcnt vmcnt(17)\n"
    "buffer_load_dword v243, v253, s[24:27], s87 offen offset:0\n"
    "v_mfma_f32_16x16x32_bf16 v[136:139], a[136:139], a[24:27], v[136:139]\n"
    "s_mov_b32 s75, 0x20000\n"
    "v_exp_f32 v166, v166\n"
    "v_mfma_f32_16x16x32_bf16 v[136:139], a[140:143], a[28:31], v[136:139]\n"
    "s_mov_b64 s[68:69], s[4:5]\n"
    "v_max3_f32 v240, v240, v131, v132\n"
    "v_max3_f32 v240, v240, v133, v134\n"
    "v_mfma_f32_16x16x32_bf16 v[140:143], a[144:147], a[16:19], v[196:199]\n"
    "s_mov_b32 s70, 131072\n"
    "ds_read_b64_tr_b16 a[162:163], v226 offset:10240\n"
    "ds_read_b64_tr_b16 a[164:165], v227 offset:2048\n"
    "v_mfma_f32_16x16x32_bf16 v[140:143], a[148:151], a[20:23], v[140:143]\n"
    "s_mov_b32 s71, 0x20000\n"
    "v_exp_f32 v167, v167\n"
    "v_mfma_f32_16x16x32_bf16 v[140:143], a[152:155], a[24:27], v[140:143]\n"
    "s_waitcnt lgkmcnt(2)\n"
    "buffer_load_dwordx4 v232, s[44:47], s91 offen offset:0 lds\n"
    "v_mfma_f32_16x16x32_bf16 v[140:143], a[156:159], a[28:31], v[140:143]\n"
    "s_and_b32 s6, s6, 7\n"
    "v_max3_f32 v240, v240, v135, v136\n"
    "v_cvt_pk_bf16_f32 v161, v162, v163\n"
    "v_mfma_f32_16x16x32_bf16 a[64:67], a[192:195], a[208:211], a[64:67]\n"
    "s_mov_b64 s[24:25], s[14:15]\n"
    "ds_read_b64_tr_b16 a[166:167], v227 offset:10240\n"
    "ds_read_b64_tr_b16 a[168:169], v226 offset:2560\n"
    "v_mfma_f32_16x16x32_bf16 a[72:75], a[192:195], a[212:215], a[72:75]\n"
    "s_mov_b32 s26, 32\n"
    "v_exp_f32 v172, v172\n"
    "v_mfma_f32_16x16x32_bf16 a[80:83], a[192:195], a[216:219], a[80:83]\n"
    "s_mov_b32 s27, 0x20000\n"
    "v_max3_f32 v240, v240, v137, v138\n"
    "v_cvt_pk_bf16_f32 v169, v170, v171\n"
    "v_mfma_f32_16x16x32_bf16 a[88:91], a[192:195], a[220:223], a[88:91]\n"
    "buffer_load_dwordx4 v232, s[52:55], s91 offen offset:2048 lds\n"
    "v_mfma_f32_16x16x32_bf16 a[68:71], a[196:199], a[208:211], a[68:71]\n"
    "s_add_u32 s86, s86, 4096\n"
    "v_exp_f32 v173, v173\n"
    "v_mfma_f32_16x16x32_bf16 a[76:79], a[196:199], a[212:215], a[76:79]\n"
    "s_add_u32 s87, s87, 12288\n"
    "v_exp_f32 v174, v174\n"
    "v_mfma_f32_16x16x32_bf16 a[84:87], a[196:199], a[216:219], a[84:87]\n"
    "v_exp_f32 v175, v175\n"
    "v_mfma_f32_16x16x32_bf16 a[92:95], a[196:199], a[220:223], a[92:95]\n"
    "v_cvt_pk_bf16_f32 v162, v164, v165\n"
    "v_cvt_pk_bf16_f32 v163, v166, v167\n"
    "v_mfma_f32_16x16x32_bf16 v[144:147], a[128:131], a[32:35], v[200:203]\n"
    "buffer_load_dwordx4 v232, s[48:51], s91 offen offset:1024 lds\n"
    "v_mfma_f32_16x16x32_bf16 v[144:147], a[132:135], a[36:39], v[144:147]\n"
    "ds_read_b64_tr_b16 a[170:171], v226 offset:10752\n"
    "ds_read_b64_tr_b16 a[172:173], v227 offset:2560\n"
    "ds_read_b64_tr_b16 a[174:175], v227 offset:10752\n"
    "v_mfma_f32_16x16x32_bf16 v[144:147], a[136:139], a[40:43], v[144:147]\n"
    "v_max3_f32 v240, v240, v139, v140\n"
    "v_cvt_pk_bf16_f32 v170, v172, v173\n"
    "v_mfma_f32_16x16x32_bf16 v[144:147], a[140:143], a[44:47], v[144:147]\n"
    "v_cvt_pk_bf16_f32 v171, v174, v175\n"
    "v_max3_f32 v240, v240, v141, v142\n"
    "v_mfma_f32_16x16x32_bf16 v[148:151], a[144:147], a[32:35], v[200:203]\n"
    "v_exp_f32 v176, v176\n"
    "v_mfma_f32_16x16x32_bf16 v[148:151], a[148:151], a[36:39], v[148:151]\n"
    "buffer_load_dwordx4 v232, s[56:59], s91 offen offset:3072 lds\n"
    "v_mfma_f32_16x16x32_bf16 v[148:151], a[152:155], a[40:43], v[148:151]\n"
    "s_add_u32 m0, s94, 12288\n"
    "ds_read_b64_tr_b16 a[176:177], v226 offset:3072\n"
    "v_max3_f32 v241, v144, v145, v146\n"
    "v_mfma_f32_16x16x32_bf16 v[148:151], a[156:159], a[44:47], v[148:151]\n"
    "v_exp_f32 v177, v177\n"
    "v_mfma_f32_16x16x32_bf16 v[152:155], a[128:131], a[48:51], v[204:207]\n"
    "v_exp_f32 v178, v178\n"
    "v_mfma_f32_16x16x32_bf16 v[152:155], a[132:135], a[52:55], v[152:155]\n"
    "v_exp_f32 v179, v179\n"
    "v_mfma_f32_16x16x32_bf16 v[152:155], a[136:139], a[56:59], v[152:155]\n"
    "buffer_load_dwordx4 v224, s[60:63], s91 offen offset:0 lds\n"
    "v_mfma_f32_16x16x32_bf16 v[152:155], a[140:143], a[60:63], v[152:155]\n"
    "v_max3_f32 v241, v241, v147, v148\n"
    "v_max3_f32 v241, v241, v149, v150\n"
    "v_mfma_f32_16x16x32_bf16 v[156:159], a[144:147], a[48:51], v[204:207]\n"
    "v_exp_f32 v180, v180\n"
    "v_mfma_f32_16x16x32_bf16 v[156:159], a[148:151], a[52:55], v[156:159]\n"
    "v_exp_f32 v181, v181\n"
    "v_mfma_f32_16x16x32_bf16 v[156:159], a[152:155], a[56:59], v[156:159]\n"
    "ds_read_b64_tr_b16 a[178:179], v226 offset:11264\n"
    "v_max3_f32 v241, v241, v151, v152\n"
    "v_max3_f32 v241, v241, v153, v154\n"
    "v_mfma_f32_16x16x32_bf16 v[156:159], a[156:159], a[60:63], v[156:159]\n"
    "ds_read_b64_tr_b16 a[180:181], v227 offset:3072\n"
    "ds_read_b64_tr_b16 a[182:183], v227 offset:11264\n"
    "v_mfma_f32_16x16x32_bf16 v[208:211], v[248:251], v[160:163], v[208:211]\n"
    "ds_read_b64_tr_b16 a[184:185], v226 offset:3584\n"
    "ds_read_b64_tr_b16 a[186:187], v226 offset:11776\n"
    "ds_read_b64_tr_b16 a[188:189], v227 offset:3584\n"
    "v_mfma_f32_16x16x32_bf16 v[212:215], v[248:251], v[168:171], v[212:215]\n"
    "buffer_load_dwordx4 v224, s[64:67], s91 offen offset:2048 lds\n"
    "s_waitcnt lgkmcnt(6)\n"
    "v_mfma_f32_16x16x32_bf16 v[0:3], a[160:163], v[160:163], v[0:3]\n"
    "ds_read_b64_tr_b16 a[190:191], v227 offset:11776\n"
    "ds_read_b128 a[128:131], v225 offset:16384\n"
    "v_max3_f32 v241, v241, v155, v156\n"
    "v_max3_f32 v241, v241, v157, v158\n"
    "v_max3_f32 v240, v240, v143, v159\n"
    "v_max_f32 v240, v240, v241\n"
    "v_cmp_lt_f32 vcc, 0x41a00000, v240\n"
    "v_mfma_f32_16x16x32_bf16 v[8:11], a[160:163], v[168:171], v[8:11]\n"
    "v_exp_f32 v182, v182\n"
    "v_mfma_f32_16x16x32_bf16 v[4:7], a[164:167], v[160:163], v[4:7]\n"
    "v_exp_f32 v183, v183\n"
    "v_mfma_f32_16x16x32_bf16 v[12:15], a[164:167], v[168:171], v[12:15]\n"
    "buffer_load_dwordx4 v224, s[60:63], s91 offen offset:1024 lds\n"
    "s_cbranch_vccnz K2_S1_12\n"
    "K2_R1_12:\n"
    "v_mfma_f32_16x16x32_bf16 v[32:35], a[168:171], v[160:163], v[32:35]\n"
    "v_exp_f32 v188, v188\n"
    "v_mfma_f32_16x16x32_bf16 v[40:43], a[168:171], v[168:171], v[40:43]\n"
    "v_cvt_pk_bf16_f32 v176, v176, v177\n"
    "v_cvt_pk_bf16_f32 v177, v178, v179\n"
    "v_mfma_f32_16x16x32_bf16 v[36:39], a[172:175], v[160:163], v[36:39]\n"
    "v_exp_f32 v189, v189\n"
    "v_mfma_f32_16x16x32_bf16 v[44:47], a[172:175], v[168:171], v[44:47]\n"
    "s_waitcnt lgkmcnt(0)\n"
    "v_cvt_pk_bf16_f32 v178, v180, v181\n"
    "v_cvt_pk_bf16_f32 v179, v182, v183\n"
    "v_mfma_f32_16x16x32_bf16 v[64:67], a[176:179], v[160:163], v[64:67]\n"
    "buffer_load_dwordx4 v224, s[64:67], s91 offen offset:3072 lds\n"
    "v_mfma_f32_16x16x32_bf16 v[72:75], a[176:179], v[168:171], v[72:75]\n"
    "s_add_u32 m0, s94, 0\n"
    "v_cvt_pk_bf16_f32 v186, v188, v189\n"
    "v_add_u32 v252, s22, v224\n"
    "v_mfma_f32_16x16x32_bf16 v[68:71], a[180:183], v[160:163], v[68:71]\n"
    "s_add_u32 s91, s91, 114688\n"
    "ds_read_b128 a[132:135], v225 offset:16896\n"
    "ds_read_b128 a[136:139], v225 offset:17408\n"
    "v_mfma_f32_16x16x32_bf16 v[76:79], a[180:183], v[168:171], v[76:79]\n"
    "s_add_u32 s22, %1, 0\n"
    "ds_read_b128 a[140:143], v225 offset:17920\n"
    "ds_read_b128 a[144:147], v225 offset:24576\n"
    "v_mfma_f32_16x16x32_bf16 v[96:99], a[184:187], v[160:163], v[96:99]\n"
    "s_and_b32 s22, s22, 3\n"
    "ds_read_b128 a[148:151], v225 offset:25088\n"
    "s_lshl_b32 s22, s22, 14\n"
    "v_mfma_f32_16x16x32_bf16 v[104:107], a[184:187], v[168:171], v[104:107]\n"
    "ds_read_b128 a[152:155], v225 offset:25600\n"
    "v_xor_b32 v253, 4096, v252\n"
    "v_mfma_f32_16x16x32_bf16 v[100:103], a[188:191], v[160:163], v[100:103]\n"
    "v_exp_f32 v128, v128\n"
    "v_mfma_f32_16x16x32_bf16 v[108:111], a[188:191], v[168:171], v[108:111]\n"
    "v_exp_f32 v129, v129\n"
    "v_mfma_f32_16x16x32_bf16 a[96:99], a[200:203], a[208:211], a[96:99]\n"
    "v_exp_f32 v136, v136\n"
    "v_mfma_f32_16x16x32_bf16 a[104:107], a[200:203], a[212:215], a[104:107]\n"
    "v_exp_f32 v137, v137\n"
    "v_mfma_f32_16x16x32_bf16 a[112:115], a[200:203], a[216:219], a[112:115]\n"
    "v_exp_f32 v130, v130\n"
    "v_mfma_f32_16x16x32_bf16 a[120:123], a[200:203], a[220:223], a[120:123]\n"
    "v_exp_f32 v131, v131\n"
    "v_mfma_f32_16x16x32_bf16 a[100:103], a[204:207], a[208:211], a[100:103]\n"
    "v_cvt_pk_bf16_f32 v128, v128, v129\n"
    "v_cvt_pk_bf16_f32 v136, v136, v137\n"
    "v_mfma_f32_16x16x32_bf16 a[108:111], a[204:207], a[212:215], a[108:111]\n"
    "v_exp_f32 v138, v138\n"
    "v_mfma_f32_16x16x32_bf16 a[116:119], a[204:207], a[216:219], a[116:119]\n"
    "v_exp_f32 v139, v139\n"
    "v_mfma_f32_16x16x32_bf16 a[124:127], a[204:207], a[220:223], a[124:127]\n"
    "v_exp_f32 v132, v132\n"
    "v_mfma_f32_16x16x32_bf16 v[216:219], v[248:251], v[176:179], v[216:219]\n"
    "v_exp_f32 v133, v133\n"
    "v_mfma_f32_16x16x32_bf16 v[220:223], v[248:251], v[184:187], v[220:223]\n"
    "v_exp_f32 v134, v134\n"
    "v_mfma_f32_16x16x32_bf16 v[16:19], a[160:163], v[176:179], v[16:19]\n"
    "v_exp_f32 v135, v135\n"
    "v_mfma_f32_16x16x32_bf16 v[24:27], a[160:163], v[184:187], v[24:27]\n"
    "v_cvt_pk_bf16_f32 v129, v130, v131\n"
    "v_cvt_pk_bf16_f32 v137, v138, v139\n"
    "v_mfma_f32_16x16x32_bf16 v[20:23], a[164:167], v[176:179], v[20:23]\n"
    "v_exp_f32 v140, v140\n"
    "v_mfma_f32_16x16x32_bf16 v[28:31], a[164:167], v[184:187], v[28:31]\n"
    "v_exp_f32 v141, v141\n"
    "v_mfma_f32_16x16x32_bf16 v[48:51], a[168:171], v[176:179], v[48:51]\n"
    "v_exp_f32 v142, v142\n"
    "v_mfma_f32_16x16x32_bf16 v[56:59], a[168:171], v[184:187], v[56:59]\n"
    "v_exp_f32 v143, v143\n"
    "v_mfma_f32_16x16x32_bf16 v[52:55], a[172:175], v[176:179], v[52:55]\n"
    "v_cvt_pk_bf16_f32 v130, v132, v133\n"
    "v_cvt_pk_bf16_f32 v131, v134, v135\n"
    "v_mfma_f32_16x16x32_bf16 v[60:63], a[172:175], v[184:187], v[60:63]\n"
    "v_cvt_pk_bf16_f32 v138, v140, v141\n"
    "v_cvt_pk_bf16_f32 v139, v142, v143\n"
    "v_mfma_f32_16x16x32_bf16 v[80:83], a[176:179], v[176:179], v[80:83]\n"
    "v_exp_f32 v144, v144\n"
    "v_mfma_f32_16x16x32_bf16 v[88:91], a[176:179], v[184:187], v[88:91]\n"
    "v_exp_f32 v145, v145\n"
    "v_mfma_f32_16x16x32_bf16 v[84:87], a[180:183], v[176:179], v[84:87]\n"
    "v_exp_f32 v146, v146\n"
    "v_mfma_f32_16x16x32_bf16 v[92:95], a[180:183], v[184:187], v[92:95]\n"
    "ds_read_b128 a[156:159], v225 offset:26112\n"
    "ds_read_b64_tr_b16 a[160:161], v226 offset:18432\n"
    "v_mfma_f32_16x16x32_bf16 v[112:115], a[184:187], v[176:179], v[112:115]\n"
    "s_waitcnt vmcnt(24)\n"
    "ds_read_b64_tr_b16 a[224:225], v228 offset:0\n"
    "s_waitcnt vmcnt(22)\n"
    "ds_read_b64_tr_b16 a[226:227], v228 offset:2048\n"
    "s_waitcnt vmcnt(19)\n"
    "ds_read_b128 a[240:243], v230 offset:4096\n"
    "v_mfma_f32_16x16x32_bf16 v[120:123], a[184:187], v[184:187], v[120:123]\n"
    "ds_read_b128 a[244:247], v230 offset:5120\n"
    "v_cvt_pk_bf16_f32 v144, v144, v145\n"
    "v_mfma_f32_16x16x32_bf16 v[116:119], a[188:191], v[176:179], v[116:119]\n"
    "v_exp_f32 v147, v147\n"
    "v_mfma_f32_16x16x32_bf16 v[124:127], a[188:191], v[184:187], v[124:127]\n"
    "v_exp_f32 v152, v152\n"
    "s_cbranch_vccnz K2_S2_12\n"
    "K2_R2_12:\n"
    "v_mfma_f32_16x16x32_bf16 v[160:163], a[128:131], a[0:3], v[192:195]\n"
    "ds_read_b64_tr_b16 a[232:233], v228 offset:512\n"
    "ds_read_b64_tr_b16 a[234:235], v228 offset:2560\n"
    "s_waitcnt vmcnt(18)\n"
    "ds_read_b128 a[248:251], v230 offset:6144\n"
    "s_waitcnt lgkmcnt(9)\n"
    "v_mfma_f32_16x16x32_bf16 v[160:163], a[132:135], a[4:7], v[160:163]\n"
    "ds_read_b64_tr_b16 a[228:229], v229 offset:0\n"
    "ds_read_b64_tr_b16 a[230:231], v229 offset:2048\n"
    "ds_read_b128 a[252:255], v230 offset:7168\n"
    "v_mfma_f32_16x16x32_bf16 v[160:163], a[136:139], a[8:11], v[160:163]\n"
    "ds_read_b64_tr_b16 a[236:237], v229 offset:512\n"
    "v_cvt_pk_bf16_f32 v145, v146, v147\n"
    "v_mfma_f32_16x16x32_bf16 v[160:163], a[140:143], a[12:15], v[160:163]\n"
    "v_exp_f32 v153, v153\n"
    "v_mfma_f32_16x16x32_bf16 v[164:167], a[144:147], a[0:3], v[192:195]\n"
    "v_exp_f32 v148, v148\n"
    "v_mfma_f32_16x16x32_bf16 v[164:167], a[148:151], a[4:7], v[164:167]\n"
    "v_exp_f32 v149, v149\n"
    "v_mfma_f32_16x16x32_bf16 v[164:167], a[152:155], a[8:11], v[164:167]\n"
    "s_waitcnt lgkmcnt(4)\n"
    "v_exp_f32 v150, v150\n"
    "v_mfma_f32_16x16x32_bf16 v[164:167], a[156:159], a[12:15], v[164:167]\n"
    "v_max3_f32 v240, v160, v161, v162\n"
    "v_cvt_pk_bf16_f32 v152, v152, v153\n"
    "v_mfma_f32_16x16x32_bf16 v[168:171], a[128:131], a[16:19], v[196:199]\n"
    "v_exp_f32 v151, v151\n"
    "v_mfma_f32_16x16x32_bf16 v[168:171], a[132:135], a[20:23], v[168:171]\n"
    "ds_read_b64_tr_b16 a[238:239], v229 offset:2560\n"
    "ds_read_b64_tr_b16 a[162:163], v226 offset:26624\n"
    "v_mfma_f32_16x16x32_bf16 v[168:171], a[136:139], a[24:27], v[168:171]\n"
    "s_waitcnt lgkmcnt(2)\n"
    "buffer_load_dwordx4 v232, s[44:47], s91 offen offset:0 lds\n"
    "v_mfma_f32_16x16x32_bf16 v[168:171], a[140:143], a[28:31], v[168:171]\n"
    "s_mov_b64 s[44:45], s[12:13]\n"
    "v_max3_f32 v240, v240, v163, v164\n"
    "v_max3_f32 v240, v240, v165, v166\n"
    "v_mfma_f32_16x16x32_bf16 v[172:175], a[144:147], a[16:19], v[196:199]\n"
    "s_mov_b32 s47, 0x20000\n"
    "ds_read_b64_tr_b16 a[164:165], v227 offset:18432\n"
    "ds_read_b64_tr_b16 a[166:167], v227 offset:26624\n"
    "v_mfma_f32_16x16x32_bf16 v[172:175], a[148:151], a[20:23], v[172:175]\n"
    "ds_read_b64_tr_b16 a[168:169], v226 offset:18944\n"
    "ds_read_b64_tr_b16 a[170:171], v226 offset:27136\n"
    "ds_read_b64_tr_b16 a[172:173], v227 offset:18944\n"
    "v_mfma_f32_16x16x32_bf16 v[172:175], a[152:155], a[24:27], v[172:175]\n"
    "ds_read_b64_tr_b16 a[174:175], v227 offset:27136\n"
    "ds_read_b64_tr_b16 a[176:177], v226 offset:19456\n"
    "ds_read_b64_tr_b16 a[178:179], v226 offset:27648\n"
    "v_mfma_f32_16x16x32_bf16 v[172:175], a[156:159], a[28:31], v[172:175]\n"
    "buffer_load_dwordx4 v232, s[48:51], s91 offen offset:1024 lds\n"
    "v_mfma_f32_16x16x32_bf16 a[64:67], a[224:227], a[240:243], a[64:67]\n"
    "s_mov_b64 s[48:49], s[12:13]\n"
    "ds_read_b64_tr_b16 a[180:181], v227 offset:19456\n"
    "ds_read_b64_tr_b16 a[182:183], v227 offset:27648\n"
    "v_mfma_f32_16x16x32_bf16 a[72:75], a[224:227], a[244:247], a[72:75]\n"
    "s_mov_b32 s51, 0x20000\n"
    "ds_read_b64_tr_b16 a[184:185], v226 offset:19968\n"
    "ds_read_b64_tr_b16 a[186:187], v226 offset:28160\n"
    "v_mfma_f32_16x16x32_bf16 a[80:83], a[224:227], a[248:251], a[80:83]\n"
    "ds_read_b64_tr_b16 a[188:189], v227 offset:19968\n"
    "s_waitcnt lgkmcnt(10)\n"
    "ds_read_b64_tr_b16 a[190:191], v227 offset:28160\n"
    "v_mfma_f32_16x16x32_bf16 a[88:91], a[224:227], a[252:255], a[88:91]\n"
    "v_max3_f32 v240, v240, v167, v168\n"
    "v_max3_f32 v240, v240, v169, v170\n"
    "v_mfma_f32_16x16x32_bf16 a[68:71], a[228:231], a[240:243], a[68:71]\n"
    "buffer_load_dwordx4 v232, s[52:55], s91 offen offset:2048 lds\n"
    "v_mfma_f32_16x16x32_bf16 a[76:79], a[228:231], a[244:247], a[76:79]\n"
    "s_mov_b64 s[52:53], s[12:13]\n"
    "v_max3_f32 v240, v240, v171, v172\n"
    "v_max3_f32 v240, v240, v173, v174\n"
    "v_mfma_f32_16x16x32_bf16 a[84:87], a[228:231], a[248:251], a[84:87]\n"
    "s_mov_b32 s55, 0x20000\n"
    "v_exp_f32 v154, v154\n"
    "v_mfma_f32_16x16x32_bf16 a[92:95], a[228:231], a[252:255], a[92:95]\n"
    "v_exp_f32 v155, v155\n"
    "v_mfma_f32_16x16x32_bf16 v[176:179], a[128:131], a[32:35], v[200:203]\n"
    "v_exp_f32 v156, v156\n"
    "v_mfma_f32_16x16x32_bf16 v[176:179], a[132:135], a[36:39], v[176:179]\n"
    "buffer_load_dwordx4 v232, s[56:59], s91 offen offset:3072 lds\n"
    "v_mfma_f32_16x16x32_bf16 v[176:179], a[136:139], a[40:43], v[176:179]\n"
    "s_add_u32 m0, s94, 4096\n"
    "v_exp_f32 v157, v157\n"
    "v_mfma_f32_16x16x32_bf16 v[176:179], a[140:143], a[44:47], v[176:179]\n"
    "s_mov_b64 s[56:57], s[12:13]\n"
    "v_exp_f32 v158, v158\n"
    "v_mfma_f32_16x16x32_bf16 v[180:183], a[144:147], a[32:35], v[200:203]\n"
    "s_mov_b32 s59, 0x20000\n"
    "v_exp_f32 v159, v159\n"
    "v_mfma_f32_16x16x32_bf16 v[180:183], a[148:151], a[36:39], v[180:183]\n"
    "v_cvt_pk_bf16_f32 v146, v148, v149\n"
    "v_cvt_pk_bf16_f32 v147, v150, v151\n"
    "v_mfma_f32_16x16x32_bf16 v[180:183], a[152:155], a[40:43], v[180:183]\n"
    "buffer_load_dwordx4 v224, s[60:63], s91 offen offset:0 lds\n"
    "v_mfma_f32_16x16x32_bf16 v[180:183], a[156:159], a[44:47], v[180:183]\n"
    "v_max3_f32 v241, v176, v177, v178\n"
    "v_cvt_pk_bf16_f32 v153, v154, v155\n"
    "v_mfma_f32_16x16x32_bf16 v[184:187], a[128:131], a[48:51], v[204:207]\n"
    "v_cvt_pk_bf16_f32 v154, v156, v157\n"
    "v_cvt_pk_bf16_f32 v155, v158, v159\n"
    "v_mfma_f32_16x16x32_bf16 v[184:187], a[132:135], a[52:55], v[184:187]\n"
    "v_xor_b32 v225, 0x8000, v225\n"
    "v_xor_b32 v226, 0x8000, v226\n"
    "v_mfma_f32_16x16x32_bf16 v[184:187], a[136:139], a[56:59], v[184:187]\n"
    "v_xor_b32 v227, 0x8000, v227\n"
    "v_xor_b32 v254, 8192, v252\n"
    "v_max3_f32 v241, v241, v179, v180\n"
    "v_mfma_f32_16x16x32_bf16 v[184:187], a[140:143], a[60:63], v[184:187]\n"
    "buffer_load_dwordx4 v224, s[64:67], s91 offen offset:2048 lds\n"
    "v_mfma_f32_16x16x32_bf16 v[188:191], a[144:147], a[48:51], v[204:207]\n"
    "v_max3_f32 v241, v241, v181, v182\n"
    "v_xor_b32 v231, 12288, v252\n"
    "v_mfma_f32_16x16x32_bf16 v[188:191], a[148:151], a[52:55], v[188:191]\n"
    "v_mov_b32 v234, 0xbdd2d3e8\n"
    "v_mfma_f32_16x16x32_bf16 v[188:191], a[152:155], a[56:59], v[188:191]\n"
    "v_mfma_f32_16x16x32_bf16 v[188:191], a[156:159], a[60:63], v[188:191]\n"
    "v_max3_f32 v241, v241, v183, v184\n"
    "v_max3_f32 v241, v241, v185, v186\n"
    "v_mfma_f32_16x16x32_bf16 v[208:211], v[248:251], v[128:131], v[208:211]\n"
    "buffer_load_dwordx4 v224, s[60:63], s91 offen offset:1024 lds\n"
    "v_mfma_f32_16x16x32_bf16 v[212:215], v[248:251], v[136:139], v[212:215]\n"
    "s_mov_b64 s[60:61], s[4:5]\n"
    "s_mov_b32 s62, 1835008\n"
    "s_mov_b32 s63, 0x20000\n"
    "v_mfma_f32_16x16x32_bf16 v[0:3], a[160:163], v[128:131], v[0:3]\n"
    "v_mfma_f32_16x16x32_bf16 v[8:11], a[160:163], v[136:139], v[8:11]\n"
    "v_max3_f32 v241, v241, v187, v188\n"
    "v_max3_f32 v241, v241, v189, v190\n"
    "v_max3_f32 v240, v240, v175, v191\n"
    "v_max_f32 v240, v240, v241\n"
    "v_cmp_lt_f32 vcc, 0x41a00000, v240\n"
    "s_waitcnt vmcnt(17) lgkmcnt(0)\n"
    "s_barrier\n"
    "v_mfma_f32_16x16x32_bf16 v[4:7], a[164:167], v[128:131], v[4:7]\n"
    "buffer_load_dwordx4 v224, s[64:67], s91 offen offset:3072 lds\n"
    "v_mfma_f32_16x16x32_bf16 v[12:15], a[164:167], v[136:139], v[12:15]\n"
    "s_add_u32 m0, s95, 0\n"
    "ds_read_b128 a[128:131], v225 offset:0\n"
    "ds_read_b128 a[132:135], v225 offset:512\n"
    "s_cbranch_vccnz K2_S1_13\n"
    "K2_R1_13:\n"
    "v_mfma_f32_16x16x32_bf16 v[32:35], a[168:171], v[128:131], v[32:35]\n"
    "s_and_b32 s95, s97, 3\n"
    "ds_read_b128 a[136:139], v225 offset:1024\n"
    "ds_read_b128 a[140:143], v225 offset:1536\n"
    "v_mfma_f32_16x16x32_bf16 v[40:43], a[168:171], v[136:139], v[40:43]\n"
    "s_mov_b64 s[64:65], s[4:5]\n"
    "ds_read_b128 a[144:147], v225 offset:8192\n"
    "ds_read_b128 a[148:151], v225 offset:8704\n"
    "v_mfma_f32_16x16x32_bf16 v[36:39], a[172:175], v[128:131], v[36:39]\n"
    "s_mov_b32 s66, 9216\n"
    "v_exp_f32 v160, v160\n"
    "v_mfma_f32_16x16x32_bf16 v[44:47], a[172:175], v[136:139], v[44:47]\n"
    "buffer_load_dwordx4 v235, s[76:79], s90 offen offset:2048 lds\n"
    "v_mfma_f32_16x16x32_bf16 v[64:67], a[176:179], v[128:131], v[64:67]\n"
    "s_mov_b32 s67, 0x20000\n"
    "ds_read_b128 a[152:155], v225 offset:9216\n"
    "ds_read_b128 a[156:159], v225 offset:9728\n"
    "v_mfma_f32_16x16x32_bf16 v[72:75], a[176:179], v[136:139], v[72:75]\n"
    "s_mov_b64 s[76:77], s[4:5]\n"
    "v_exp_f32 v161, v161\n"
    "v_mfma_f32_16x16x32_bf16 v[68:71], a[180:183], v[128:131], v[68:71]\n"
    "s_mul_i32 s91, s6, 229376\n"
    "v_exp_f32 v168, v168\n"
    "v_mfma_f32_16x16x32_bf16 v[76:79], a[180:183], v[136:139], v[76:79]\n"
    "s_and_b32 s91, s97, 3\n"
    "v_exp_f32 v169, v169\n"
    "v_mfma_f32_16x16x32_bf16 v[96:99], a[184:187], v[128:131], v[96:99]\n"
    "buffer_load_dwordx4 v235, s[80:83], s90 offen offset:3072 lds\n"
    "v_mfma_f32_16x16x32_bf16 v[104:107], a[184:187], v[136:139], v[104:107]\n"
    "s_add_u32 m0, s94, 8192\n"
    "v_exp_f32 v162, v162\n"
    "v_mfma_f32_16x16x32_bf16 v[100:103], a[188:191], v[128:131], v[100:103]\n"
    "s_and_b32 s90, s96, 6\n"
    "v_exp_f32 v163, v163\n"
    "v_mfma_f32_16x16x32_bf16 v[108:111], a[188:191], v[136:139], v[108:111]\n"
    "s_lshl_b32 s90, s90, 2\n"
    "v_exp_f32 v170, v170\n"
    "v_mfma_f32_16x16x32_bf16 a[96:99], a[232:235], a[240:243], a[96:99]\n"
    "s_mul_i32 s91, s91, 393216\n"
    "v_cvt_pk_bf16_f32 v160, v160, v161\n"
    "v_cvt_pk_bf16_f32 v168, v168, v169\n"
    "v_mfma_f32_16x16x32_bf16 a[104:107], a[232:235], a[244:247], a[104:107]\n"
    "s_mov_b32 s89, s91\n"
    "v_exp_f32 v171, v171\n"
    "v_mfma_f32_16x16x32_bf16 a[112:115], a[232:235], a[248:251], a[112:115]\n"
    "s_mov_b32 s78, 73728\n"
    "v_exp_f32 v164, v164\n"
    "v_mfma_f32_16x16x32_bf16 a[120:123], a[232:235], a[252:255], a[120:123]\n"
    "s_mov_b32 s79, 0x20000\n"
    "v_exp_f32 v165, v165\n"
    "v_mfma_f32_16x16x32_bf16 a[100:103], a[236:239], a[240:243], a[100:103]\n"
    "s_mov_b64 s[80:81], s[16:17]\n"
    "v_exp_f32 v166, v166\n"
    "v_mfma_f32_16x16x32_bf16 a[108:111], a[236:239], a[244:247], a[108:111]\n"
    "s_mov_b32 s83, 0x20000\n"
    "v_exp_f32 v167, v167\n"
    "v_mfma_f32_16x16x32_bf16 a[116:119], a[236:239], a[248:251], a[116:119]\n"
    "s_add_u32 s6, s6, 1\n"
    "v_exp_f32 v172, v172\n"
    "v_mfma_f32_16x16x32_bf16 a[124:127], a[236:239], a[252:255], a[124:127]\n"
    "s_and_b32 s6, s6, 7\n"
    "v_exp_f32 v173, v173\n"
    "v_mfma_f32_16x16x32_bf16 v[216:219], v[248:251], v[144:147], v[216:219]\n"
    "v_exp_f32 v174, v174\n"
    "v_mfma_f32_16x16x32_bf16 v[220:223], v[248:251], v[152:155], v[220:223]\n"
    "v_cvt_pk_bf16_f32 v161, v162, v163\n"
    "v_cvt_pk_bf16_f32 v169, v170, v171\n"
    "v_mfma_f32_16x16x32_bf16 v[16:19], a[160:163], v[144:147], v[16:19]\n"
    "v_exp_f32 v175, v175\n"
    "v_mfma_f32_16x16x32_bf16 v[24:27], a[160:163], v[152:155], v[24:27]\n"
    "v_cvt_pk_bf16_f32 v162, v164, v165\n"
    "v_cvt_pk_bf16_f32 v163, v166, v167\n"
    "v_mfma_f32_16x16x32_bf16 v[20:23], a[164:167], v[144:147], v[20:23]\n"
    "v_cvt_pk_bf16_f32 v170, v172, v173\n"
    "v_cvt_pk_bf16_f32 v171, v174, v175\n"
    "v_mfma_f32_16x16x32_bf16 v[28:31], a[164:167], v[152:155], v[28:31]\n"
    "v_exp_f32 v176, v176\n"
    "v_mfma_f32_16x16x32_bf16 v[48:51], a[168:171], v[144:147], v[48:51]\n"
    "v_exp_f32 v177, v177\n"
    "v_mfma_f32_16x16x32_bf16 v[56:59], a[168:171], v[152:155], v[56:59]\n"
    "v_exp_f32 v178, v178\n"
    "v_mfma_f32_16x16x32_bf16 v[52:55], a[172:175], v[144:147], v[52:55]\n"
    "v_exp_f32 v179, v179\n"
    "v_mfma_f32_16x16x32_bf16 v[60:63], a[172:175], v[152:155], v[60:63]\n"
    "v_exp_f32 v184, v184\n"
    "v_mfma_f32_16x16x32_bf16 v[80:83], a[176:179], v[144:147], v[80:83]\n"
    "v_exp_f32 v185, v185\n"
    "v_mfma_f32_16x16x32_bf16 v[88:91], a[176:179], v[152:155], v[88:91]\n"
    "v_exp_f32 v180, v180\n"
    "v_mfma_f32_16x16x32_bf16 v[84:87], a[180:183], v[144:147], v[84:87]\n"
    "v_exp_f32 v181, v181\n"
    "v_mfma_f32_16x16x32_bf16 v[92:95], a[180:183], v[152:155], v[92:95]\n"
    "v_exp_f32 v186, v186\n"
    "v_mfma_f32_16x16x32_bf16 v[112:115], a[184:187], v[144:147], v[112:115]\n"
    "v_exp_f32 v187, v187\n"
    "v_mfma_f32_16x16x32_bf16 v[120:123], a[184:187], v[152:155], v[120:123]\n"
    "v_exp_f32 v182, v182\n"
    "v_mfma_f32_16x16x32_bf16 v[116:119], a[188:191], v[144:147], v[116:119]\n"
    "v_exp_f32 v183, v183\n"
    "v_mfma_f32_16x16x32_bf16 v[124:127], a[188:191], v[152:155], v[124:127]\n"
    "v_cvt_pk_bf16_f32 v176, v176, v177\n"
    "v_cvt_pk_bf16_f32 v177, v178, v179\n"
    "s_cbranch_vccnz K2_S2_13\n"
    "K2_R2_13:\n"
    "s_waitcnt lgkmcnt(0)\n"
    "v_mfma_f32_16x16x32_bf16 v[128:131], a[128:131], a[0:3], v[192:195]\n"
    "s_waitcnt vmcnt(15)\n"
    "ds_read_b64_tr_b16 a[192:193], v228 offset:8192\n"
    "s_waitcnt vmcnt(14)\n"
    "ds_read_b64_tr_b16 a[194:195], v228 offset:10240\n"
    "s_waitcnt vmcnt(11)\n"
    "ds_read_b128 a[208:211], v230 offset:12288\n"
    "v_mfma_f32_16x16x32_bf16 v[128:131], a[132:135], a[4:7], v[128:131]\n"
    "ds_read_b64_tr_b16 a[200:201], v228 offset:8704\n"
    "ds_read_b64_tr_b16 a[202:203], v228 offset:10752\n"
    "ds_read_b128 a[212:215], v230 offset:13312\n"
    "v_mfma_f32_16x16x32_bf16 v[128:131], a[136:139], a[8:11], v[128:131]\n"
    "ds_read_b64_tr_b16 a[196:197], v229 offset:8192\n"
    "v_cvt_pk_bf16_f32 v184, v184, v185\n"
    "v_mfma_f32_16x16x32_bf16 v[128:131], a[140:143], a[12:15], v[128:131]\n"
    "v_exp_f32 v188, v188\n"
    "v_mfma_f32_16x16x32_bf16 v[132:135], a[144:147], a[0:3], v[192:195]\n"
    "v_exp_f32 v189, v189\n"
    "v_mfma_f32_16x16x32_bf16 v[132:135], a[148:151], a[4:7], v[132:135]\n"
    "v_exp_f32 v190, v190\n"
    "v_mfma_f32_16x16x32_bf16 v[132:135], a[152:155], a[8:11], v[132:135]\n"
    "s_waitcnt vmcnt(10)\n"
    "ds_read_b128 a[216:219], v230 offset:14336\n"
    "ds_read_b128 a[220:223], v230 offset:15360\n"
    "v_mfma_f32_16x16x32_bf16 v[132:135], a[156:159], a[12:15], v[132:135]\n"
    "ds_read_b64_tr_b16 a[198:199], v229 offset:10240\n"
    "ds_read_b64_tr_b16 a[204:205], v229 offset:8704\n"
    "ds_read_b64_tr_b16 a[206:207], v229 offset:10752\n"
    "v_mfma_f32_16x16x32_bf16 v[136:139], a[128:131], a[16:19], v[196:199]\n"
    "v_max3_f32 v240, v128, v129, v130\n"
    "v_cvt_pk_bf16_f32 v178, v180, v181\n"
    "v_mfma_f32_16x16x32_bf16 v[136:139], a[132:135], a[20:23], v[136:139]\n"
    "v_exp_f32 v191, v191\n"
    "v_mfma_f32_16x16x32_bf16 v[136:139], a[136:139], a[24:27], v[136:139]\n"
    "v_cvt_pk_bf16_f32 v185, v186, v187\n"
    "v_cvt_pk_bf16_f32 v179, v182, v183\n"
    "v_mfma_f32_16x16x32_bf16 v[136:139], a[140:143], a[28:31], v[136:139]\n"
    "v_max3_f32 v240, v240, v131, v132\n"
    "v_max3_f32 v240, v240, v133, v134\n"
    "v_mfma_f32_16x16x32_bf16 v[140:143], a[144:147], a[16:19], v[196:199]\n"
    "v_cvt_pk_bf16_f32 v186, v188, v189\n"
    "v_cvt_pk_bf16_f32 v187, v190, v191\n"
    "v_mfma_f32_16x16x32_bf16 v[140:143], a[148:151], a[20:23], v[140:143]\n"
    "ds_read_b64_tr_b16 a[160:161], v226 offset:2048\n"
    "ds_read_b64_tr_b16 a[162:163], v226 offset:10240\n"
    "v_mfma_f32_16x16x32_bf16 v[140:143], a[152:155], a[24:27], v[140:143]\n"
    "ds_read_b64_tr_b16 a[164:165], v227 offset:2048\n"
    "s_waitcnt lgkmcnt(3)\n"
    "ds_read_b64_tr_b16 a[166:167], v227 offset:10240\n"
    "ds_read_b64_tr_b16 a[168:169], v226 offset:2560\n"
    "v_mfma_f32_16x16x32_bf16 v[140:143], a[156:159], a[28:31], v[140:143]\n"
    "buffer_load_dwordx4 v233, s[28:31], s88 offen offset:0 lds\n"
    "v_mfma_f32_16x16x32_bf16 a[64:67], a[192:195], a[208:211], a[64:67]\n"
    "v_max3_f32 v240, v240, v135, v136\n"
    "v_max3_f32 v240, v240, v137, v138\n"
    "v_mfma_f32_16x16x32_bf16 a[72:75], a[192:195], a[212:215], a[72:75]\n"
    "ds_read_b64_tr_b16 a[170:171], v226 offset:10752\n"
    "ds_read_b64_tr_b16 a[172:173], v227 offset:2560\n"
    "v_mfma_f32_16x16x32_bf16 a[80:83], a[192:195], a[216:219], a[80:83]\n"
    "buffer_load_dwordx4 v233, s[36:39], s88 offen offset:2048 lds\n"
    "v_mfma_f32_16x16x32_bf16 a[88:91], a[192:195], a[220:223], a[88:91]\n"
    "ds_read_b64_tr_b16 a[174:175], v227 offset:10752\n"
    "ds_read_b64_tr_b16 a[176:177], v226 offset:3072\n"
    "ds_read_b64_tr_b16 a[178:179], v226 offset:11264\n"
    "v_mfma_f32_16x16x32_bf16 a[68:71], a[196:199], a[208:211], a[68:71]\n"
    "v_max3_f32 v240, v240, v139, v140\n"
    "v_max3_f32 v240, v240, v141, v142\n"
    "v_mfma_f32_16x16x32_bf16 a[76:79], a[196:199], a[212:215], a[76:79]\n"
    "buffer_load_dwordx4 v233, s[32:35], s88 offen offset:1024 lds\n"
    "v_mfma_f32_16x16x32_bf16 a[84:87], a[196:199], a[216:219], a[84:87]\n"
    "ds_read_b64_tr_b16 a[180:181], v227 offset:3072\n"
    "ds_read_b64_tr_b16 a[182:183], v227 offset:11264\n"
    "ds_read_b64_tr_b16 a[184:185], v226 offset:3584\n"
    "v_mfma_f32_16x16x32_bf16 a[92:95], a[196:199], a[220:223], a[92:95]\n"
    "ds_read_b64_tr_b16 a[186:187], v226 offset:11776\n"
    "ds_read_b64_tr_b16 a[188:189], v227 offset:3584\n"
    "s_waitcnt lgkmcnt(8)\n"
    "ds_read_b64_tr_b16 a[190:191], v227 offset:11776\n"
    "v_mfma_f32_16x16x32_bf16 v[144:147], a[128:131], a[32:35], v[200:203]\n"
    "buffer_load_dwordx4 v233, s[40:43], s88 offen offset:3072 lds\n"
    "v_mfma_f32_16x16x32_bf16 v[144:147], a[132:135], a[36:39], v[144:147]\n"
    "s_add_u32 m0, s94, 12288\n"
    "s_add_u32 s88, s88, 32768\n"
    "v_mfma_f32_16x16x32_bf16 v[144:147], a[136:139], a[40:43], v[144:147]\n"
    "s_add_u32 s64, s64, 35651584\n"
    "s_addc_u32 s65, s65, 0\n"
    "s_and_b32 s65, s65, 0xffff\n"
    "v_mfma_f32_16x16x32_bf16 v[144:147], a[140:143], a[44:47], v[144:147]\n"
    "buffer_load_dwordx4 v233, s[28:31], s88 offen offset:0 lds\n"
    "v_mfma_f32_16x16x32_bf16 v[148:151], a[144:147], a[32:35], v[200:203]\n"
    "v_mfma_f32_16x16x32_bf16 v[148:151], a[148:151], a[36:39], v[148:151]\n"
    "v_mfma_f32_16x16x32_bf16 v[148:151], a[152:155], a[40:43], v[148:151]\n"
    "buffer_load_dwordx4 v233, s[36:39], s88 offen offset:2048 lds\n"
    "v_mfma_f32_16x16x32_bf16 v[148:151], a[156:159], a[44:47], v[148:151]\n"
    "s_nop 1\n"
    "v_max3_f32 v241, v144, v145, v146\n"
    "v_mfma_f32_16x16x32_bf16 v[152:155], a[128:131], a[48:51], v[204:207]\n"
    "v_mfma_f32_16x16x32_bf16 v[152:155], a[132:135], a[52:55], v[152:155]\n"
    "buffer_load_dwordx4 v233, s[32:35], s88 offen offset:1024 lds\n"
    "v_mfma_f32_16x16x32_bf16 v[152:155], a[136:139], a[56:59], v[152:155]\n"
    "ds_read_b128 a[128:131], v225 offset:16384\n"
    "ds_read_b128 a[132:135], v225 offset:16896\n"
    "v_max3_f32 v241, v241, v147, v148\n"
    "v_max3_f32 v241, v241, v149, v150\n"
    "v_mfma_f32_16x16x32_bf16 v[152:155], a[140:143], a[60:63], v[152:155]\n"
    "ds_read_b128 a[136:139], v225 offset:17408\n"
    "ds_read_b128 a[140:143], v225 offset:17920\n"
    "v_mfma_f32_16x16x32_bf16 v[156:159], a[144:147], a[48:51], v[204:207]\n"
    "buffer_load_dwordx4 v233, s[40:43], s88 offen offset:3072 lds\n"
    "v_mfma_f32_16x16x32_bf16 v[156:159], a[148:151], a[52:55], v[156:159]\n"
    "s_add_u32 m0, s94, 16384\n"
    "ds_read_b128 a[144:147], v225 offset:24576\n"
    "ds_read_b128 a[148:151], v225 offset:25088\n"
    "v_mfma_f32_16x16x32_bf16 v[156:159], a[152:155], a[56:59], v[156:159]\n"
    "s_sub_u32 s88, s88, 32640\n"
    "v_mfma_f32_16x16x32_bf16 v[156:159], a[156:159], a[60:63], v[156:159]\n"
    "v_max3_f32 v241, v241, v151, v152\n"
    "v_max3_f32 v241, v241, v153, v154\n"
    "v_mfma_f32_16x16x32_bf16 v[208:211], v[248:251], v[160:163], v[208:211]\n"
    "buffer_load_dwordx4 v233, s[28:31], s88 offen offset:0 lds\n"
    "v_mfma_f32_16x16x32_bf16 v[212:215], v[248:251], v[168:171], v[212:215]\n"
    "s_waitcnt lgkmcnt(2)\n"
    "ds_read_b128 a[152:155], v225 offset:25600\n"
    "ds_read_b128 a[156:159], v225 offset:26112\n"
    "v_mfma_f32_16x16x32_bf16 v[0:3], a[160:163], v[160:163], v[0:3]\n"
    "v_mov_b32 v225, 0\n"
    "v_mfma_f32_16x16x32_bf16 v[8:11], a[160:163], v[168:171], v[8:11]\n"
    "v_max3_f32 v241, v241, v155, v156\n"
    "v_max3_f32 v241, v241, v157, v158\n"
    "v_max3_f32 v240, v240, v143, v159\n"
    "v_max_f32 v240, v240, v241\n"
    "v_cmp_lt_f32 vcc, 0x41a00000, v240\n"
    "v_mfma_f32_16x16x32_bf16 v[4:7], a[164:167], v[160:163], v[4:7]\n"
    "buffer_load_dwordx4 v233, s[36:39], s88 offen offset:2048 lds\n"
    "v_mfma_f32_16x16x32_bf16 v[12:15], a[164:167], v[168:171], v[12:15]\n"
    "s_cbranch_vccnz K2_S1_14\n"
    "K2_R1_14:\n"
    "v_exp_f32 v128, v128\n"
    "v_mfma_f32_16x16x32_bf16 v[32:35], a[168:171], v[160:163], v[32:35]\n"
    "v_exp_f32 v129, v129\n"
    "v_mfma_f32_16x16x32_bf16 v[40:43], a[168:171], v[168:171], v[40:43]\n"
    "buffer_load_dwordx4 v233, s[32:35], s88 offen offset:1024 lds\n"
    "v_mfma_f32_16x16x32_bf16 v[36:39], a[172:175], v[160:163], v[36:39]\n"
    "v_exp_f32 v136, v136\n"
    "v_mfma_f32_16x16x32_bf16 v[44:47], a[172:175], v[168:171], v[44:47]\n"
    "v_exp_f32 v137, v137\n"
    "v_mfma_f32_16x16x32_bf16 v[64:67], a[176:179], v[160:163], v[64:67]\n"
    "buffer_load_dwordx4 v233, s[40:43], s88 offen offset:3072 lds\n"
    "v_mfma_f32_16x16x32_bf16 v[72:75], a[176:179], v[168:171], v[72:75]\n"
    "s_add_u32 m0, s94, 20480\n"
    "v_exp_f32 v130, v130\n"
    "v_mfma_f32_16x16x32_bf16 v[68:71], a[180:183], v[160:163], v[68:71]\n"
    "s_add_u32 s88, s88, 32768\n"
    "v_exp_f32 v131, v131\n"
    "v_mfma_f32_16x16x32_bf16 v[76:79], a[180:183], v[168:171], v[76:79]\n"
    "buffer_load_dwordx4 v233, s[28:31], s88 offen offset:0 lds\n"
    "v_mfma_f32_16x16x32_bf16 v[96:99], a[184:187], v[160:163], v[96:99]\n"
    "v_exp_f32 v138, v138\n"
    "v_mfma_f32_16x16x32_bf16 v[104:107], a[184:187], v[168:171], v[104:107]\n"
    "v_exp_f32 v139, v139\n"
    "v_mfma_f32_16x16x32_bf16 v[100:103], a[188:191], v[160:163], v[100:103]\n"
    "buffer_load_dwordx4 v233, s[36:39], s88 offen offset:2048 lds\n"
    "v_mfma_f32_16x16x32_bf16 v[108:111], a[188:191], v[168:171], v[108:111]\n"
    "v_exp_f32 v132, v132\n"
    "v_mfma_f32_16x16x32_bf16 a[96:99], a[200:203], a[208:211], a[96:99]\n"
    "v_cvt_pk_bf16_f32 v128, v128, v129\n"
    "v_cvt_pk_bf16_f32 v136, v136, v137\n"
    "v_mfma_f32_16x16x32_bf16 a[104:107], a[200:203], a[212:215], a[104:107]\n"
    "buffer_load_dwordx4 v233, s[32:35], s88 offen offset:1024 lds\n"
    "v_mfma_f32_16x16x32_bf16 a[112:115], a[200:203], a[216:219], a[112:115]\n"
    "v_exp_f32 v133, v133\n"
    "v_mfma_f32_16x16x32_bf16 a[120:123], a[200:203], a[220:223], a[120:123]\n"
    "v_exp_f32 v134, v134\n"
    "v_mfma_f32_16x16x32_bf16 a[100:103], a[204:207], a[208:211], a[100:103]\n"
    "buffer_load_dwordx4 v233, s[40:43], s88 offen offset:3072 lds\n"
    "v_mfma_f32_16x16x32_bf16 a[108:111], a[204:207], a[212:215], a[108:111]\n"
    "s_add_u32 s88, s88, 32640\n"
    "v_exp_f32 v135, v135\n"
    "v_mfma_f32_16x16x32_bf16 a[116:119], a[204:207], a[216:219], a[116:119]\n"
    "s_add_u32 m0, s94, 0\n"
    "v_exp_f32 v140, v140\n"
    "v_mfma_f32_16x16x32_bf16 a[124:127], a[204:207], a[220:223], a[124:127]\n"
    "s_and_b32 s88, s88, 524287\n"
    "v_cvt_pk_bf16_f32 v129, v130, v131\n"
    "v_cvt_pk_bf16_f32 v137, v138, v139\n"
    "v_mfma_f32_16x16x32_bf16 v[216:219], v[248:251], v[176:179], v[216:219]\n"
    "v_exp_f32 v141, v141\n"
    "v_mfma_f32_16x16x32_bf16 v[220:223], v[248:251], v[184:187], v[220:223]\n"
    "v_exp_f32 v142, v142\n"
    "v_mfma_f32_16x16x32_bf16 v[16:19], a[160:163], v[176:179], v[16:19]\n"
    "v_exp_f32 v143, v143\n"
    "v_mfma_f32_16x16x32_bf16 v[24:27], a[160:163], v[184:187], v[24:27]\n"
    "v_cvt_pk_bf16_f32 v130, v132, v133\n"
    "v_cvt_pk_bf16_f32 v131, v134, v135\n"
    "v_mfma_f32_16x16x32_bf16 v[20:23], a[164:167], v[176:179], v[20:23]\n"
    "v_exp_f32 v144, v144\n"
    "v_mfma_f32_16x16x32_bf16 v[28:31], a[164:167], v[184:187], v[28:31]\n"
    "v_cvt_pk_bf16_f32 v138, v140, v141\n"
    "v_cvt_pk_bf16_f32 v139, v142, v143\n"
    "v_mfma_f32_16x16x32_bf16 v[48:51], a[168:171], v[176:179], v[48:51]\n"
    "v_exp_f32 v145, v145\n"
    "v_mfma_f32_16x16x32_bf16 v[56:59], a[168:171], v[184:187], v[56:59]\n"
    "v_exp_f32 v152, v152\n"
    "v_mfma_f32_16x16x32_bf16 v[52:55], a[172:175], v[176:179], v[52:55]\n"
    "v_exp_f32 v153, v153\n"
    "v_mfma_f32_16x16x32_bf16 v[60:63], a[172:175], v[184:187], v[60:63]\n"
    "v_exp_f32 v146, v146\n"
    "v_mfma_f32_16x16x32_bf16 v[80:83], a[176:179], v[176:179], v[80:83]\n"
    "v_exp_f32 v147, v147\n"
    "v_mfma_f32_16x16x32_bf16 v[88:91], a[176:179], v[184:187], v[88:91]\n"
    "v_exp_f32 v154, v154\n"
    "v_mfma_f32_16x16x32_bf16 v[84:87], a[180:183], v[176:179], v[84:87]\n"
    "v_exp_f32 v155, v155\n"
    "v_mfma_f32_16x16x32_bf16 v[92:95], a[180:183], v[184:187], v[92:95]\n"
    "v_exp_f32 v148, v148\n"
    "v_mfma_f32_16x16x32_bf16 v[112:115], a[184:187], v[176:179], v[112:115]\n"
    "s_waitcnt vmcnt(24)\n"
    "ds_read_b64_tr_b16 a[224:225], v228 offset:0\n"
    "s_waitcnt vmcnt(22)\n"
    "ds_read_b64_tr_b16 a[226:227], v228 offset:2048\n"
    "v_mfma_f32_16x16x32_bf16 v[120:123], a[184:187], v[184:187], v[120:123]\n"
    "s_waitcnt vmcnt(19)\n"
    "ds_read_b128 a[240:243], v230 offset:4096\n"
    "ds_read_b128 a[244:247], v230 offset:5120\n"
    "ds_read_b64_tr_b16 a[160:161], v226 offset:18432\n"
    "v_mfma_f32_16x16x32_bf16 v[116:119], a[188:191], v[176:179], v[116:119]\n"
    "ds_read_b64_tr_b16 a[232:233], v228 offset:512\n"
    "ds_read_b64_tr_b16 a[234:235], v228 offset:2560\n"
    "v_mfma_f32_16x16x32_bf16 v[124:127], a[188:191], v[184:187], v[124:127]\n"
    "s_waitcnt vmcnt(18)\n"
    "ds_read_b128 a[248:251], v230 offset:6144\n"
    "v_cvt_pk_bf16_f32 v144, v144, v145\n"
    "s_cbranch_vccnz K2_S2_14\n"
    "K2_R2_14:\n"
    "v_mfma_f32_16x16x32_bf16 v[160:163], a[128:131], a[0:3], v[192:195]\n"
    "ds_read_b64_tr_b16 a[228:229], v229 offset:0\n"
    "ds_read_b64_tr_b16 a[230:231], v229 offset:2048\n"
    "v_mfma_f32_16x16x32_bf16 v[160:163], a[132:135], a[4:7], v[160:163]\n"
    "ds_read_b128 a[252:255], v230 offset:7168\n"
    "v_cvt_pk_bf16_f32 v152, v152, v153\n"
    "v_mfma_f32_16x16x32_bf16 v[160:163], a[136:139], a[8:11], v[160:163]\n"
    "s_waitcnt lgkmcnt(9)\n"
    "ds_read_b64_tr_b16 a[236:237], v229 offset:512\n"
    "ds_read_b64_tr_b16 a[238:239], v229 offset:2560\n"
    "v_mfma_f32_16x16x32_bf16 v[160:163], a[140:143], a[12:15], v[160:163]\n"
    "v_exp_f32 v149, v149\n"
    "v_mfma_f32_16x16x32_bf16 v[164:167], a[144:147], a[0:3], v[192:195]\n"
    "v_exp_f32 v150, v150\n"
    "v_mfma_f32_16x16x32_bf16 v[164:167], a[148:151], a[4:7], v[164:167]\n"
    "v_exp_f32 v151, v151\n"
    "v_mfma_f32_16x16x32_bf16 v[164:167], a[152:155], a[8:11], v[164:167]\n"
    "v_exp_f32 v156, v156\n"
    "v_mfma_f32_16x16x32_bf16 v[164:167], a[156:159], a[12:15], v[164:167]\n"
    "v_max3_f32 v240, v160, v161, v162\n"
    "v_cvt_pk_bf16_f32 v145, v146, v147\n"
    "v_mfma_f32_16x16x32_bf16 v[168:171], a[128:131], a[16:19], v[196:199]\n"
    "v_exp_f32 v157, v157\n"
    "v_mfma_f32_16x16x32_bf16 v[168:171], a[132:135], a[20:23], v[168:171]\n"
    "v_exp_f32 v158, v158\n"
    "s_waitcnt lgkmcnt(0)\n"
    "buffer_load_dwordx4 v233, s[28:31], s88 offen offset:0 lds\n"
    "v_mfma_f32_16x16x32_bf16 v[168:171], a[136:139], a[24:27], v[168:171]\n"
    "v_max3_f32 v240, v240, v163, v164\n"
    "buffer_load_dwordx4 v233, s[36:39], s88 offen offset:2048 lds\n"
    "v_mfma_f32_16x16x32_bf16 v[168:171], a[140:143], a[28:31], v[168:171]\n"
    "v_max3_f32 v240, v240, v165, v166\n"
    "buffer_load_dwordx4 v233, s[32:35], s88 offen offset:1024 lds\n"
    "v_mfma_f32_16x16x32_bf16 v[172:175], a[144:147], a[16:19], v[196:199]\n"
    "ds_read_b64_tr_b16 a[162:163], v226 offset:26624\n"
    "ds_read_b64_tr_b16 a[164:165], v227 offset:18432\n"
    "ds_read_b64_tr_b16 a[166:167], v227 offset:26624\n"
    "v_mfma_f32_16x16x32_bf16 v[172:175], a[148:151], a[20:23], v[172:175]\n"
    "buffer_load_dwordx4 v233, s[40:43], s88 offen offset:3072 lds\n"
    "s_add_u32 m0, s94, 4096\n"
    "s_add_u32 s88, s88, 32768\n"
    "v_mfma_f32_16x16x32_bf16 v[172:175], a[152:155], a[24:27], v[172:175]\n"
    "v_max3_f32 v240, v240, v167, v168\n"
    "buffer_load_dwordx4 v233, s[28:31], s88 offen offset:0 lds\n"
    "v_mfma_f32_16x16x32_bf16 v[172:175], a[156:159], a[28:31], v[172:175]\n"
    "v_max3_f32 v240, v240, v169, v170\n"
    "buffer_load_dwordx4 v233, s[36:39], s88 offen offset:2048 lds\n"
    "v_mfma_f32_16x16x32_bf16 a[64:67], a[224:227], a[240:243], a[64:67]\n"
    "ds_read_b64_tr_b16 a[168:169], v226 offset:18944\n"
    "ds_read_b64_tr_b16 a[170:171], v226 offset:27136\n"
    "ds_read_b64_tr_b16 a[172:173], v227 offset:18944\n"
    "v_mfma_f32_16x16x32_bf16 a[72:75], a[224:227], a[244:247], a[72:75]\n"
    "ds_read_b64_tr_b16 a[174:175], v227 offset:27136\n"
    "ds_read_b64_tr_b16 a[176:177], v226 offset:19456\n"
    "ds_read_b64_tr_b16 a[178:179], v226 offset:27648\n"
    "v_mfma_f32_16x16x32_bf16 a[80:83], a[224:227], a[248:251], a[80:83]\n"
    "buffer_load_dwordx4 v233, s[32:35], s88 offen offset:1024 lds\n"
    "v_mfma_f32_16x16x32_bf16 a[88:91], a[224:227], a[252:255], a[88:91]\n"
    "ds_read_b64_tr_b16 a[180:181], v227 offset:19456\n"
    "ds_read_b64_tr_b16 a[182:183], v227 offset:27648\n"
    "ds_read_b64_tr_b16 a[184:185], v226 offset:19968\n"
    "v_mfma_f32_16x16x32_bf16 a[68:71], a[228:231], a[240:243], a[68:71]\n"
    "buffer_load_dwordx4 v233, s[40:43], s88 offen offset:3072 lds\n"
    "v_mfma_f32_16x16x32_bf16 a[76:79], a[228:231], a[244:247], a[76:79]\n"
    "s_sub_u32 s88, s88, 32640\n"
    "ds_read_b64_tr_b16 a[186:187], v226 offset:28160\n"
    "ds_read_b64_tr_b16 a[188:189], v227 offset:19968\n"
    "v_mfma_f32_16x16x32_bf16 a[84:87], a[228:231], a[248:251], a[84:87]\n"
    "s_add_u32 m0, s94, 8192\n"
    "v_max3_f32 v240, v240, v171, v172\n"
    "v_max3_f32 v240, v240, v173, v174\n"
    "v_mfma_f32_16x16x32_bf16 a[92:95], a[228:231], a[252:255], a[92:95]\n"
    "v_exp_f32 v159, v159\n"
    "v_mfma_f32_16x16x32_bf16 v[176:179], a[128:131], a[32:35], v[200:203]\n"
    "v_cvt_pk_bf16_f32 v153, v154, v155\n"
    "v_cvt_pk_bf16_f32 v146, v148, v149\n"
    "v_mfma_f32_16x16x32_bf16 v[176:179], a[132:135], a[36:39], v[176:179]\n"
    "ds_read_b64_tr_b16 a[190:191], v227 offset:28160\n"
    "v_mfma_f32_16x16x32_bf16 v[176:179], a[136:139], a[40:43], v[176:179]\n"
    "v_cvt_pk_bf16_f32 v147, v150, v151\n"
    "v_cvt_pk_bf16_f32 v154, v156, v157\n"
    "v_mfma_f32_16x16x32_bf16 v[176:179], a[140:143], a[44:47], v[176:179]\n"
    "v_cvt_pk_bf16_f32 v155, v158, v159\n"
    "v_xor_b32 v226, 0x8000, v226\n"
    "v_mfma_f32_16x16x32_bf16 v[180:183], a[144:147], a[32:35], v[200:203]\n"
    "v_xor_b32 v227, 0x8000, v227\n"
    "v_mfma_f32_16x16x32_bf16 v[180:183], a[148:151], a[36:39], v[180:183]\n"
    "v_mfma_f32_16x16x32_bf16 v[180:183], a[152:155], a[40:43], v[180:183]\n"
    "v_mfma_f32_16x16x32_bf16 v[180:183], a[156:159], a[44:47], v[180:183]\n"
    "s_nop 0\n"
    "v_max3_f32 v241, v176, v177, v178\n"
    "v_mfma_f32_16x16x32_bf16 v[184:187], a[128:131], a[48:51], v[204:207]\n"
    "v_mfma_f32_16x16x32_bf16 v[184:187], a[132:135], a[52:55], v[184:187]\n"
    "v_mfma_f32_16x16x32_bf16 v[184:187], a[136:139], a[56:59], v[184:187]\n"
    "v_mfma_f32_16x16x32_bf16 v[184:187], a[140:143], a[60:63], v[184:187]\n"
    "s_nop 1\n"
    "v_max3_f32 v241, v241, v179, v180\n"
    "v_max3_f32 v241, v241, v181, v182\n"
    "v_mfma_f32_16x16x32_bf16 v[188:191], a[144:147], a[48:51], v[204:207]\n"
    "v_mfma_f32_16x16x32_bf16 v[188:191], a[148:151], a[52:55], v[188:191]\n"
    "v_mfma_f32_16x16x32_bf16 v[188:191], a[152:155], a[56:59], v[188:191]\n"
    "v_mfma_f32_16x16x32_bf16 v[188:191], a[156:159], a[60:63], v[188:191]\n"
    "v_max3_f32 v241, v241, v183, v184\n"
    "v_max3_f32 v241, v241, v185, v186\n"
    "v_mfma_f32_16x16x32_bf16 v[208:211], v[248:251], v[128:131], v[208:211]\n"
    "v_mfma_f32_16x16x32_bf16 v[212:215], v[248:251], v[136:139], v[212:215]\n"
    "s_waitcnt lgkmcnt(0)\n"
    "v_mfma_f32_16x16x32_bf16 v[0:3], a[160:163], v[128:131], v[0:3]\n"
    "v_mfma_f32_16x16x32_bf16 v[8:11], a[160:163], v[136:139], v[8:11]\n"
    "s_nop 0\n"
    "v_max3_f32 v241, v241, v187, v188\n"
    "v_max3_f32 v241, v241, v189, v190\n"
    "v_max3_f32 v240, v240, v175, v191\n"
    "v_max_f32 v240, v240, v241\n"
    "v_cmp_lt_f32 vcc, 0x41a00000, v240\n"
    "s_waitcnt vmcnt(24)\n"
    "s_barrier\n"
    "v_mfma_f32_16x16x32_bf16 v[4:7], a[164:167], v[128:131], v[4:7]\n"
    "v_mfma_f32_16x16x32_bf16 v[12:15], a[164:167], v[136:139], v[12:15]\n"
    "s_cbranch_vccnz K2_S1_15\n"
    "K2_R1_15:\n"
    "v_exp_f32 v160, v160\n"
    "v_exp_f32 v161, v161\n"
    "v_exp_f32 v176, v176\n"
    "v_exp_f32 v177, v177\n"
    "v_exp_f32 v163, v163\n"
    "v_exp_f32 v166, v166\n"
    "v_mfma_f32_16x16x32_bf16 v[32:35], a[168:171], v[128:131], v[32:35]\n"
    "v_cvt_pk_bf16_f32 v160, v160, v161\n"
    "v_exp_f32 v167, v167\n"
    "v_cvt_pk_bf16_f32 v176, v176, v177\n"
    "v_exp_f32 v185, v185\n"
    "v_mfma_f32_16x16x32_bf16 v[40:43], a[168:171], v[136:139], v[40:43]\n"
    "v_exp_f32 v170, v170\n"
    "v_exp_f32 v172, v172\n"
    "v_exp_f32 v184, v184\n"
    "v_mfma_f32_16x16x32_bf16 v[36:39], a[172:175], v[128:131], v[36:39]\n"
    "v_exp_f32 v168, v168\n"
    "v_exp_f32 v173, v173\n"
    "v_exp_f32 v186, v186\n"
    "v_mfma_f32_16x16x32_bf16 v[44:47], a[172:175], v[136:139], v[44:47]\n"
    "v_exp_f32 v169, v169\n"
    "v_exp_f32 v174, v174\n"
    "v_exp_f32 v187, v187\n"
    "v_mfma_f32_16x16x32_bf16 v[64:67], a[176:179], v[128:131], v[64:67]\n"
    "v_exp_f32 v171, v171\n"
    "v_exp_f32 v175, v175\n"
    "v_cvt_pk_bf16_f32 v184, v184, v185\n"
    "v_mfma_f32_16x16x32_bf16 v[72:75], a[176:179], v[136:139], v[72:75]\n"
    "v_cvt_pk_bf16_f32 v168, v168, v169\n"
    "v_exp_f32 v180, v180\n"
    "v_mfma_f32_16x16x32_bf16 v[68:71], a[180:183], v[128:131], v[68:71]\n"
    "v_exp_f32 v178, v178\n"
    "v_exp_f32 v181, v181\n"
    "v_mfma_f32_16x16x32_bf16 v[76:79], a[180:183], v[136:139], v[76:79]\n"
    "v_exp_f32 v162, v162\n"
    "v_exp_f32 v164, v164\n"
    "v_exp_f32 v182, v182\n"
    "v_mfma_f32_16x16x32_bf16 v[96:99], a[184:187], v[128:131], v[96:99]\n"
    "v_exp_f32 v165, v165\n"
    "v_exp_f32 v183, v183\n"
    "v_mfma_f32_16x16x32_bf16 v[104:107], a[184:187], v[136:139], v[104:107]\n"
    "v_cvt_pk_bf16_f32 v161, v162, v163\n"
    "v_exp_f32 v179, v179\n"
    "v_exp_f32 v188, v188\n"
    "v_mfma_f32_16x16x32_bf16 v[100:103], a[188:191], v[128:131], v[100:103]\n"
    "v_cvt_pk_bf16_f32 v169, v170, v171\n"
    "v_exp_f32 v189, v189\n"
    "v_mfma_f32_16x16x32_bf16 v[108:111], a[188:191], v[136:139], v[108:111]\n"
    "v_cvt_pk_bf16_f32 v177, v178, v179\n"
    "v_exp_f32 v190, v190\n"
    "v_mfma_f32_16x16x32_bf16 a[96:99], a[232:235], a[240:243], a[96:99]\n"
    "v_exp_f32 v191, v191\n"
    "v_mfma_f32_16x16x32_bf16 a[104:107], a[232:235], a[244:247], a[104:107]\n"
    "v_cvt_pk_bf16_f32 v185, v186, v187\n"
    "v_cvt_pk_bf16_f32 v162, v164, v165\n"
    "v_mfma_f32_16x16x32_bf16 a[112:115], a[232:235], a[248:251], a[112:115]\n"
    "v_cvt_pk_bf16_f32 v163, v166, v167\n"
    "v_cvt_pk_bf16_f32 v170, v172, v173\n"
    "v_mfma_f32_16x16x32_bf16 a[120:123], a[232:235], a[252:255], a[120:123]\n"
    "v_cvt_pk_bf16_f32 v171, v174, v175\n"
    "v_cvt_pk_bf16_f32 v178, v180, v181\n"
    "v_mfma_f32_16x16x32_bf16 a[100:103], a[236:239], a[240:243], a[100:103]\n"
    "v_cvt_pk_bf16_f32 v179, v182, v183\n"
    "v_cvt_pk_bf16_f32 v186, v188, v189\n"
    "v_mfma_f32_16x16x32_bf16 a[108:111], a[236:239], a[244:247], a[108:111]\n"
    "v_cvt_pk_bf16_f32 v187, v190, v191\n"
    "v_mfma_f32_16x16x32_bf16 a[116:119], a[236:239], a[248:251], a[116:119]\n"
    "v_mfma_f32_16x16x32_bf16 a[124:127], a[236:239], a[252:255], a[124:127]\n"
    "v_mfma_f32_16x16x32_bf16 v[216:219], v[248:251], v[144:147], v[216:219]\n"
    "v_mfma_f32_16x16x32_bf16 v[220:223], v[248:251], v[152:155], v[220:223]\n"
    "v_mfma_f32_16x16x32_bf16 v[16:19], a[160:163], v[144:147], v[16:19]\n"
    "v_mfma_f32_16x16x32_bf16 v[24:27], a[160:163], v[152:155], v[24:27]\n"
    "ds_write_b128 v252, a[80:83] offset:16384\n"
    "v_mfma_f32_16x16x32_bf16 v[20:23], a[164:167], v[144:147], v[20:23]\n"
    "ds_write_b128 v252, a[84:87] offset:17408\n"
    "v_mfma_f32_16x16x32_bf16 v[28:31], a[164:167], v[152:155], v[28:31]\n"
    "v_mfma_f32_16x16x32_bf16 v[48:51], a[168:171], v[144:147], v[48:51]\n"
    "ds_write_b128 v252, a[88:91] offset:18432\n"
    "v_mfma_f32_16x16x32_bf16 v[56:59], a[168:171], v[152:155], v[56:59]\n"
    "ds_write_b128 v252, a[92:95] offset:19456\n"
    "v_mfma_f32_16x16x32_bf16 v[52:55], a[172:175], v[144:147], v[52:55]\n"
    "ds_write_b128 v252, a[96:99] offset:32768\n"
    "v_mfma_f32_16x16x32_bf16 v[60:63], a[172:175], v[152:155], v[60:63]\n"
    "ds_read_b64_tr_b16 a[160:161], v226 offset:2048\n"
    "ds_write_b128 v252, a[100:103] offset:33792\n"
    "v_mfma_f32_16x16x32_bf16 v[80:83], a[176:179], v[144:147], v[80:83]\n"
    "ds_read_b64_tr_b16 a[162:163], v226 offset:10240\n"
    "ds_write_b128 v252, a[104:107] offset:34816\n"
    "v_mfma_f32_16x16x32_bf16 v[88:91], a[176:179], v[152:155], v[88:91]\n"
    "ds_write_b128 v252, a[108:111] offset:35840\n"
    "v_mfma_f32_16x16x32_bf16 v[84:87], a[180:183], v[144:147], v[84:87]\n"
    "ds_write_b128 v252, a[112:115] offset:49152\n"
    "v_mfma_f32_16x16x32_bf16 v[92:95], a[180:183], v[152:155], v[92:95]\n"
    "ds_read_b64_tr_b16 a[164:165], v227 offset:2048\n"
    "ds_write_b128 v252, a[116:119] offset:50176\n"
    "v_mfma_f32_16x16x32_bf16 v[112:115], a[184:187], v[144:147], v[112:115]\n"
    "ds_read_b64_tr_b16 a[166:167], v227 offset:10240\n"
    "ds_write_b128 v252, a[120:123] offset:51200\n"
    "v_mfma_f32_16x16x32_bf16 v[120:123], a[184:187], v[152:155], v[120:123]\n"
    "s_waitcnt lgkmcnt(8)\n"
    "ds_write_b128 v252, a[124:127] offset:52224\n"
    "v_mfma_f32_16x16x32_bf16 v[116:119], a[188:191], v[144:147], v[116:119]\n"
    "ds_read_b64_tr_b16 a[172:173], v227 offset:2560\n"
    "v_mov_b32 v252, 0xc0135761\n"
    "v_mfma_f32_16x16x32_bf16 v[124:127], a[188:191], v[152:155], v[124:127]\n"
    "ds_read_b64_tr_b16 a[168:169], v226 offset:2560\n"
    "ds_read_b64_tr_b16 a[174:175], v227 offset:10752\n"
    "s_cbranch_vccnz K2_S2_15\n"
    "K2_R2_15:\n"
    "v_add_u32 v236, s22, v224\n"
    "s_add_u32 s22, %1, 1\n"
    "s_and_b32 s22, s22, 3\n"
    "s_lshl_b32 s22, s22, 14\n"
    "v_add_u32 v237, s22, v224\n"
    "s_add_u32 s22, %1, 2\n"
    "s_and_b32 s22, s22, 3\n"
    "s_lshl_b32 s22, s22, 14\n"
    "v_add_u32 v238, s22, v224\n"
    "s_add_u32 s22, %1, 3\n"
    "s_and_b32 s22, s22, 3\n"
    "s_lshl_b32 s22, s22, 14\n"
    "v_add_u32 v239, s22, v224\n"
    "v_mfma_f32_16x16x32_bf16 v[208:211], v[248:251], v[160:163], v[208:211]\n"
    "s_lshr_b32 s22, %1, 1\n"
    "ds_read_b64_tr_b16 a[170:171], v226 offset:10752\n"
    "ds_read_b64_tr_b16 a[176:177], v226 offset:3072\n"
    "ds_read_b64_tr_b16 a[178:179], v226 offset:11264\n"
    "s_waitcnt lgkmcnt(10)\n"
    "ds_read_b64_tr_b16 a[180:181], v227 offset:3072\n"
    "v_mfma_f32_16x16x32_bf16 v[212:215], v[248:251], v[168:171], v[212:215]\n"
    "s_add_u32 s84, s84, s22\n"
    "ds_read_b64_tr_b16 a[182:183], v227 offset:11264\n"
    "ds_read_b64_tr_b16 a[184:185], v226 offset:3584\n"
    "ds_read_b64_tr_b16 a[186:187], v226 offset:11776\n"
    "v_mfma_f32_16x16x32_bf16 v[216:219], v[248:251], v[176:179], v[216:219]\n"
    "s_lshr_b32 s22, s93, 1\n"
    "ds_read_b64_tr_b16 a[188:189], v227 offset:3584\n"
    "s_waitcnt lgkmcnt(10)\n"
    "ds_read_b64_tr_b16 a[190:191], v227 offset:11776\n"
    "v_mfma_f32_16x16x32_bf16 v[220:223], v[248:251], v[184:187], v[220:223]\n"
    "s_lshl_b32 s22, s22, 13\n"
    "v_rcp_f32 v240, v208\n"
    "v_mfma_f32_16x16x32_bf16 v[0:3], a[160:163], v[160:163], v[0:3]\n"
    "s_lshl_b32 s84, s84, 17\n"
    "v_mov_b32 v226, 1\n"
    "s_add_u32 s22, s22, s23\n"
    "v_mfma_f32_16x16x32_bf16 v[8:11], a[160:163], v[168:171], v[8:11]\n"
    "s_add_u32 s84, s84, s22\n"
    "v_rcp_f32 v241, v212\n"
    "v_rcp_f32 v242, v216\n"
    "v_mfma_f32_16x16x32_bf16 v[16:19], a[160:163], v[176:179], v[16:19]\n"
    "s_and_b32 s22, %1, 1\n"
    "v_rcp_f32 v243, v220\n"
    "v_mfma_f32_16x16x32_bf16 v[24:27], a[160:163], v[184:187], v[24:27]\n"
    "s_lshl_b32 s22, s22, 12\n"
    "v_mul_f32 v1, v240, v1\n"
    "v_mul_f32 v2, v240, v2\n"
    "v_mul_f32 v3, v240, v3\n"
    "v_mfma_f32_16x16x32_bf16 v[4:7], a[164:167], v[160:163], v[4:7]\n"
    "s_add_u32 s84, s84, s22\n"
    "v_mul_f32 v8, v241, v8\n"
    "v_mul_f32 v9, v241, v9\n"
    "v_mfma_f32_16x16x32_bf16 v[12:15], a[164:167], v[168:171], v[12:15]\n"
    "s_and_b32 s22, s85, 2048\n"
    "v_mul_f32 v10, v241, v10\n"
    "v_mul_f32 v11, v241, v11\n"
    "v_mul_f32 v16, v242, v16\n"
    "v_mfma_f32_16x16x32_bf16 v[20:23], a[164:167], v[176:179], v[20:23]\n"
    "s_mov_b32 s74, s22\n"
    "v_mul_f32 v0, v240, v0\n"
    "v_mul_f32 v17, v242, v17\n"
    "v_mul_f32 v18, v242, v18\n"
    "v_mul_f32 v19, v242, v19\n"
    "v_mfma_f32_16x16x32_bf16 v[28:31], a[164:167], v[184:187], v[28:31]\n"
    "s_and_b32 s22, s96, 1\n"
    "v_mul_f32 v4, v240, v4\n"
    "v_mul_f32 v6, v240, v6\n"
    "v_mul_f32 v7, v240, v7\n"
    "v_mul_f32 v24, v243, v24\n"
    "v_mul_f32 v25, v243, v25\n"
    "v_mul_f32 v26, v243, v26\n"
    "s_waitcnt lgkmcnt(0)\n"
    "v_mfma_f32_16x16x32_bf16 v[32:35], a[168:171], v[160:163], v[32:35]\n"
    "s_lshl_b32 s22, s22, 2\n"
    "v_mul_f32 v5, v240, v5\n"
    "v_mul_f32 v14, v241, v14\n"
    "v_mul_f32 v15, v241, v15\n"
    "v_mul_f32 v27, v243, v27\n"
    "v_mfma_f32_16x16x32_bf16 v[40:43], a[168:171], v[168:171], v[40:43]\n"
    "s_add_u32 s95, s95, s22\n"
    "v_cvt_pk_bf16_f32 v128, v0, v1\n"
    "v_cvt_pk_bf16_f32 v129, v2, v3\n"
    "v_mul_f32 v12, v241, v12\n"
    "v_mul_f32 v21, v242, v21\n"
    "v_mul_f32 v23, v242, v23\n"
    "v_mfma_f32_16x16x32_bf16 v[48:51], a[168:171], v[176:179], v[48:51]\n"
    "s_lshr_b32 s22, s97, 2\n"
    "v_cvt_pk_bf16_f32 v130, v4, v5\n"
    "v_cvt_pk_bf16_f32 v131, v6, v7\n"
    "v_cvt_pk_bf16_f32 v132, v8, v9\n"
    "v_mul_f32 v13, v241, v13\n"
    "v_mul_f32 v22, v242, v22\n"
    "v_mul_f32 v28, v243, v28\n"
    "v_mfma_f32_16x16x32_bf16 v[56:59], a[168:171], v[184:187], v[56:59]\n"
    "s_add_u32 s90, s90, s22\n"
    "v_cvt_pk_bf16_f32 v133, v10, v11\n"
    "v_cvt_pk_bf16_f32 v134, v12, v13\n"
    "v_mul_f32 v20, v242, v20\n"
    "v_mul_f32 v29, v243, v29\n"
    "v_mul_f32 v33, v240, v33\n"
    "v_mul_f32 v40, v241, v40\n"
    "v_mul_f32 v41, v241, v41\n"
    "v_mul_f32 v42, v241, v42\n"
    "v_mul_f32 v43, v241, v43\n"
    "s_barrier\n"
    "ds_read_b128 v[192:195], v253 offset:16384\n"
    "ds_read_b128 v[208:211], v254 offset:32768\n"
    "ds_read_b128 v[200:203], v253 offset:18432\n"
    "ds_read_b128 v[204:207], v253 offset:19456\n"
    "ds_read_b128 v[216:219], v254 offset:34816\n"
    "ds_read_b128 v[220:223], v254 offset:35840\n"
    "ds_read_b128 v[196:199], v253 offset:17408\n"
    "ds_read_b128 v[212:215], v254 offset:33792\n"
    "ds_read_b128 a[192:195], v231 offset:49152\n"
    "ds_read_b128 a[200:203], v231 offset:51200\n"
    "ds_read_b128 a[204:207], v231 offset:52224\n"
    "ds_read_b128 a[196:199], v231 offset:50176\n"
    "v_mfma_f32_16x16x32_bf16 v[36:39], a[172:175], v[160:163], v[36:39]\n"
    "s_lshl_b32 s20, s95, 7\n"
    "s_waitcnt vmcnt(21)\n"
    "ds_read_b64_tr_b16 a[128:129], v228 offset:8192\n"
    "s_waitcnt vmcnt(20)\n"
    "ds_read_b64_tr_b16 a[130:131], v228 offset:10240\n"
    "v_cvt_pk_bf16_f32 v135, v14, v15\n"
    "v_mul_f32 v30, v243, v30\n"
    "v_mul_f32 v32, v240, v32\n"
    "v_mul_f32 v34, v240, v34\n"
    "v_cvt_pk_bf16_f32 v148, v40, v41\n"
    "v_cvt_pk_bf16_f32 v149, v42, v43\n"
    "v_mfma_f32_16x16x32_bf16 v[44:47], a[172:175], v[168:171], v[44:47]\n"
    "s_lshl_b32 s22, %1, 5\n"
    "ds_read_b64_tr_b16 a[132:133], v229 offset:8192\n"
    "s_waitcnt lgkmcnt(10)\n"
    "ds_read_b64_tr_b16 a[134:135], v229 offset:10240\n"
    "s_waitcnt vmcnt(17)\n"
    "ds_read_b64_tr_b16 a[144:145], v228 offset:12288\n"
    "s_waitcnt vmcnt(16)\n"
    "ds_read_b64_tr_b16 a[146:147], v228 offset:14336\n"
    "v_cvt_pk_bf16_f32 v136, v16, v17\n"
    "v_cvt_pk_bf16_f32 v137, v18, v19\n"
    "v_cvt_pk_bf16_f32 v138, v20, v21\n"
    "v_mul_f32 v31, v243, v31\n"
    "v_mul_f32 v35, v240, v35\n"
    "v_mul_f32 v48, v242, v48\n"
    "v_mul_f32 v50, v242, v50\n"
    "v_mfma_f32_16x16x32_bf16 v[52:55], a[172:175], v[176:179], v[52:55]\n"
    "s_mul_i32 s23, %1, 1835008\n"
    "ds_read_b64_tr_b16 a[136:137], v228 offset:8704\n"
    "ds_read_b64_tr_b16 a[138:139], v228 offset:10752\n"
    "s_waitcnt lgkmcnt(10)\n"
    "ds_read_b64_tr_b16 a[148:149], v229 offset:12288\n"
    "ds_read_b64_tr_b16 a[150:151], v229 offset:14336\n"
    "v_cvt_pk_bf16_f32 v139, v22, v23\n"
    "v_mul_f32 v49, v242, v49\n"
    "v_mul_f32 v51, v242, v51\n"
    "v_mul_f32 v56, v243, v56\n"
    "v_mul_f32 v58, v243, v58\n"
    "v_mfma_f32_16x16x32_bf16 v[60:63], a[172:175], v[184:187], v[60:63]\n"
    "v_mul_f32 v36, v240, v36\n"
    "v_mul_f32 v38, v240, v38\n"
    "v_cvt_pk_bf16_f32 v152, v48, v49\n"
    "v_mul_f32 v57, v243, v57\n"
    "v_mul_f32 v59, v243, v59\n"
    "v_add_f32 v192, v192, v208\n"
    "ds_read_b64_tr_b16 a[140:141], v229 offset:8704\n"
    "ds_read_b64_tr_b16 a[142:143], v229 offset:10752\n"
    "ds_read_b64_tr_b16 a[152:153], v228 offset:12800\n"
    "s_waitcnt lgkmcnt(10)\n"
    "ds_read_b64_tr_b16 a[154:155], v228 offset:14848\n"
    "v_mfma_f32_16x16x32_bf16 v[64:67], a[176:179], v[160:163], v[64:67]\n"
    "ds_read_b64_tr_b16 a[156:157], v229 offset:12800\n"
    "ds_read_b64_tr_b16 a[158:159], v229 offset:14848\n"
    "v_cvt_pk_bf16_f32 v144, v32, v33\n"
    "v_mul_f32 v37, v240, v37\n"
    "v_mul_f32 v39, v240, v39\n"
    "v_mul_f32 v47, v241, v47\n"
    "v_cvt_pk_bf16_f32 v153, v50, v51\n"
    "v_accvgpr_read_b32 v208, a64\n"
    "v_add_f32 v200, v200, v216\n"
    "v_add_f32 v204, v204, v220\n"
    "v_mfma_f32_16x16x32_bf16 v[72:75], a[176:179], v[168:171], v[72:75]\n"
    "v_cvt_pk_bf16_f32 v140, v24, v25\n"
    "v_cvt_pk_bf16_f32 v145, v34, v35\n"
    "v_mul_f32 v44, v241, v44\n"
    "v_mul_f32 v52, v242, v52\n"
    "v_add_f32 v192, v192, v208\n"
    "v_add_f32 v193, v193, v209\n"
    "v_add_f32 v195, v195, v211\n"
    "v_add_f32 v197, v197, v213\n"
    "v_accvgpr_read_b32 v216, a72\n"
    "v_add_f32 v202, v202, v218\n"
    "s_waitcnt lgkmcnt(5)\n"
    "buffer_load_dwordx4 v233, s[28:31], s88 offen offset:0 lds\n"
    "v_accvgpr_read_b32 v220, a76\n"
    "v_add_f32 v207, v207, v223\n"
    "buffer_load_dwordx4 v233, s[32:35], s88 offen offset:1024 lds\n"
    "v_mfma_f32_16x16x32_bf16 v[80:83], a[176:179], v[176:179], v[80:83]\n"
    "v_cvt_pk_bf16_f32 v141, v26, v27\n"
    "s_waitcnt lgkmcnt(4)\n"
    "buffer_load_dwordx4 v233, s[36:39], s88 offen offset:2048 lds\n"
    "v_cvt_pk_bf16_f32 v142, v28, v29\n"
    "v_cvt_pk_bf16_f32 v143, v30, v31\n"
    "buffer_load_dwordx4 v233, s[40:43], s88 offen offset:3072 lds\n"
    "s_add_u32 m0, s94, 12288\n"
    "s_add_u32 s88, s88, 32768\n"
    "v_cvt_pk_bf16_f32 v146, v36, v37\n"
    "s_waitcnt lgkmcnt(1)\n"
    "buffer_load_dwordx4 v233, s[28:31], s88 offen offset:0 lds\n"
    "v_mul_f32 v45, v241, v45\n"
    "v_mul_f32 v53, v242, v53\n"
    "s_waitcnt lgkmcnt(0)\n"
    "buffer_load_dwordx4 v233, s[36:39], s88 offen offset:2048 lds\n"
    "v_mul_f32 v60, v243, v60\n"
    "v_mul_f32 v61, v243, v61\n"
    "buffer_load_dwordx4 v233, s[32:35], s88 offen offset:1024 lds\n"
    "v_mul_f32 v62, v243, v62\n"
    "v_mul_f32 v63, v243, v63\n"
    "buffer_load_dwordx4 v233, s[40:43], s88 offen offset:3072 lds\n"
    "s_add_u32 s88, s88, 32640\n"
    "s_add_u32 m0, s94, 16384\n"
    "s_and_b32 s88, s88, 524287\n"
    "v_accvgpr_read_b32 v208, a192\n"
    "v_accvgpr_read_b32 v209, a65\n"
    "v_add_f32 v194, v194, v210\n"
    "v_accvgpr_read_b32 v211, a67\n"
    "v_add_f32 v196, v196, v212\n"
    "v_accvgpr_read_b32 v213, a69\n"
    "v_add_f32 v198, v198, v214\n"
    "v_add_f32 v200, v200, v216\n"
    "v_accvgpr_read_b32 v218, a74\n"
    "v_add_f32 v204, v204, v220\n"
    "v_accvgpr_read_b32 v223, a79\n"
    "v_mfma_f32_16x16x32_bf16 v[88:91], a[176:179], v[184:187], v[88:91]\n"
    "v_cvt_pk_bf16_f32 v147, v38, v39\n"
    "v_mul_f32 v46, v241, v46\n"
    "v_mul_f32 v54, v242, v54\n"
    "v_cvt_pk_bf16_f32 v156, v56, v57\n"
    "v_cvt_pk_bf16_f32 v157, v58, v59\n"
    "v_cvt_pk_bf16_f32 v158, v60, v61\n"
    "v_add_f32 v192, v192, v208\n"
    "v_add_f32 v193, v193, v209\n"
    "v_accvgpr_read_b32 v210, a66\n"
    "v_add_f32 v195, v195, v211\n"
    "v_accvgpr_read_b32 v212, a68\n"
    "v_add_f32 v197, v197, v213\n"
    "v_accvgpr_read_b32 v214, a70\n"
    "v_accvgpr_read_b32 v216, a200\n"
    "v_add_f32 v201, v201, v217\n"
    "v_add_f32 v202, v202, v218\n"
    "v_add_f32 v203, v203, v219\n"
    "v_accvgpr_read_b32 v220, a204\n"
    "v_add_f32 v207, v207, v223\n"
    "v_mfma_f32_16x16x32_bf16 v[68:71], a[180:183], v[160:163], v[68:71]\n"
    "v_mul_f32 v55, v242, v55\n"
    "v_cvt_pk_bf16_f32 v159, v62, v63\n"
    "v_lshlrev_b32 v208, 16, v244\n"
    "v_accvgpr_read_b32 v209, a193\n"
    "v_add_f32 v194, v194, v210\n"
    "v_accvgpr_read_b32 v211, a195\n"
    "v_add_f32 v196, v196, v212\n"
    "v_accvgpr_read_b32 v213, a197\n"
    "v_add_f32 v198, v198, v214\n"
    "v_add_f32 v199, v199, v215\n"
    "v_add_f32 v200, v200, v216\n"
    "v_accvgpr_read_b32 v217, a73\n"
    "v_accvgpr_read_b32 v218, a202\n"
    "v_accvgpr_read_b32 v219, a75\n"
    "v_add_f32 v204, v204, v220\n"
    "v_add_f32 v205, v205, v221\n"
    "v_add_f32 v206, v206, v222\n"
    "v_accvgpr_read_b32 v223, a207\n"
    "v_mfma_f32_16x16x32_bf16 v[76:79], a[180:183], v[168:171], v[76:79]\n"
    "v_cvt_pk_bf16_f32 v150, v44, v45\n"
    "v_mul_f32 v64, v240, v64\n"
    "v_add_f32 v192, v192, v208\n"
    "v_add_f32 v193, v193, v209\n"
    "v_add_f32 v195, v195, v211\n"
    "v_accvgpr_read_b32 v214, a198\n"
    "v_add_f32 v201, v201, v217\n"
    "v_add_f32 v203, v203, v219\n"
    "v_accvgpr_read_b32 v221, a77\n"
    "v_mfma_f32_16x16x32_bf16 v[84:87], a[180:183], v[176:179], v[84:87]\n"
    "v_cvt_pk_bf16_f32 v151, v46, v47\n"
    "v_mul_f32 v65, v240, v65\n"
    "v_mul_f32 v66, v240, v66\n"
    "v_mul_f32 v67, v240, v67\n"
    "v_accvgpr_read_b32 v210, a194\n"
    "v_and_b32 v211, 0xffff0000, v245\n"
    "v_accvgpr_read_b32 v215, a71\n"
    "v_add_f32 v205, v205, v221\n"
    "v_accvgpr_read_b32 v222, a78\n"
    "v_mfma_f32_16x16x32_bf16 v[92:95], a[180:183], v[184:187], v[92:95]\n"
    "v_mul_f32 v72, v241, v72\n"
    "v_mul_f32 v73, v241, v73\n"
    "v_mul_f32 v74, v241, v74\n"
    "v_and_b32 v209, 0xffff0000, v244\n"
    "v_add_f32 v194, v194, v210\n"
    "v_add_f32 v197, v197, v213\n"
    "v_accvgpr_read_b32 v219, a203\n"
    "v_add_f32 v206, v206, v222\n"
    "v_mfma_f32_16x16x32_bf16 v[96:99], a[184:187], v[160:163], v[96:99]\n"
    "v_mul_f32 v75, v241, v75\n"
    "v_mul_f32 v80, v242, v80\n"
    "v_mul_f32 v81, v242, v81\n"
    "v_lshlrev_b32 v210, 16, v245\n"
    "v_accvgpr_read_b32 v212, a196\n"
    "v_accvgpr_read_b32 v217, a201\n"
    "v_accvgpr_read_b32 v221, a205\n"
    "v_mfma_f32_16x16x32_bf16 v[104:107], a[184:187], v[168:171], v[104:107]\n"
    "v_mul_f32 v82, v242, v82\n"
    "v_mul_f32 v83, v242, v83\n"
    "v_add_f32 v193, v193, v209\n"
    "v_add_f32 v196, v196, v212\n"
    "v_lshlrev_b32 v216, 16, v244\n"
    "v_mfma_f32_16x16x32_bf16 v[112:115], a[184:187], v[176:179], v[112:115]\n"
    "v_mul_f32 v88, v243, v88\n"
    "v_mul_f32 v89, v243, v89\n"
    "v_add_f32 v198, v198, v214\n"
    "v_add_f32 v199, v199, v215\n"
    "v_accvgpr_read_b32 v222, a206\n"
    "v_mfma_f32_16x16x32_bf16 v[120:123], a[184:187], v[184:187], v[120:123]\n"
    "v_mul_f32 v90, v243, v90\n"
    "v_mul_f32 v91, v243, v91\n"
    "v_add_f32 v194, v194, v210\n"
    "v_mfma_f32_16x16x32_bf16 v[100:103], a[188:191], v[160:163], v[100:103]\n"
    "v_cvt_pk_bf16_f32 v154, v52, v53\n"
    "v_lshlrev_b32 v212, 16, v246\n"
    "v_accvgpr_read_b32 v215, a199\n"
    "v_mfma_f32_16x16x32_bf16 v[108:111], a[188:191], v[168:171], v[108:111]\n"
    "v_cvt_pk_bf16_f32 v155, v54, v55\n"
    "v_mul_f32 v208, v192, v192\n"
    "v_add_f32 v195, v195, v211\n"
    "v_and_b32 v213, 0xffff0000, v246\n"
    "v_add_f32 v201, v201, v217\n"
    "v_mfma_f32_16x16x32_bf16 v[116:119], a[188:191], v[176:179], v[116:119]\n"
    "v_mul_f32 v68, v240, v68\n"
    "v_mul_f32 v69, v240, v69\n"
    "v_mul_f32 v70, v240, v70\n"
    "v_mfma_f32_16x16x32_bf16 v[124:127], a[188:191], v[184:187], v[124:127]\n"
    "v_mul_f32 v71, v240, v71\n"
    "v_mul_f32 v209, v193, v193\n"
    "v_lshlrev_b32 v214, 16, v247\n"
    "v_add_f32 v199, v199, v215\n"
    "v_and_b32 v217, 0xffff0000, v244\n"
    "v_mfma_f32_16x16x32_bf16 v[0:3], a[128:131], v[128:131], 0\n"
    "v_mul_f32 v76, v241, v76\n"
    "v_mul_f32 v77, v241, v77\n"
    "v_and_b32 v215, 0xffff0000, v247\n"
    "v_add_f32 v202, v202, v218\n"
    "v_mfma_f32_16x16x32_bf16 v[0:3], a[144:147], v[144:147], v[0:3]\n"
    "v_mul_f32 v78, v241, v78\n"
    "v_mul_f32 v79, v241, v79\n"
    "v_add_f32 v203, v203, v219\n"
    "v_add_f32 v205, v205, v221\n"
    "v_mfma_f32_16x16x32_bf16 v[8:11], a[128:131], v[132:135], 0\n"
    "v_mul_f32 v84, v242, v84\n"
    "v_add_f32 v200, v200, v216\n"
    "v_add_f32 v206, v206, v222\n"
    "v_mfma_f32_16x16x32_bf16 v[8:11], a[144:147], v[148:151], v[8:11]\n"
    "v_mul_f32 v85, v242, v85\n"
    "v_mul_f32 v86, v242, v86\n"
    "v_mul_f32 v87, v242, v87\n"
    "v_mfma_f32_16x16x32_bf16 v[16:19], a[128:131], v[136:139], 0\n"
    "v_add_f32 v201, v201, v217\n"
    "v_lshlrev_b32 v218, 16, v245\n"
    "v_and_b32 v219, 0xffff0000, v245\n"
    "v_mfma_f32_16x16x32_bf16 v[16:19], a[144:147], v[152:155], v[16:19]\n"
    "v_mul_f32 v92, v243, v92\n"
    "v_mul_f32 v93, v243, v93\n"
    "v_lshlrev_b32 v220, 16, v246\n"
    "v_mfma_f32_16x16x32_bf16 v[24:27], a[128:131], v[140:143], 0\n"
    "v_mul_f32 v94, v243, v94\n"
    "v_mul_f32 v95, v243, v95\n"
    "v_add_f32 v207, v207, v223\n"
    "v_mfma_f32_16x16x32_bf16 v[24:27], a[144:147], v[156:159], v[24:27]\n"
    "v_mul_f32 v96, v240, v96\n"
    "v_mul_f32 v97, v240, v97\n"
    "v_mfma_f32_16x16x32_bf16 v[4:7], a[132:135], v[128:131], 0\n"
    "v_mul_f32 v98, v240, v98\n"
    "s_waitcnt vmcnt(21)\n"
    "ds_read_b64_tr_b16 a[160:161], v228 offset:16384\n"
    "ds_read_b64_tr_b16 a[164:165], v229 offset:16384\n"
    "ds_read_b64_tr_b16 a[168:169], v228 offset:16896\n"
    "ds_read_b64_tr_b16 a[172:173], v229 offset:16896\n"
    "v_mfma_f32_16x16x32_bf16 v[4:7], a[148:151], v[144:147], v[4:7]\n"
    "v_cvt_pk_bf16_f32 v160, v64, v65\n"
    "v_cvt_pk_bf16_f32 v161, v66, v67\n"
    "v_mfma_f32_16x16x32_bf16 v[12:15], a[132:135], v[132:135], 0\n"
    "v_mul_f32 v99, v240, v99\n"
    "s_waitcnt vmcnt(20)\n"
    "ds_read_b64_tr_b16 a[162:163], v228 offset:18432\n"
    "ds_read_b64_tr_b16 a[166:167], v229 offset:18432\n"
    "ds_read_b64_tr_b16 a[170:171], v228 offset:18944\n"
    "ds_read_b64_tr_b16 a[174:175], v229 offset:18944\n"
    "v_mfma_f32_16x16x32_bf16 v[12:15], a[148:151], v[148:151], v[12:15]\n"
    "v_cvt_pk_bf16_f32 v164, v72, v73\n"
    "v_mul_f32 v104, v241, v104\n"
    "v_mul_f32 v105, v241, v105\n"
    "v_mfma_f32_16x16x32_bf16 v[20:23], a[132:135], v[136:139], 0\n"
    "v_cvt_pk_bf16_f32 v165, v74, v75\n"
    "v_mul_f32 v106, v241, v106\n"
    "v_mul_f32 v107, v241, v107\n"
    "v_mfma_f32_16x16x32_bf16 v[20:23], a[148:151], v[152:155], v[20:23]\n"
    "v_cvt_pk_bf16_f32 v168, v80, v81\n"
    "v_cvt_pk_bf16_f32 v169, v82, v83\n"
    "s_waitcnt lgkmcnt(4)\n"
    "buffer_load_dwordx4 v233, s[28:31], s88 offen offset:0 lds\n"
    "s_waitcnt vmcnt(18)\n"
    "ds_read_b64_tr_b16 a[176:177], v228 offset:20480\n"
    "s_waitcnt vmcnt(17)\n"
    "ds_read_b64_tr_b16 a[178:179], v228 offset:22528\n"
    "v_mul_f32 v112, v242, v112\n"
    "buffer_load_dwordx4 v233, s[32:35], s88 offen offset:1024 lds\n"
    "v_mfma_f32_16x16x32_bf16 v[28:31], a[132:135], v[140:143], 0\n"
    "v_mul_f32 v113, v242, v113\n"
    "s_waitcnt lgkmcnt(2)\n"
    "buffer_load_dwordx4 v233, s[36:39], s88 offen offset:2048 lds\n"
    "v_mfma_f32_16x16x32_bf16 v[28:31], a[148:151], v[156:159], v[28:31]\n"
    "v_cvt_pk_bf16_f32 v172, v88, v89\n"
    "buffer_load_dwordx4 v233, s[40:43], s88 offen offset:3072 lds\n"
    "s_add_u32 m0, s94, 20480\n"
    "s_add_u32 s88, s88, 32768\n"
    "v_cvt_pk_bf16_f32 v173, v90, v91\n"
    "v_mul_f32 v114, v242, v114\n"
    "v_mfma_f32_16x16x32_bf16 v[32:35], a[136:139], v[128:131], 0\n"
    "v_mul_f32 v115, v242, v115\n"
    "v_mul_f32 v210, v194, v194\n"
    "v_add_f32 v196, v196, v212\n"
    "v_mfma_f32_16x16x32_bf16 v[32:35], a[152:155], v[144:147], v[32:35]\n"
    "v_fma_f32 v208, v208, v234, v252\n"
    "v_mul_f32 v211, v195, v195\n"
    "v_add_f32 v197, v197, v213\n"
    "v_mfma_f32_16x16x32_bf16 v[40:43], a[136:139], v[132:135], 0\n"
    "v_cvt_pk_bf16_f32 v162, v68, v69\n"
    "v_cvt_pk_bf16_f32 v163, v70, v71\n"
    "v_mul_f32 v120, v243, v120\n"
    "v_mfma_f32_16x16x32_bf16 v[40:43], a[152:155], v[148:151], v[40:43]\n"
    "v_mul_f32 v121, v243, v121\n"
    "v_mul_f32 v122, v243, v122\n"
    "v_mul_f32 v123, v243, v123\n"
    "v_mfma_f32_16x16x32_bf16 v[48:51], a[136:139], v[136:139], 0\n"
    "v_fma_f32 v209, v209, v234, v252\n"
    "v_add_f32 v198, v198, v214\n"
    "v_add_f32 v199, v199, v215\n"
    "v_mfma_f32_16x16x32_bf16 v[48:51], a[152:155], v[152:155], v[48:51]\n"
    "v_cvt_pk_bf16_f32 v166, v76, v77\n"
    "v_cvt_pk_bf16_f32 v167, v78, v79\n"
    "v_and_b32 v221, 0xffff0000, v246\n"
    "v_mfma_f32_16x16x32_bf16 v[56:59], a[136:139], v[140:143], 0\n"
    "v_mul_f32 v100, v240, v100\n"
    "v_mul_f32 v101, v240, v101\n"
    "v_lshlrev_b32 v222, 16, v247\n"
    "v_mfma_f32_16x16x32_bf16 v[56:59], a[152:155], v[156:159], v[56:59]\n"
    "v_mul_f32 v102, v240, v102\n"
    "v_mul_f32 v103, v240, v103\n"
    "v_mul_f32 v216, v200, v200\n"
    "ds_read_b64_tr_b16 a[180:181], v229 offset:20480\n"
    "ds_read_b64_tr_b16 a[184:185], v228 offset:20992\n"
    "ds_read_b64_tr_b16 a[188:189], v229 offset:20992\n"
    "ds_read_b64_tr_b16 a[182:183], v229 offset:22528\n"
    "ds_read_b64_tr_b16 a[186:187], v228 offset:23040\n"
    "v_mfma_f32_16x16x32_bf16 v[36:39], a[140:143], v[128:131], 0\n"
    "v_cvt_pk_bf16_f32 v170, v84, v85\n"
    "v_cvt_pk_bf16_f32 v171, v86, v87\n"
    "v_add_f32 v202, v202, v218\n"
    "v_mfma_f32_16x16x32_bf16 v[36:39], a[156:159], v[144:147], v[36:39]\n"
    "v_mul_f32 v217, v201, v201\n"
    "v_add_f32 v203, v203, v219\n"
    "v_add_f32 v204, v204, v220\n"
    "v_mfma_f32_16x16x32_bf16 v[44:47], a[140:143], v[132:135], 0\n"
    "v_cvt_pk_bf16_f32 v174, v92, v93\n"
    "v_cvt_pk_bf16_f32 v175, v94, v95\n"
    "v_and_b32 v223, 0xffff0000, v247\n"
    "v_mfma_f32_16x16x32_bf16 v[44:47], a[156:159], v[148:151], v[44:47]\n"
    "v_mul_f32 v108, v241, v108\n"
    "v_mul_f32 v109, v241, v109\n"
    "v_mul_f32 v110, v241, v110\n"
    "v_mfma_f32_16x16x32_bf16 v[52:55], a[140:143], v[136:139], 0\n"
    "v_cvt_pk_bf16_f32 v176, v96, v97\n"
    "v_mul_f32 v111, v241, v111\n"
    "ds_read_b64_tr_b16 a[190:191], v229 offset:23040\n"
    "s_waitcnt lgkmcnt(3)\n"
    "buffer_load_dwordx4 v233, s[28:31], s88 offen offset:0 lds\n"
    "v_mfma_f32_16x16x32_bf16 v[52:55], a[156:159], v[152:155], v[52:55]\n"
    "v_cvt_pk_bf16_f32 v177, v98, v99\n"
    "buffer_load_dwordx4 v233, s[32:35], s88 offen offset:1024 lds\n"
    "v_mul_f32 v116, v242, v116\n"
    "v_mul_f32 v117, v242, v117\n"
    "v_mfma_f32_16x16x32_bf16 v[60:63], a[140:143], v[140:143], 0\n"
    "v_mul_f32 v118, v242, v118\n"
    "v_mul_f32 v119, v242, v119\n"
    "v_mfma_f32_16x16x32_bf16 v[60:63], a[156:159], v[156:159], v[60:63]\n"
    "v_cvt_pk_bf16_f32 v180, v104, v105\n"
    "v_cvt_pk_bf16_f32 v181, v106, v107\n"
    "s_barrier\n"
    "v_mfma_f32_16x16x32_bf16 v[64:67], a[160:163], v[128:131], 0\n"
    "v_mul_f32 v124, v243, v124\n"
    "v_mul_f32 v125, v243, v125\n"
    "s_waitcnt lgkmcnt(0)\n"
    "buffer_load_dwordx4 v233, s[36:39], s88 offen offset:2048 lds\n"
    "v_mfma_f32_16x16x32_bf16 v[64:67], a[176:179], v[144:147], v[64:67]\n"
    "v_mul_f32 v126, v243, v126\n"
    "buffer_load_dwordx4 v233, s[40:43], s88 offen offset:3072 lds\n"
    "s_add_u32 m0, s94, 0\n"
    "s_sub_u32 s88, s88, 32640\n"
    "v_mfma_f32_16x16x32_bf16 v[72:75], a[160:163], v[132:135], 0\n"
    "v_mul_f32 v127, v243, v127\n"
    "v_cvt_pk_bf16_f32 v184, v112, v113\n"
    "v_mfma_f32_16x16x32_bf16 v[72:75], a[176:179], v[148:151], v[72:75]\n"
    "v_cvt_pk_bf16_f32 v185, v114, v115\n"
    "v_fma_f32 v210, v210, v234, v252\n"
    "v_mfma_f32_16x16x32_bf16 v[80:83], a[160:163], v[136:139], 0\n"
    "v_mul_f32 v212, v196, v196\n"
    "v_mul_f32 v208, v208, v192\n"
    "v_mfma_f32_16x16x32_bf16 v[80:83], a[176:179], v[152:155], v[80:83]\n"
    "v_fma_f32 v211, v211, v234, v252\n"
    "v_mul_f32 v213, v197, v197\n"
    "v_mfma_f32_16x16x32_bf16 v[88:91], a[160:163], v[140:143], 0\n"
    "v_cvt_pk_bf16_f32 v188, v120, v121\n"
    "v_cvt_pk_bf16_f32 v189, v122, v123\n"
    "v_mfma_f32_16x16x32_bf16 v[88:91], a[176:179], v[156:159], v[88:91]\n"
    "v_mul_f32 v209, v209, v193\n"
    "s_waitcnt vmcnt(21)\n"
    "ds_read_b64_tr_b16 a[128:129], v228 offset:0\n"
    "ds_read_b64_tr_b16 a[132:133], v229 offset:0\n"
    "ds_read_b64_tr_b16 a[136:137], v228 offset:512\n"
    "ds_read_b64_tr_b16 a[140:141], v229 offset:512\n"
    "v_mfma_f32_16x16x32_bf16 v[68:71], a[164:167], v[128:131], 0\n"
    "v_mul_f32 v214, v198, v198\n"
    "v_mul_f32 v215, v199, v199\n"
    "v_add_f32 v205, v205, v221\n"
    "s_waitcnt vmcnt(20)\n"
    "ds_read_b64_tr_b16 a[130:131], v228 offset:2048\n"
    "ds_read_b64_tr_b16 a[134:135], v229 offset:2048\n"
    "ds_read_b64_tr_b16 a[138:139], v228 offset:2560\n"
    "ds_read_b64_tr_b16 a[142:143], v229 offset:2560\n"
    "v_mfma_f32_16x16x32_bf16 v[68:71], a[180:183], v[144:147], v[68:71]\n"
    "v_add_f32 v206, v206, v222\n"
    "v_cvt_pk_bf16_f32 v178, v100, v101\n"
    "v_mfma_f32_16x16x32_bf16 v[76:79], a[164:167], v[132:135], 0\n"
    "v_cvt_pk_bf16_f32 v179, v102, v103\n"
    "v_fma_f32 v216, v216, v234, v252\n"
    "v_mfma_f32_16x16x32_bf16 v[76:79], a[180:183], v[148:151], v[76:79]\n"
    "v_mul_f32 v218, v202, v202\n"
    "v_mul_f32 v219, v203, v203\n"
    "v_mfma_f32_16x16x32_bf16 v[84:87], a[164:167], v[136:139], 0\n"
    "v_fma_f32 v217, v217, v234, v252\n"
    "v_mul_f32 v220, v204, v204\n"
    "v_mfma_f32_16x16x32_bf16 v[84:87], a[180:183], v[152:155], v[84:87]\n"
    "v_add_f32 v207, v207, v223\n"
    "s_waitcnt vmcnt(17)\n"
    "ds_read_b64_tr_b16 a[144:145], v228 offset:4096\n"
    "s_waitcnt vmcnt(16)\n"
    "ds_read_b64_tr_b16 a[146:147], v228 offset:6144\n"
    "ds_read_b64_tr_b16 a[148:149], v229 offset:4096\n"
    "ds_read_b64_tr_b16 a[152:153], v228 offset:4608\n"
    "v_mfma_f32_16x16x32_bf16 v[92:95], a[164:167], v[140:143], 0\n"
    "ds_read_b64_tr_b16 a[150:151], v229 offset:6144\n"
    "ds_read_b64_tr_b16 a[156:157], v229 offset:4608\n"
    "v_cvt_pk_bf16_f32 v182, v108, v109\n"
    "v_cvt_pk_bf16_f32 v183, v110, v111\n"
    "v_mfma_f32_16x16x32_bf16 v[92:95], a[180:183], v[156:159], v[92:95]\n"
    "s_waitcnt lgkmcnt(6)\n"
    "buffer_load_dwordx4 v233, s[28:31], s88 offen offset:0 lds\n"
    "ds_read_b64_tr_b16 a[154:155], v228 offset:6656\n"
    "ds_read_b64_tr_b16 a[158:159], v229 offset:6656\n"
    "v_cvt_pk_bf16_f32 v186, v116, v117\n"
    "v_cvt_pk_bf16_f32 v187, v118, v119\n"
    "v_mfma_f32_16x16x32_bf16 v[96:99], a[168:171], v[128:131], 0\n"
    "buffer_load_dwordx4 v233, s[32:35], s88 offen offset:1024 lds\n"
    "v_mfma_f32_16x16x32_bf16 v[96:99], a[184:187], v[144:147], v[96:99]\n"
    "v_cvt_pk_bf16_f32 v190, v124, v125\n"
    "buffer_load_dwordx4 v233, s[36:39], s88 offen offset:2048 lds\n"
    "v_mfma_f32_16x16x32_bf16 v[104:107], a[168:171], v[132:135], 0\n"
    "v_cvt_pk_bf16_f32 v191, v126, v127\n"
    "buffer_load_dwordx4 v233, s[40:43], s88 offen offset:3072 lds\n"
    "v_mfma_f32_16x16x32_bf16 v[104:107], a[184:187], v[148:151], v[104:107]\n"
    "s_add_u32 m0, s94, 4096\n"
    "v_mul_f32 v210, v210, v194\n"
    "v_fma_f32 v212, v212, v234, v252\n"
    "v_fma_f32 v213, v213, v234, v252\n"
    "v_mfma_f32_16x16x32_bf16 v[112:115], a[168:171], v[136:139], 0\n"
    "s_add_u32 s88, s88, 32768\n"
    "v_exp_f32 v208, v208\n"
    "v_mul_f32 v211, v211, v195\n"
    "v_mfma_f32_16x16x32_bf16 v[112:115], a[184:187], v[152:155], v[112:115]\n"
    "v_exp_f32 v209, v209\n"
    "v_fma_f32 v214, v214, v234, v252\n"
    "v_mfma_f32_16x16x32_bf16 v[120:123], a[168:171], v[140:143], 0\n"
    "v_fma_f32 v215, v215, v234, v252\n"
    "v_mul_f32 v221, v205, v205\n"
    "v_mul_f32 v222, v206, v206\n"
    "v_mfma_f32_16x16x32_bf16 v[120:123], a[184:187], v[156:159], v[120:123]\n"
    "v_mul_f32 v216, v216, v200\n"
    "v_fma_f32 v218, v218, v234, v252\n"
    "v_fma_f32 v219, v219, v234, v252\n"
    "v_mfma_f32_16x16x32_bf16 v[100:103], a[172:175], v[128:131], 0\n"
    "v_mul_f32 v217, v217, v201\n"
    "v_fma_f32 v220, v220, v234, v252\n"
    "v_mul_f32 v223, v207, v207\n"
    "v_mfma_f32_16x16x32_bf16 v[100:103], a[188:191], v[144:147], v[100:103]\n"
    "ds_write_b128 v236, v[128:131] offset:0\n"
    "v_mfma_f32_16x16x32_bf16 v[108:111], a[172:175], v[132:135], 0\n"
    "ds_write_b128 v236, v[132:135] offset:1024\n"
    "v_mfma_f32_16x16x32_bf16 v[108:111], a[188:191], v[148:151], v[108:111]\n"
    "ds_write_b128 v236, v[136:139] offset:2048\n"
    "v_mfma_f32_16x16x32_bf16 v[116:119], a[172:175], v[136:139], 0\n"
    "ds_write_b128 v236, v[140:143] offset:3072\n"
    "v_mfma_f32_16x16x32_bf16 v[116:119], a[188:191], v[152:155], v[116:119]\n"
    "ds_write_b128 v236, v[144:147] offset:4096\n"
    "v_mfma_f32_16x16x32_bf16 v[124:127], a[172:175], v[140:143], 0\n"
    "ds_write_b128 v236, v[148:151] offset:5120\n"
    "v_mfma_f32_16x16x32_bf16 v[124:127], a[188:191], v[156:159], v[124:127]\n"
    "ds_write_b128 v236, v[152:155] offset:6144\n"
    "v_mfma_f32_16x16x32_bf16 v[0:3], a[128:131], v[160:163], v[0:3]\n"
    "s_waitcnt lgkmcnt(5)\n"
    "ds_write_b128 v236, v[156:159] offset:7168\n"
    "v_mfma_f32_16x16x32_bf16 v[0:3], a[144:147], v[176:179], v[0:3]\n"
    "ds_write_b128 v236, v[160:163] offset:8192\n"
    "v_mfma_f32_16x16x32_bf16 v[8:11], a[128:131], v[164:167], v[8:11]\n"
    "ds_write_b128 v236, v[164:167] offset:9216\n"
    "v_mfma_f32_16x16x32_bf16 v[8:11], a[144:147], v[180:183], v[8:11]\n"
    "ds_write_b128 v236, v[168:171] offset:10240\n"
    "v_mfma_f32_16x16x32_bf16 v[16:19], a[128:131], v[168:171], v[16:19]\n"
    "ds_write_b128 v236, v[172:175] offset:11264\n"
    "v_mfma_f32_16x16x32_bf16 v[16:19], a[144:147], v[184:187], v[16:19]\n"
    "ds_write_b128 v236, v[176:179] offset:12288\n"
    "v_mfma_f32_16x16x32_bf16 v[24:27], a[128:131], v[172:175], v[24:27]\n"
    "ds_write_b128 v236, v[180:183] offset:13312\n"
    "v_mfma_f32_16x16x32_bf16 v[24:27], a[144:147], v[188:191], v[24:27]\n"
    "ds_write_b128 v236, v[184:187] offset:14336\n"
    "v_mfma_f32_16x16x32_bf16 v[4:7], a[132:135], v[160:163], v[4:7]\n"
    "ds_write_b128 v236, v[188:191] offset:15360\n"
    "v_mfma_f32_16x16x32_bf16 v[4:7], a[148:151], v[176:179], v[4:7]\n"
    "buffer_load_dwordx4 v233, s[28:31], s88 offen offset:0 lds\n"
    "v_mfma_f32_16x16x32_bf16 v[12:15], a[132:135], v[164:167], v[12:15]\n"
    "v_exp_f32 v210, v210\n"
    "v_mfma_f32_16x16x32_bf16 v[12:15], a[148:151], v[180:183], v[12:15]\n"
    "v_mul_f32 v212, v212, v196\n"
    "v_mul_f32 v213, v213, v197\n"
    "v_mfma_f32_16x16x32_bf16 v[20:23], a[132:135], v[168:171], v[20:23]\n"
    "s_waitcnt vmcnt(19)\n"
    "ds_read_b64_tr_b16 a[160:161], v228 offset:8192\n"
    "s_waitcnt vmcnt(17) lgkmcnt(3)\n"
    "ds_read_b64_tr_b16 a[162:163], v228 offset:10240\n"
    "v_mfma_f32_16x16x32_bf16 v[20:23], a[148:151], v[184:187], v[20:23]\n"
    "s_waitcnt vmcnt(14)\n"
    "ds_read_b64_tr_b16 a[176:177], v228 offset:12288\n"
    "s_waitcnt vmcnt(13)\n"
    "ds_read_b64_tr_b16 a[178:179], v228 offset:14336\n"
    "ds_read_b64_tr_b16 a[164:165], v229 offset:8192\n"
    "ds_read_b64_tr_b16 a[166:167], v229 offset:10240\n"
    "v_mfma_f32_16x16x32_bf16 v[28:31], a[132:135], v[172:175], v[28:31]\n"
    "ds_read_b64_tr_b16 a[180:181], v229 offset:12288\n"
    "ds_read_b64_tr_b16 a[182:183], v229 offset:14336\n"
    "ds_read_b64_tr_b16 a[168:169], v228 offset:8704\n"
    "ds_read_b64_tr_b16 a[170:171], v228 offset:10752\n"
    "v_mfma_f32_16x16x32_bf16 v[28:31], a[148:151], v[188:191], v[28:31]\n"
    "ds_read_b64_tr_b16 a[184:185], v228 offset:12800\n"
    "ds_read_b64_tr_b16 a[186:187], v228 offset:14848\n"
    "ds_read_b64_tr_b16 a[172:173], v229 offset:8704\n"
    "s_waitcnt lgkmcnt(10)\n"
    "ds_read_b64_tr_b16 a[174:175], v229 offset:10752\n"
    "v_mfma_f32_16x16x32_bf16 v[32:35], a[136:139], v[160:163], v[32:35]\n"
    "v_exp_f32 v211, v211\n"
    "v_mfma_f32_16x16x32_bf16 v[32:35], a[152:155], v[176:179], v[32:35]\n"
    "v_add_f32 v208, 1.0, v208\n"
    "v_add_f32 v209, 1.0, v209\n"
    "v_mfma_f32_16x16x32_bf16 v[40:43], a[136:139], v[164:167], v[40:43]\n"
    "v_mul_f32 v214, v214, v198\n"
    "v_fma_f32 v221, v221, v234, v252\n"
    "v_fma_f32 v222, v222, v234, v252\n"
    "v_mfma_f32_16x16x32_bf16 v[40:43], a[152:155], v[180:183], v[40:43]\n"
    "v_mul_f32 v215, v215, v199\n"
    "v_exp_f32 v216, v216\n"
    "v_mfma_f32_16x16x32_bf16 v[48:51], a[136:139], v[168:171], v[48:51]\n"
    "v_mul_f32 v218, v218, v202\n"
    "v_mul_f32 v219, v219, v203\n"
    "v_fma_f32 v223, v223, v234, v252\n"
    "ds_read_b64_tr_b16 a[188:189], v229 offset:12800\n"
    "ds_read_b64_tr_b16 a[190:191], v229 offset:14848\n"
    "v_mfma_f32_16x16x32_bf16 v[48:51], a[152:155], v[184:187], v[48:51]\n"
    "v_exp_f32 v217, v217\n"
    "v_mul_f32 v220, v220, v204\n"
    "v_mfma_f32_16x16x32_bf16 v[56:59], a[136:139], v[172:175], v[56:59]\n"
    "buffer_load_dwordx4 v233, s[36:39], s88 offen offset:2048 lds\n"
    "v_mfma_f32_16x16x32_bf16 v[56:59], a[152:155], v[188:191], v[56:59]\n"
    "v_exp_f32 v212, v212\n"
    "v_mfma_f32_16x16x32_bf16 v[36:39], a[140:143], v[160:163], v[36:39]\n"
    "v_add_f32 v210, 1.0, v210\n"
    "v_add_f32 v211, 1.0, v211\n"
    "v_mfma_f32_16x16x32_bf16 v[36:39], a[156:159], v[176:179], v[36:39]\n"
    "v_exp_f32 v213, v213\n"
    "v_mfma_f32_16x16x32_bf16 v[44:47], a[140:143], v[164:167], v[44:47]\n"
    "v_rcp_f32 v208, v208\n"
    "v_mfma_f32_16x16x32_bf16 v[44:47], a[156:159], v[180:183], v[44:47]\n"
    "v_rcp_f32 v209, v209\n"
    "v_mul_f32 v221, v221, v205\n"
    "v_mfma_f32_16x16x32_bf16 v[52:55], a[140:143], v[168:171], v[52:55]\n"
    "v_exp_f32 v214, v214\n"
    "v_mul_f32 v222, v222, v206\n"
    "v_mfma_f32_16x16x32_bf16 v[52:55], a[156:159], v[184:187], v[52:55]\n"
    "v_exp_f32 v215, v215\n"
    "v_mul_f32 v223, v223, v207\n"
    "v_mfma_f32_16x16x32_bf16 v[60:63], a[140:143], v[172:175], v[60:63]\n"
    "v_add_f32 v216, 1.0, v216\n"
    "v_exp_f32 v218, v218\n"
    "v_mfma_f32_16x16x32_bf16 v[60:63], a[156:159], v[188:191], v[60:63]\n"
    "v_exp_f32 v219, v219\n"
    "s_barrier\n"
    "v_mfma_f32_16x16x32_bf16 v[64:67], a[160:163], v[160:163], v[64:67]\n"
    "s_waitcnt lgkmcnt(0)\n"
    "v_add_f32 v217, 1.0, v217\n"
    "v_exp_f32 v220, v220\n"
    "v_mfma_f32_16x16x32_bf16 v[64:67], a[176:179], v[176:179], v[64:67]\n"
    "buffer_load_dwordx4 v233, s[32:35], s88 offen offset:1024 lds\n"
    "v_mfma_f32_16x16x32_bf16 v[72:75], a[160:163], v[164:167], v[72:75]\n"
    "v_rcp_f32 v210, v210\n"
    "v_mfma_f32_16x16x32_bf16 v[72:75], a[176:179], v[180:183], v[72:75]\n"
    "buffer_load_dwordx4 v233, s[40:43], s88 offen offset:3072 lds\n"
    "v_mfma_f32_16x16x32_bf16 v[80:83], a[160:163], v[168:171], v[80:83]\n"
    "s_add_u32 s88, s88, 32640\n"
    "v_rcp_f32 v211, v211\n"
    "v_mfma_f32_16x16x32_bf16 v[80:83], a[176:179], v[184:187], v[80:83]\n"
    "s_and_b32 s88, s88, 524287\n"
    "v_add_f32 v212, 1.0, v212\n"
    "v_exp_f32 v221, v221\n"
    "v_mfma_f32_16x16x32_bf16 v[88:91], a[160:163], v[172:175], v[88:91]\n"
    "s_add_u32 m0, s94, 8192\n"
    "v_add_f32 v213, 1.0, v213\n"
    "v_mul_f32 v192, v192, v208\n"
    "v_mfma_f32_16x16x32_bf16 v[88:91], a[176:179], v[188:191], v[88:91]\n"
    "ds_read_b128 a[224:227], v237 offset:0\n"
    "ds_read_b128 a[240:243], v237 offset:4096\n"
    "v_mfma_f32_16x16x32_bf16 v[68:71], a[164:167], v[160:163], v[68:71]\n"
    "buffer_load_dwordx4 v233, s[28:31], s88 offen offset:0 lds\n"
    "v_mfma_f32_16x16x32_bf16 v[68:71], a[180:183], v[176:179], v[68:71]\n"
    "v_mul_f32 v193, v193, v209\n"
    "v_add_f32 v214, 1.0, v214\n"
    "v_mfma_f32_16x16x32_bf16 v[76:79], a[164:167], v[164:167], v[76:79]\n"
    "v_exp_f32 v222, v222\n"
    "v_mfma_f32_16x16x32_bf16 v[76:79], a[180:183], v[180:183], v[76:79]\n"
    "ds_read_b128 a[228:231], v237 offset:1024\n"
    "ds_read_b128 a[244:247], v237 offset:5120\n"
    "v_mfma_f32_16x16x32_bf16 v[84:87], a[164:167], v[168:171], v[84:87]\n"
    "s_waitcnt vmcnt(15)\n"
    "ds_read_b64_tr_b16 a[128:129], v228 offset:16384\n"
    "s_waitcnt vmcnt(13)\n"
    "ds_read_b64_tr_b16 a[130:131], v228 offset:18432\n"
    "v_exp_f32 v223, v223\n"
    "v_mfma_f32_16x16x32_bf16 v[84:87], a[180:183], v[184:187], v[84:87]\n"
    "v_add_f32 v215, 1.0, v215\n"
    "s_waitcnt vmcnt(11)\n"
    "ds_read_b64_tr_b16 a[144:145], v228 offset:20480\n"
    "s_waitcnt vmcnt(9)\n"
    "ds_read_b64_tr_b16 a[146:147], v228 offset:22528\n"
    "ds_read_b64_tr_b16 a[132:133], v229 offset:16384\n"
    "ds_read_b64_tr_b16 a[134:135], v229 offset:18432\n"
    "v_mfma_f32_16x16x32_bf16 v[92:95], a[164:167], v[172:175], v[92:95]\n"
    "ds_read_b64_tr_b16 a[148:149], v229 offset:20480\n"
    "ds_read_b64_tr_b16 a[150:151], v229 offset:22528\n"
    "ds_read_b64_tr_b16 a[136:137], v228 offset:16896\n"
    "ds_read_b64_tr_b16 a[138:139], v228 offset:18944\n"
    "v_mfma_f32_16x16x32_bf16 v[92:95], a[180:183], v[188:191], v[92:95]\n"
    "ds_read_b64_tr_b16 a[152:153], v228 offset:20992\n"
    "s_waitcnt lgkmcnt(10)\n"
    "ds_read_b64_tr_b16 a[154:155], v228 offset:23040\n"
    "ds_read_b64_tr_b16 a[140:141], v229 offset:16896\n"
    "ds_read_b64_tr_b16 a[142:143], v229 offset:18944\n"
    "v_rcp_f32 v216, v216\n"
    "v_add_f32 v218, 1.0, v218\n"
    "v_mfma_f32_16x16x32_bf16 v[96:99], a[168:171], v[160:163], v[96:99]\n"
    "v_rcp_f32 v217, v217\n"
    "v_mfma_f32_16x16x32_bf16 v[96:99], a[184:187], v[176:179], v[96:99]\n"
    "s_add_u32 s72, s72, s84\n"
    "s_addc_u32 s73, s73, 0\n"
    "ds_read_b128 a[232:235], v237 offset:2048\n"
    "ds_read_b64_tr_b16 a[156:157], v229 offset:20992\n"
    "v_mfma_f32_16x16x32_bf16 v[104:107], a[168:171], v[164:167], v[104:107]\n"
    "v_rcp_f32 v212, v212\n"
    "v_mfma_f32_16x16x32_bf16 v[104:107], a[184:187], v[180:183], v[104:107]\n"
    "v_add_f32 v219, 1.0, v219\n"
    "s_waitcnt lgkmcnt(10)\n"
    "ds_read_b64_tr_b16 a[158:159], v229 offset:23040\n"
    "v_mfma_f32_16x16x32_bf16 v[112:115], a[168:171], v[168:171], v[112:115]\n"
    "v_rcp_f32 v213, v213\n"
    "v_mfma_f32_16x16x32_bf16 v[112:115], a[184:187], v[184:187], v[112:115]\n"
    "v_add_f32 v220, 1.0, v220\n"
    "ds_read_b128 a[248:251], v237 offset:6144\n"
    "ds_read_b128 a[236:239], v237 offset:3072\n"
    "ds_read_b128 a[252:255], v237 offset:7168\n"
    "v_mfma_f32_16x16x32_bf16 v[120:123], a[168:171], v[172:175], v[120:123]\n"
    "v_mul_f32 v194, v194, v210\n"
    "v_mul_f32 v195, v195, v211\n"
    "v_add_f32 v221, 1.0, v221\n"
    "v_mfma_f32_16x16x32_bf16 v[120:123], a[184:187], v[188:191], v[120:123]\n"
    "v_rcp_f32 v214, v214\n"
    "v_cvt_pk_bf16_f32 v192, v192, v193\n"
    "v_mfma_f32_16x16x32_bf16 v[100:103], a[172:175], v[160:163], v[100:103]\n"
    "v_rcp_f32 v215, v215\n"
    "v_add_f32 v222, 1.0, v222\n"
    "v_mfma_f32_16x16x32_bf16 v[100:103], a[188:191], v[176:179], v[100:103]\n"
    "v_rcp_f32 v218, v218\n"
    "v_add_f32 v223, 1.0, v223\n"
    "v_mfma_f32_16x16x32_bf16 v[108:111], a[172:175], v[164:167], v[108:111]\n"
    "v_mul_f32 v200, v200, v216\n"
    "v_rcp_f32 v219, v219\n"
    "v_mfma_f32_16x16x32_bf16 v[108:111], a[188:191], v[180:183], v[108:111]\n"
    "s_add_u32 s72, s72, 10485760\n"
    "s_addc_u32 s73, s73, 0\n"
    "v_mul_f32 v201, v201, v217\n"
    "v_mfma_f32_16x16x32_bf16 v[116:119], a[172:175], v[168:171], v[116:119]\n"
    "v_mul_f32 v196, v196, v212\n"
    "v_mul_f32 v197, v197, v213\n"
    "v_mfma_f32_16x16x32_bf16 v[116:119], a[188:191], v[184:187], v[116:119]\n"
    "v_rcp_f32 v220, v220\n"
    "v_mfma_f32_16x16x32_bf16 v[124:127], a[172:175], v[172:175], v[124:127]\n"
    "v_rcp_f32 v221, v221\n"
    "v_cvt_pk_bf16_f32 v193, v194, v195\n"
    "v_mfma_f32_16x16x32_bf16 v[124:127], a[188:191], v[188:191], v[124:127]\n"
    "v_mul_f32 v198, v198, v214\n"
    "v_rcp_f32 v222, v222\n"
    "v_mfma_f32_16x16x32_bf16 v[0:3], a[128:131], a[224:227], v[0:3]\n"
    "v_mul_f32 v199, v199, v215\n"
    "v_rcp_f32 v223, v223\n"
    "v_mfma_f32_16x16x32_bf16 v[0:3], a[144:147], a[240:243], v[0:3]\n"
    "v_mul_f32 v202, v202, v218\n"
    "v_mul_f32 v203, v203, v219\n"
    "v_cvt_pk_bf16_f32 v200, v200, v201\n"
    "v_mfma_f32_16x16x32_bf16 v[8:11], a[128:131], a[228:231], v[8:11]\n"
    "s_and_b32 s73, s73, 0xffff\n"
    "v_cvt_pk_bf16_f32 v194, v196, v197\n"
    "v_mul_f32 v204, v204, v220\n"
    "v_mfma_f32_16x16x32_bf16 v[8:11], a[144:147], a[244:247], v[8:11]\n"
    "buffer_load_dwordx4 v233, s[36:39], s88 offen offset:2048 lds\n"
    "s_waitcnt lgkmcnt(0)\n"
    "v_mfma_f32_16x16x32_bf16 v[16:19], a[128:131], a[232:235], v[16:19]\n"
    "s_lshl_b32 s84, s97, 2\n"
    "v_mul_f32 v205, v205, v221\n"
    "v_cvt_pk_bf16_f32 v195, v198, v199\n"
    "v_mfma_f32_16x16x32_bf16 v[16:19], a[144:147], a[248:251], v[16:19]\n"
    "v_mul_f32 v206, v206, v222\n"
    "v_mul_f32 v207, v207, v223\n"
    "v_cvt_pk_bf16_f32 v201, v202, v203\n"
    "v_mfma_f32_16x16x32_bf16 v[24:27], a[128:131], a[236:239], v[24:27]\n"
    "buffer_store_dwordx4 v[192:195], v224, s[72:75], 0 offen offset:0 sc1\n"
    "v_mfma_f32_16x16x32_bf16 v[24:27], a[144:147], a[252:255], v[24:27]\n"
    "s_add_u32 s84, s84, %1\n"
    "v_cvt_pk_bf16_f32 v202, v204, v205\n"
    "v_cvt_pk_bf16_f32 v203, v206, v207\n"
    "v_mfma_f32_16x16x32_bf16 v[4:7], a[132:135], a[224:227], v[4:7]\n"
    "buffer_store_dwordx4 v[200:203], v224, s[72:75], 0 offen offset:1024 sc1\n"
    "v_mfma_f32_16x16x32_bf16 v[4:7], a[148:151], a[240:243], v[4:7]\n"
    "s_lshl_b32 s84, s84, 7\n"
    "ds_read_b128 a[192:195], v237 offset:8192\n"
    "ds_read_b128 a[208:211], v237 offset:12288\n"
    "v_mfma_f32_16x16x32_bf16 v[12:15], a[132:135], a[228:231], v[12:15]\n"
    "buffer_load_dwordx4 v233, s[32:35], s88 offen offset:1024 lds\n"
    "v_mfma_f32_16x16x32_bf16 v[12:15], a[148:151], a[244:247], v[12:15]\n"
    "s_add_u32 s84, s84, s96\n"
    "ds_read_b128 a[196:199], v237 offset:9216\n"
    "ds_read_b128 a[212:215], v237 offset:13312\n"
    "v_mfma_f32_16x16x32_bf16 v[20:23], a[132:135], a[232:235], v[20:23]\n"
    "buffer_load_dwordx4 v233, s[40:43], s88 offen offset:3072 lds\n"
    "v_mfma_f32_16x16x32_bf16 v[20:23], a[148:151], a[248:251], v[20:23]\n"
    "s_add_u32 s88, s88, 32768\n"
    "s_add_u32 m0, s94, 12288\n"
    "v_and_b32 v240, 127, v233\n"
    "v_mfma_f32_16x16x32_bf16 v[28:31], a[132:135], a[236:239], v[28:31]\n"
    "s_lshl_b32 s84, s84, 10\n"
    "s_waitcnt vmcnt(12)\n"
    "ds_read_b64_tr_b16 a[160:161], v228 offset:0\n"
    "s_waitcnt vmcnt(10)\n"
    "ds_read_b64_tr_b16 a[162:163], v228 offset:2048\n"
    "v_mfma_f32_16x16x32_bf16 v[28:31], a[148:151], a[252:255], v[28:31]\n"
    "buffer_load_dwordx4 v233, s[28:31], s88 offen offset:0 lds\n"
    "v_mfma_f32_16x16x32_bf16 v[32:35], a[136:139], a[224:227], v[32:35]\n"
    "s_waitcnt vmcnt(8)\n"
    "ds_read_b64_tr_b16 a[176:177], v228 offset:4096\n"
    "s_waitcnt vmcnt(7)\n"
    "ds_read_b64_tr_b16 a[178:179], v228 offset:6144\n"
    "ds_read_b64_tr_b16 a[164:165], v229 offset:0\n"
    "v_mfma_f32_16x16x32_bf16 v[32:35], a[152:155], a[240:243], v[32:35]\n"
    "buffer_load_dwordx4 v233, s[36:39], s88 offen offset:2048 lds\n"
    "v_mfma_f32_16x16x32_bf16 v[40:43], a[136:139], a[228:231], v[40:43]\n"
    "ds_read_b64_tr_b16 a[166:167], v229 offset:2048\n"
    "ds_read_b64_tr_b16 a[180:181], v229 offset:4096\n"
    "ds_read_b64_tr_b16 a[182:183], v229 offset:6144\n"
    "v_mfma_f32_16x16x32_bf16 v[40:43], a[152:155], a[244:247], v[40:43]\n"
    "buffer_load_dwordx4 v233, s[32:35], s88 offen offset:1024 lds\n"
    "v_mfma_f32_16x16x32_bf16 v[48:51], a[136:139], a[232:235], v[48:51]\n"
    "ds_read_b64_tr_b16 a[168:169], v228 offset:512\n"
    "ds_read_b64_tr_b16 a[170:171], v228 offset:2560\n"
    "ds_read_b64_tr_b16 a[184:185], v228 offset:4608\n"
    "v_mfma_f32_16x16x32_bf16 v[48:51], a[152:155], a[248:251], v[48:51]\n"
    "buffer_load_dwordx4 v233, s[40:43], s88 offen offset:3072 lds\n"
    "v_mfma_f32_16x16x32_bf16 v[56:59], a[136:139], a[236:239], v[56:59]\n"
    "s_add_u32 m0, s94, 16384\n"
    "s_waitcnt lgkmcnt(6)\n"
    "ds_read_b64_tr_b16 a[186:187], v228 offset:6656\n"
    "ds_read_b64_tr_b16 a[172:173], v229 offset:512\n"
    "v_mfma_f32_16x16x32_bf16 v[56:59], a[152:155], a[252:255], v[56:59]\n"
    "s_sub_u32 s88, s88, 32640\n"
    "ds_read_b64_tr_b16 a[174:175], v229 offset:2560\n"
    "ds_read_b64_tr_b16 a[188:189], v229 offset:4608\n"
    "v_mfma_f32_16x16x32_bf16 v[36:39], a[140:143], a[224:227], v[36:39]\n"
    "ds_read_b64_tr_b16 a[190:191], v229 offset:6656\n"
    "ds_read_b128 a[200:203], v237 offset:10240\n"
    "ds_read_b128 a[216:219], v237 offset:14336\n"
    "v_mfma_f32_16x16x32_bf16 v[36:39], a[156:159], a[240:243], v[36:39]\n"
    "buffer_load_dwordx4 v233, s[28:31], s88 offen offset:0 lds\n"
    "v_mfma_f32_16x16x32_bf16 v[44:47], a[140:143], a[228:231], v[44:47]\n"
    "ds_read_b128 a[204:207], v237 offset:11264\n"
    "ds_read_b128 a[220:223], v237 offset:15360\n"
    "v_mfma_f32_16x16x32_bf16 v[44:47], a[156:159], a[244:247], v[44:47]\n"
    "v_xor_b32 v240, s22, v240\n"
    "v_lshrrev_b32 v241, 10, v233\n"
    "v_mfma_f32_16x16x32_bf16 v[52:55], a[140:143], a[232:235], v[52:55]\n"
    "buffer_load_dwordx4 v233, s[36:39], s88 offen offset:2048 lds\n"
    "v_mfma_f32_16x16x32_bf16 v[52:55], a[156:159], a[248:251], v[52:55]\n"
    "s_lshl_b32 s22, s90, 7\n"
    "v_lshl_add_u32 v232, v241, 12, v240\n"
    "v_bfe_u32 v240, v224, 4, 4\n"
    "v_mfma_f32_16x16x32_bf16 v[60:63], a[140:143], a[236:239], v[60:63]\n"
    "v_lshlrev_b32 v235, 1, v240\n"
    "v_lshrrev_b32 v243, 8, v224\n"
    "v_mfma_f32_16x16x32_bf16 v[60:63], a[156:159], a[252:255], v[60:63]\n"
    "buffer_load_dwordx4 v233, s[32:35], s88 offen offset:1024 lds\n"
    "v_mfma_f32_16x16x32_bf16 v[64:67], a[160:163], a[224:227], v[64:67]\n"
    "v_bfe_u32 v242, v224, 4, 4\n"
    "v_lshlrev_b32 v243, 2, v243\n"
    "v_mfma_f32_16x16x32_bf16 v[64:67], a[176:179], a[240:243], v[64:67]\n"
    "v_lshlrev_b32 v234, 10, v240\n"
    "v_lshrrev_b32 v241, 8, v224\n"
    "v_mfma_f32_16x16x32_bf16 v[72:75], a[160:163], a[228:231], v[72:75]\n"
    "buffer_load_dwordx4 v233, s[40:43], s88 offen offset:3072 lds\n"
    "v_mfma_f32_16x16x32_bf16 v[72:75], a[176:179], a[244:247], v[72:75]\n"
    "s_add_u32 m0, s94, 20480\n"
    "v_sub_u32 v242, v242, v243\n"
    "v_lshl_add_u32 v234, v241, 3, v234\n"
    "v_mfma_f32_16x16x32_bf16 v[80:83], a[160:163], a[232:235], v[80:83]\n"
    "s_add_u32 s88, s88, 32768\n"
    "v_and_b32 v240, 1, v242\n"
    "v_lshlrev_b32 v240, 4, v240\n"
    "v_mfma_f32_16x16x32_bf16 v[80:83], a[176:179], a[248:251], v[80:83]\n"
    "buffer_load_dwordx4 v233, s[28:31], s88 offen offset:0 lds\n"
    "v_mfma_f32_16x16x32_bf16 v[88:91], a[160:163], a[236:239], v[88:91]\n"
    "v_mov_b32 v241, 0x3f80\n"
    "v_lshlrev_b32 v240, v240, v241\n"
    "v_mfma_f32_16x16x32_bf16 v[88:91], a[176:179], a[252:255], v[88:91]\n"
    "s_waitcnt lgkmcnt(0)\n"
    "v_lshrrev_b32 v241, 1, v242\n"
    "v_cmp_eq_u32 vcc, 0, v241\n"
    "v_mfma_f32_16x16x32_bf16 v[68:71], a[164:167], a[224:227], v[68:71]\n"
    "buffer_load_dwordx4 v233, s[36:39], s88 offen offset:2048 lds\n"
    "v_mfma_f32_16x16x32_bf16 v[68:71], a[180:183], a[240:243], v[68:71]\n"
    "s_add_u32 s68, s68, s84\n"
    "s_addc_u32 s69, s69, 0\n"
    "s_add_u32 s84, s23, s22\n"
    "v_mfma_f32_16x16x32_bf16 v[76:79], a[164:167], a[228:231], v[76:79]\n"
    "s_sub_u32 s85, 7340032, s84\n"
    "s_mov_b32 s46, s85\n"
    "s_sub_u32 s85, 7308288, s84\n"
    "v_mfma_f32_16x16x32_bf16 v[76:79], a[180:183], a[244:247], v[76:79]\n"
    "buffer_load_dwordx4 v233, s[32:35], s88 offen offset:1024 lds\n"
    "v_mfma_f32_16x16x32_bf16 v[84:87], a[164:167], a[232:235], v[84:87]\n"
    "s_mov_b32 s50, s85\n"
    "s_waitcnt vmcnt(12)\n"
    "ds_read_b64_tr_b16 a[128:129], v228 offset:8192\n"
    "s_waitcnt vmcnt(11)\n"
    "ds_read_b64_tr_b16 a[130:131], v228 offset:10240\n"
    "v_mfma_f32_16x16x32_bf16 v[84:87], a[180:183], a[248:251], v[84:87]\n"
    "s_sub_u32 s85, 7276544, s84\n"
    "s_waitcnt vmcnt(8)\n"
    "ds_read_b64_tr_b16 a[144:145], v228 offset:12288\n"
    "s_waitcnt vmcnt(7)\n"
    "ds_read_b64_tr_b16 a[146:147], v228 offset:14336\n"
    "v_mfma_f32_16x16x32_bf16 v[92:95], a[164:167], a[236:239], v[92:95]\n"
    "buffer_load_dwordx4 v233, s[40:43], s88 offen offset:3072 lds\n"
    "v_mfma_f32_16x16x32_bf16 v[92:95], a[180:183], a[252:255], v[92:95]\n"
    "s_add_u32 s88, s88, 32640\n"
    "ds_read_b64_tr_b16 a[132:133], v229 offset:8192\n"
    "ds_read_b64_tr_b16 a[134:135], v229 offset:10240\n"
    "v_mfma_f32_16x16x32_bf16 v[96:99], a[168:171], a[224:227], v[96:99]\n"
    "s_add_u32 m0, s94, 0\n"
    "ds_read_b64_tr_b16 a[148:149], v229 offset:12288\n"
    "ds_read_b64_tr_b16 a[150:151], v229 offset:14336\n"
    "v_mfma_f32_16x16x32_bf16 v[96:99], a[184:187], a[240:243], v[96:99]\n"
    "s_and_b32 s88, s88, 524287\n"
    "ds_read_b64_tr_b16 a[136:137], v228 offset:8704\n"
    "ds_read_b64_tr_b16 a[138:139], v228 offset:10752\n"
    "v_mfma_f32_16x16x32_bf16 v[104:107], a[168:171], a[228:231], v[104:107]\n"
    "buffer_load_dwordx4 v233, s[28:31], s88 offen offset:0 lds\n"
    "v_mfma_f32_16x16x32_bf16 v[104:107], a[184:187], a[244:247], v[104:107]\n"
    "s_mov_b32 s54, s85\n"
    "ds_read_b64_tr_b16 a[152:153], v228 offset:12800\n"
    "ds_read_b64_tr_b16 a[154:155], v228 offset:14848\n"
    "v_mfma_f32_16x16x32_bf16 v[112:115], a[168:171], a[232:235], v[112:115]\n"
    "s_sub_u32 s85, 7244800, s84\n"
    "ds_read_b64_tr_b16 a[140:141], v229 offset:8704\n"
    "ds_read_b64_tr_b16 a[142:143], v229 offset:10752\n"
    "v_mfma_f32_16x16x32_bf16 v[112:115], a[184:187], a[248:251], v[112:115]\n"
    "buffer_load_dwordx4 v233, s[36:39], s88 offen offset:2048 lds\n"
    "v_mfma_f32_16x16x32_bf16 v[120:123], a[168:171], a[236:239], v[120:123]\n"
    "s_lshr_b32 s22, s95, 1\n"
    "ds_read_b64_tr_b16 a[156:157], v229 offset:12800\n"
    "s_waitcnt lgkmcnt(7)\n"
    "ds_read_b64_tr_b16 a[158:159], v229 offset:14848\n"
    "v_mfma_f32_16x16x32_bf16 v[120:123], a[184:187], a[252:255], v[120:123]\n"
    "s_lshl_b32 s22, s22, 13\n"
    "s_add_u32 s44, s44, s84\n"
    "s_addc_u32 s45, s45, 0\n"
    "v_mfma_f32_16x16x32_bf16 v[100:103], a[172:175], a[224:227], v[100:103]\n"
    "buffer_load_dwordx4 v233, s[32:35], s88 offen offset:1024 lds\n"
    "v_mfma_f32_16x16x32_bf16 v[100:103], a[188:191], a[240:243], v[100:103]\n"
    "s_mov_b32 s58, s85\n"
    "ds_read_b128 a[224:227], v238 offset:0\n"
    "ds_read_b128 a[240:243], v238 offset:4096\n"
    "v_mfma_f32_16x16x32_bf16 v[108:111], a[172:175], a[228:231], v[108:111]\n"
    "s_lshl_b32 s85, %1, 13\n"
    "s_add_u32 s48, s48, s84\n"
    "s_addc_u32 s49, s49, 0\n"
    "v_mfma_f32_16x16x32_bf16 v[108:111], a[188:191], a[244:247], v[108:111]\n"
    "buffer_load_dwordx4 v233, s[40:43], s88 offen offset:3072 lds\n"
    "v_mfma_f32_16x16x32_bf16 v[116:119], a[172:175], a[232:235], v[116:119]\n"
    "s_add_u32 m0, s94, 4096\n"
    "ds_read_b128 a[228:231], v238 offset:1024\n"
    "ds_read_b128 a[244:247], v238 offset:5120\n"
    "v_mfma_f32_16x16x32_bf16 v[116:119], a[188:191], a[248:251], v[116:119]\n"
    "s_add_u32 s88, s88, 32768\n"
    "ds_read_b128 a[232:235], v238 offset:2048\n"
    "ds_read_b128 a[248:251], v238 offset:6144\n"
    "v_mfma_f32_16x16x32_bf16 v[124:127], a[172:175], a[236:239], v[124:127]\n"
    "buffer_load_dwordx4 v233, s[28:31], s88 offen offset:0 lds\n"
    "v_mfma_f32_16x16x32_bf16 v[124:127], a[188:191], a[252:255], v[124:127]\n"
    "s_and_b32 s45, s45, 0xffff\n"
    "ds_read_b128 a[236:239], v238 offset:3072\n"
    "s_waitcnt lgkmcnt(5)\n"
    "ds_read_b128 a[252:255], v238 offset:7168\n"
    "v_mfma_f32_16x16x32_bf16 v[0:3], a[128:131], a[192:195], v[0:3]\n"
    "s_add_u32 s52, s52, s84\n"
    "s_addc_u32 s53, s53, 0\n"
    "v_mfma_f32_16x16x32_bf16 v[0:3], a[144:147], a[208:211], v[0:3]\n"
    "buffer_load_dwordx4 v233, s[36:39], s88 offen offset:2048 lds\n"
    "v_mfma_f32_16x16x32_bf16 v[8:11], a[128:131], a[196:199], v[8:11]\n"
    "s_add_u32 s56, s56, s84\n"
    "s_addc_u32 s57, s57, 0\n"
    "s_and_b32 s84, s95, 1\n"
    "v_mfma_f32_16x16x32_bf16 v[8:11], a[144:147], a[212:215], v[8:11]\n"
    "s_lshl_b32 s84, s84, 11\n"
    "s_add_u32 s22, s22, s84\n"
    "s_add_u32 s84, s23, s22\n"
    "v_mfma_f32_16x16x32_bf16 v[16:19], a[128:131], a[200:203], v[16:19]\n"
    "buffer_load_dwordx4 v233, s[32:35], s88 offen offset:1024 lds\n"
    "v_mfma_f32_16x16x32_bf16 v[16:19], a[144:147], a[216:219], v[16:19]\n"
    "s_lshl_b32 s22, s22, 4\n"
    "s_add_u32 s60, s60, s84\n"
    "s_addc_u32 s61, s61, 0\n"
    "v_mfma_f32_16x16x32_bf16 v[24:27], a[128:131], a[204:207], v[24:27]\n"
    "s_lshl_b32 s84, s90, 19\n"
    "s_add_u32 s84, s84, s22\n"
    "s_add_u32 s84, s84, s85\n"
    "v_mfma_f32_16x16x32_bf16 v[24:27], a[144:147], a[220:223], v[24:27]\n"
    "buffer_load_dwordx4 v233, s[40:43], s88 offen offset:3072 lds\n"
    "v_mfma_f32_16x16x32_bf16 v[4:7], a[132:135], a[192:195], v[4:7]\n"
    "s_add_u32 m0, s94, 8192\n"
    "s_sub_u32 s88, s88, 32640\n"
    "s_lshr_b32 s22, s22, 9\n"
    "v_mfma_f32_16x16x32_bf16 v[4:7], a[148:151], a[208:211], v[4:7]\n"
    "s_add_u32 s76, s76, s84\n"
    "s_addc_u32 s77, s77, 0\n"
    "s_lshl_b32 s84, s90, 2\n"
    "v_mfma_f32_16x16x32_bf16 v[12:15], a[132:135], a[196:199], v[12:15]\n"
    "buffer_load_dwordx4 v233, s[28:31], s88 offen offset:0 lds\n"
    "v_mfma_f32_16x16x32_bf16 v[12:15], a[148:151], a[212:215], v[12:15]\n"
    "s_add_u32 s84, s84, %1\n"
    "s_lshl_b32 s92, s84, 5\n"
    "s_lshl_b32 s84, s84, 14\n"
    "v_mfma_f32_16x16x32_bf16 v[20:23], a[132:135], a[200:203], v[20:23]\n"
    "s_add_u32 s99, s84, s22\n"
    "s_waitcnt vmcnt(14)\n"
    "ds_read_b64_tr_b16 a[160:161], v228 offset:16384\n"
    "s_waitcnt vmcnt(13)\n"
    "ds_read_b64_tr_b16 a[162:163], v228 offset:18432\n"
    "v_mfma_f32_16x16x32_bf16 v[20:23], a[148:151], a[216:219], v[20:23]\n"
    "buffer_load_dwordx4 v233, s[36:39], s88 offen offset:2048 lds\n"
    "v_mfma_f32_16x16x32_bf16 v[28:31], a[132:135], a[204:207], v[28:31]\n"
    "s_lshl_b32 s22, s93, 7\n"
    "s_waitcnt vmcnt(11)\n"
    "ds_read_b64_tr_b16 a[176:177], v228 offset:20480\n"
    "s_waitcnt vmcnt(10)\n"
    "ds_read_b64_tr_b16 a[178:179], v228 offset:22528\n"
    "v_mfma_f32_16x16x32_bf16 v[28:31], a[148:151], a[220:223], v[28:31]\n"
    "s_sub_u32 s85, 2097152, s99\n"
    "ds_read_b64_tr_b16 a[164:165], v229 offset:16384\n"
    "ds_read_b64_tr_b16 a[166:167], v229 offset:18432\n"
    "v_mfma_f32_16x16x32_bf16 v[32:35], a[136:139], a[192:195], v[32:35]\n"
    "buffer_load_dwordx4 v233, s[32:35], s88 offen offset:1024 lds\n"
    "v_mfma_f32_16x16x32_bf16 v[32:35], a[152:155], a[208:211], v[32:35]\n"
    "s_mov_b32 s82, s85\n"
    "ds_read_b64_tr_b16 a[180:181], v229 offset:20480\n"
    "ds_read_b64_tr_b16 a[182:183], v229 offset:22528\n"
    "v_mfma_f32_16x16x32_bf16 v[40:43], a[136:139], a[196:199], v[40:43]\n"
    "s_sub_u32 s85, 2097152, s99\n"
    "ds_read_b64_tr_b16 a[168:169], v228 offset:16896\n"
    "s_waitcnt lgkmcnt(7)\n"
    "ds_read_b64_tr_b16 a[170:171], v228 offset:18944\n"
    "v_mfma_f32_16x16x32_bf16 v[40:43], a[152:155], a[212:215], v[40:43]\n"
    "buffer_load_dwordx4 v233, s[40:43], s88 offen offset:3072 lds\n"
    "v_mfma_f32_16x16x32_bf16 v[48:51], a[136:139], a[200:203], v[48:51]\n"
    "s_add_u32 m0, s94, 12288\n"
    "ds_read_b64_tr_b16 a[184:185], v228 offset:20992\n"
    "ds_read_b64_tr_b16 a[186:187], v228 offset:23040\n"
    "v_mfma_f32_16x16x32_bf16 v[48:51], a[152:155], a[216:219], v[48:51]\n"
    "s_add_u32 s88, s88, 32768\n"
    "ds_read_b64_tr_b16 a[172:173], v229 offset:16896\n"
    "ds_read_b64_tr_b16 a[174:175], v229 offset:18944\n"
    "v_mfma_f32_16x16x32_bf16 v[56:59], a[136:139], a[204:207], v[56:59]\n"
    "buffer_load_dwordx4 v233, s[28:31], s88 offen offset:0 lds\n"
    "v_mfma_f32_16x16x32_bf16 v[56:59], a[152:155], a[220:223], v[56:59]\n"
    "ds_read_b64_tr_b16 a[188:189], v229 offset:20992\n"
    "ds_read_b64_tr_b16 a[190:191], v229 offset:23040\n"
    "v_mfma_f32_16x16x32_bf16 v[36:39], a[140:143], a[192:195], v[36:39]\n"
    "s_cmp_lt_u32 s97, 28\n"
    "s_cselect_b32 s22, s22, 9216\n"
    "v_mfma_f32_16x16x32_bf16 v[36:39], a[156:159], a[208:211], v[36:39]\n"
    "buffer_load_dwordx4 v233, s[36:39], s88 offen offset:2048 lds\n"
    "v_mfma_f32_16x16x32_bf16 v[44:47], a[140:143], a[196:199], v[44:47]\n"
    "s_add_u32 s68, s68, 18874368\n"
    "s_addc_u32 s69, s69, 0\n"
    "s_and_b32 s69, s69, 0xffff\n"
    "v_mfma_f32_16x16x32_bf16 v[44:47], a[156:159], a[212:215], v[44:47]\n"
    "s_add_u32 s48, s48, 31744\n"
    "s_addc_u32 s49, s49, 0\n"
    "s_and_b32 s49, s49, 0xffff\n"
    "v_mfma_f32_16x16x32_bf16 v[52:55], a[140:143], a[200:203], v[52:55]\n"
    "buffer_load_dwordx4 v233, s[32:35], s88 offen offset:1024 lds\n"
    "v_mfma_f32_16x16x32_bf16 v[52:55], a[156:159], a[216:219], v[52:55]\n"
    "s_add_u32 s52, s52, 63488\n"
    "s_addc_u32 s53, s53, 0\n"
    "s_and_b32 s53, s53, 0xffff\n"
    "v_mfma_f32_16x16x32_bf16 v[60:63], a[140:143], a[204:207], v[60:63]\n"
    "s_add_u32 s56, s56, 95232\n"
    "s_addc_u32 s57, s57, 0\n"
    "s_and_b32 s57, s57, 0xffff\n"
    "v_mfma_f32_16x16x32_bf16 v[60:63], a[156:159], a[220:223], v[60:63]\n"
    "buffer_load_dwordx4 v233, s[40:43], s88 offen offset:3072 lds\n"
    "v_mfma_f32_16x16x32_bf16 v[64:67], a[160:163], a[192:195], v[64:67]\n"
    "s_add_u32 s88, s88, 32640\n"
    "s_add_u32 m0, s94, 16384\n"
    "s_and_b32 s88, s88, 524287\n"
    "s_waitcnt lgkmcnt(0)\n"
    "v_mfma_f32_16x16x32_bf16 v[64:67], a[176:179], a[208:211], v[64:67]\n"
    "s_add_u32 s60, s60, 10485760\n"
    "s_addc_u32 s61, s61, 0\n"
    "s_and_b32 s61, s61, 0xffff\n"
    "v_mfma_f32_16x16x32_bf16 v[72:75], a[160:163], a[196:199], v[72:75]\n"
    "buffer_load_dwordx4 v233, s[28:31], s88 offen offset:0 lds\n"
    "v_mfma_f32_16x16x32_bf16 v[72:75], a[176:179], a[212:215], v[72:75]\n"
    "s_add_u32 s76, s76, 18874368\n"
    "s_addc_u32 s77, s77, 0\n"
    "s_and_b32 s77, s77, 0xffff\n"
    "v_mfma_f32_16x16x32_bf16 v[80:83], a[160:163], a[200:203], v[80:83]\n"
    "s_add_u32 s24, s24, s92\n"
    "s_addc_u32 s25, s25, 0\n"
    "s_and_b32 s25, s25, 0xffff\n"
    "v_mfma_f32_16x16x32_bf16 v[80:83], a[176:179], a[216:219], v[80:83]\n"
    "buffer_load_dwordx4 v233, s[36:39], s88 offen offset:2048 lds\n"
    "v_mfma_f32_16x16x32_bf16 v[88:91], a[160:163], a[204:207], v[88:91]\n"
    "s_add_u32 s80, s80, s99\n"
    "s_addc_u32 s81, s81, 0\n"
    "s_and_b32 s81, s81, 0xffff\n"
    "v_mfma_f32_16x16x32_bf16 v[88:91], a[176:179], a[220:223], v[88:91]\n"
    "buffer_load_ushort v216, v235, s[24:27], 0 offen offset:0\n"
    "v_mfma_f32_16x16x32_bf16 v[68:71], a[164:167], a[192:195], v[68:71]\n"
    "v_mfma_f32_16x16x32_bf16 v[68:71], a[180:183], a[208:211], v[68:71]\n"
    "buffer_load_dwordx2 v[208:209], v234, s[80:83], 0 offen offset:0\n"
    "v_mfma_f32_16x16x32_bf16 v[76:79], a[164:167], a[196:199], v[76:79]\n"
    "v_mfma_f32_16x16x32_bf16 v[76:79], a[180:183], a[212:215], v[76:79]\n"
    "buffer_load_dwordx2 v[210:211], v234, s[80:83], 0 offen offset:32\n"
    "v_mfma_f32_16x16x32_bf16 v[84:87], a[164:167], a[200:203], v[84:87]\n"
    "s_waitcnt vmcnt(18)\n"
    "ds_read_b64_tr_b16 a[128:129], v228 offset:0\n"
    "s_waitcnt vmcnt(17)\n"
    "ds_read_b64_tr_b16 a[130:131], v228 offset:2048\n"
    "v_mfma_f32_16x16x32_bf16 v[84:87], a[180:183], a[216:219], v[84:87]\n"
    "buffer_load_dwordx2 v[212:213], v234, s[80:83], 0 offen offset:128\n"
    "v_mfma_f32_16x16x32_bf16 v[92:95], a[164:167], a[204:207], v[92:95]\n"
    "s_waitcnt vmcnt(15)\n"
    "ds_read_b64_tr_b16 a[144:145], v228 offset:4096\n"
    "s_waitcnt vmcnt(14)\n"
    "ds_read_b64_tr_b16 a[146:147], v228 offset:6144\n"
    "ds_read_b64_tr_b16 a[132:133], v229 offset:0\n"
    "v_mfma_f32_16x16x32_bf16 v[92:95], a[180:183], a[220:223], v[92:95]\n"
    "buffer_load_dwordx2 v[214:215], v234, s[80:83], 0 offen offset:160\n"
    "v_mfma_f32_16x16x32_bf16 v[96:99], a[168:171], a[192:195], v[96:99]\n"
    "ds_read_b64_tr_b16 a[134:135], v229 offset:2048\n"
    "ds_read_b64_tr_b16 a[148:149], v229 offset:4096\n"
    "ds_read_b64_tr_b16 a[150:151], v229 offset:6144\n"
    "v_mfma_f32_16x16x32_bf16 v[96:99], a[184:187], a[208:211], v[96:99]\n"
    "ds_read_b64_tr_b16 a[136:137], v228 offset:512\n"
    "ds_read_b64_tr_b16 a[138:139], v228 offset:2560\n"
    "ds_read_b64_tr_b16 a[152:153], v228 offset:4608\n"
    "v_mfma_f32_16x16x32_bf16 v[104:107], a[168:171], a[196:199], v[104:107]\n"
    "buffer_load_dwordx4 v233, s[32:35], s88 offen offset:1024 lds\n"
    "v_mfma_f32_16x16x32_bf16 v[104:107], a[184:187], a[212:215], v[104:107]\n"
    "ds_read_b64_tr_b16 a[154:155], v228 offset:6656\n"
    "ds_read_b64_tr_b16 a[140:141], v229 offset:512\n"
    "ds_read_b64_tr_b16 a[142:143], v229 offset:2560\n"
    "v_mfma_f32_16x16x32_bf16 v[112:115], a[168:171], a[200:203], v[112:115]\n"
    "ds_read_b64_tr_b16 a[156:157], v229 offset:4608\n"
    "s_waitcnt lgkmcnt(10)\n"
    "ds_read_b64_tr_b16 a[158:159], v229 offset:6656\n"
    "v_mfma_f32_16x16x32_bf16 v[112:115], a[184:187], a[216:219], v[112:115]\n"
    "buffer_load_dwordx4 v233, s[40:43], s88 offen offset:3072 lds\n"
    "v_mfma_f32_16x16x32_bf16 v[120:123], a[168:171], a[204:207], v[120:123]\n"
    "s_add_u32 m0, s94, 20480\n"
    "s_add_u32 s88, s88, 32768\n"
    "v_mfma_f32_16x16x32_bf16 v[120:123], a[184:187], a[220:223], v[120:123]\n"
    "v_mfma_f32_16x16x32_bf16 v[100:103], a[172:175], a[192:195], v[100:103]\n"
    "buffer_load_dwordx4 v233, s[28:31], s88 offen offset:0 lds\n"
    "v_mfma_f32_16x16x32_bf16 v[100:103], a[188:191], a[208:211], v[100:103]\n"
    "ds_read_b128 a[192:195], v238 offset:8192\n"
    "ds_read_b128 a[208:211], v238 offset:12288\n"
    "v_mfma_f32_16x16x32_bf16 v[108:111], a[172:175], a[196:199], v[108:111]\n"
    "v_mfma_f32_16x16x32_bf16 v[108:111], a[188:191], a[212:215], v[108:111]\n"
    "buffer_load_dwordx4 v233, s[36:39], s88 offen offset:2048 lds\n"
    "v_mfma_f32_16x16x32_bf16 v[116:119], a[172:175], a[200:203], v[116:119]\n"
    "ds_read_b128 a[196:199], v238 offset:9216\n"
    "ds_read_b128 a[212:215], v238 offset:13312\n"
    "s_waitcnt lgkmcnt(4)\n"
    "ds_read_b128 a[200:203], v238 offset:10240\n"
    "v_mfma_f32_16x16x32_bf16 v[116:119], a[188:191], a[216:219], v[116:119]\n"
    "v_mfma_f32_16x16x32_bf16 v[124:127], a[172:175], a[204:207], v[124:127]\n"
    "buffer_load_dwordx4 v233, s[32:35], s88 offen offset:1024 lds\n"
    "v_mfma_f32_16x16x32_bf16 v[124:127], a[188:191], a[220:223], v[124:127]\n"
    "ds_read_b128 a[216:219], v238 offset:14336\n"
    "ds_read_b128 a[204:207], v238 offset:11264\n"
    "ds_read_b128 a[220:223], v238 offset:15360\n"
    "s_barrier\n"
    "v_mfma_f32_16x16x32_bf16 v[0:3], a[128:131], a[224:227], v[0:3]\n"
    "s_cmp_lt_u32 s97, 28\n"
    "s_cselect_b64 exec, s[8:9], 0\n"
    "buffer_atomic_add v226, v225, s[64:67], s22 offen\n"
    "s_mov_b64 exec, -1\n"
    "v_mfma_f32_16x16x32_bf16 v[0:3], a[144:147], a[240:243], v[0:3]\n"
    "s_mov_b32 s22, 0\n"
    "v_mfma_f32_16x16x32_bf16 v[8:11], a[128:131], a[228:231], v[8:11]\n"
    "v_mfma_f32_16x16x32_bf16 v[8:11], a[144:147], a[244:247], v[8:11]\n"
    "buffer_load_dwordx4 v233, s[40:43], s88 offen offset:3072 lds\n"
    "v_mfma_f32_16x16x32_bf16 v[16:19], a[128:131], a[232:235], v[16:19]\n"
    "s_add_u32 m0, s94, 0\n"
    "s_sub_u32 s88, s88, 32640\n"
    "v_mfma_f32_16x16x32_bf16 v[16:19], a[144:147], a[248:251], v[16:19]\n"
    "v_mfma_f32_16x16x32_bf16 v[24:27], a[128:131], a[236:239], v[24:27]\n"
    "buffer_load_dwordx4 v233, s[28:31], s88 offen offset:0 lds\n"
    "v_mfma_f32_16x16x32_bf16 v[24:27], a[144:147], a[252:255], v[24:27]\n"
    "v_mfma_f32_16x16x32_bf16 v[4:7], a[132:135], a[224:227], v[4:7]\n"
    "v_mfma_f32_16x16x32_bf16 v[4:7], a[148:151], a[240:243], v[4:7]\n"
    "buffer_load_dwordx4 v233, s[36:39], s88 offen offset:2048 lds\n"
    "v_mfma_f32_16x16x32_bf16 v[12:15], a[132:135], a[228:231], v[12:15]\n"
    "v_mfma_f32_16x16x32_bf16 v[12:15], a[148:151], a[244:247], v[12:15]\n"
    "v_mfma_f32_16x16x32_bf16 v[20:23], a[132:135], a[232:235], v[20:23]\n"
    "buffer_load_dwordx4 v233, s[32:35], s88 offen offset:1024 lds\n"
    "v_mfma_f32_16x16x32_bf16 v[20:23], a[148:151], a[248:251], v[20:23]\n"
    "s_waitcnt vmcnt(21)\n"
    "ds_read_b64_tr_b16 a[160:161], v228 offset:8192\n"
    "s_waitcnt vmcnt(20)\n"
    "ds_read_b64_tr_b16 a[162:163], v228 offset:10240\n"
    "s_waitcnt vmcnt(17)\n"
    "ds_read_b64_tr_b16 a[176:177], v228 offset:12288\n"
    "v_mfma_f32_16x16x32_bf16 v[28:31], a[132:135], a[236:239], v[28:31]\n"
    "s_waitcnt vmcnt(16)\n"
    "ds_read_b64_tr_b16 a[178:179], v228 offset:14336\n"
    "ds_read_b64_tr_b16 a[164:165], v229 offset:8192\n"
    "ds_read_b64_tr_b16 a[166:167], v229 offset:10240\n"
    "v_mfma_f32_16x16x32_bf16 v[28:31], a[148:151], a[252:255], v[28:31]\n"
    "buffer_load_dwordx4 v233, s[40:43], s88 offen offset:3072 lds\n"
    "v_mfma_f32_16x16x32_bf16 v[32:35], a[136:139], a[224:227], v[32:35]\n"
    "s_add_u32 m0, s94, 4096\n"
    "ds_read_b64_tr_b16 a[180:181], v229 offset:12288\n"
    "s_waitcnt lgkmcnt(7)\n"
    "ds_read_b64_tr_b16 a[182:183], v229 offset:14336\n"
    "v_mfma_f32_16x16x32_bf16 v[32:35], a[152:155], a[240:243], v[32:35]\n"
    "s_add_u32 s88, s88, 32768\n"
    "ds_read_b64_tr_b16 a[168:169], v228 offset:8704\n"
    "ds_read_b64_tr_b16 a[170:171], v228 offset:10752\n"
    "v_mfma_f32_16x16x32_bf16 v[40:43], a[136:139], a[228:231], v[40:43]\n"
    "buffer_load_dwordx4 v233, s[28:31], s88 offen offset:0 lds\n"
    "v_mfma_f32_16x16x32_bf16 v[40:43], a[152:155], a[244:247], v[40:43]\n"
    "ds_read_b64_tr_b16 a[184:185], v228 offset:12800\n"
    "ds_read_b64_tr_b16 a[186:187], v228 offset:14848\n"
    "ds_read_b64_tr_b16 a[172:173], v229 offset:8704\n"
    "v_mfma_f32_16x16x32_bf16 v[48:51], a[136:139], a[232:235], v[48:51]\n"
    "ds_read_b64_tr_b16 a[174:175], v229 offset:10752\n"
    "ds_read_b64_tr_b16 a[188:189], v229 offset:12800\n"
    "s_waitcnt lgkmcnt(9)\n"
    "ds_read_b64_tr_b16 a[190:191], v229 offset:14848\n"
    "v_mfma_f32_16x16x32_bf16 v[48:51], a[152:155], a[248:251], v[48:51]\n"
    "buffer_load_dwordx4 v233, s[36:39], s88 offen offset:2048 lds\n"
    "v_mfma_f32_16x16x32_bf16 v[56:59], a[136:139], a[236:239], v[56:59]\n"
    "v_mfma_f32_16x16x32_bf16 v[56:59], a[152:155], a[252:255], v[56:59]\n"
    "v_mfma_f32_16x16x32_bf16 v[36:39], a[140:143], a[224:227], v[36:39]\n"
    "buffer_load_dwordx4 v233, s[32:35], s88 offen offset:1024 lds\n"
    "v_mfma_f32_16x16x32_bf16 v[36:39], a[156:159], a[240:243], v[36:39]\n"
    "v_mfma_f32_16x16x32_bf16 v[44:47], a[140:143], a[228:231], v[44:47]\n"
    "v_mfma_f32_16x16x32_bf16 v[44:47], a[156:159], a[244:247], v[44:47]\n"
    "buffer_load_dwordx4 v233, s[40:43], s88 offen offset:3072 lds\n"
    "v_mfma_f32_16x16x32_bf16 v[52:55], a[140:143], a[232:235], v[52:55]\n"
    "s_add_u32 s88, s88, 32640\n"
    "s_add_u32 m0, s94, 8192\n"
    "s_and_b32 s88, s88, 524287\n"
    "v_mfma_f32_16x16x32_bf16 v[52:55], a[156:159], a[248:251], v[52:55]\n"
    "v_mfma_f32_16x16x32_bf16 v[60:63], a[140:143], a[236:239], v[60:63]\n"
    "s_waitcnt lgkmcnt(0)\n"
    "buffer_load_dwordx4 v233, s[28:31], s88 offen offset:0 lds\n"
    "v_mfma_f32_16x16x32_bf16 v[60:63], a[156:159], a[252:255], v[60:63]\n"
    "v_mfma_f32_16x16x32_bf16 v[64:67], a[160:163], a[224:227], v[64:67]\n"
    "v_mfma_f32_16x16x32_bf16 v[64:67], a[176:179], a[240:243], v[64:67]\n"
    "buffer_load_dwordx4 v233, s[36:39], s88 offen offset:2048 lds\n"
    "v_mfma_f32_16x16x32_bf16 v[72:75], a[160:163], a[228:231], v[72:75]\n"
    "v_mfma_f32_16x16x32_bf16 v[72:75], a[176:179], a[244:247], v[72:75]\n"
    "v_mfma_f32_16x16x32_bf16 v[80:83], a[160:163], a[232:235], v[80:83]\n"
    "buffer_load_dwordx4 v233, s[32:35], s88 offen offset:1024 lds\n"
    "v_mfma_f32_16x16x32_bf16 v[80:83], a[176:179], a[248:251], v[80:83]\n"
    "v_mfma_f32_16x16x32_bf16 v[88:91], a[160:163], a[236:239], v[88:91]\n"
    "v_mfma_f32_16x16x32_bf16 v[88:91], a[176:179], a[252:255], v[88:91]\n"
    "buffer_load_dwordx4 v233, s[40:43], s88 offen offset:3072 lds\n"
    "v_mfma_f32_16x16x32_bf16 v[68:71], a[164:167], a[224:227], v[68:71]\n"
    "s_add_u32 m0, s94, 12288\n"
    "s_add_u32 s88, s88, 32768\n"
    "v_mfma_f32_16x16x32_bf16 v[68:71], a[180:183], a[240:243], v[68:71]\n"
    "v_mfma_f32_16x16x32_bf16 v[76:79], a[164:167], a[228:231], v[76:79]\n"
    "buffer_load_dwordx4 v233, s[28:31], s88 offen offset:0 lds\n"
    "v_mfma_f32_16x16x32_bf16 v[76:79], a[180:183], a[244:247], v[76:79]\n"
    "v_mfma_f32_16x16x32_bf16 v[84:87], a[164:167], a[232:235], v[84:87]\n"
    "s_waitcnt vmcnt(18)\n"
    "ds_read_b64_tr_b16 a[128:129], v228 offset:16384\n"
    "s_waitcnt vmcnt(17)\n"
    "ds_read_b64_tr_b16 a[130:131], v228 offset:18432\n"
    "v_mfma_f32_16x16x32_bf16 v[84:87], a[180:183], a[248:251], v[84:87]\n"
    "buffer_load_dwordx4 v233, s[36:39], s88 offen offset:2048 lds\n"
    "v_mfma_f32_16x16x32_bf16 v[92:95], a[164:167], a[236:239], v[92:95]\n"
    "s_waitcnt vmcnt(15)\n"
    "ds_read_b64_tr_b16 a[144:145], v228 offset:20480\n"
    "s_waitcnt vmcnt(14)\n"
    "ds_read_b64_tr_b16 a[146:147], v228 offset:22528\n"
    "ds_read_b64_tr_b16 a[132:133], v229 offset:16384\n"
    "v_mfma_f32_16x16x32_bf16 v[92:95], a[180:183], a[252:255], v[92:95]\n"
    "ds_read_b64_tr_b16 a[134:135], v229 offset:18432\n"
    "ds_read_b64_tr_b16 a[148:149], v229 offset:20480\n"
    "ds_read_b64_tr_b16 a[150:151], v229 offset:22528\n"
    "v_mfma_f32_16x16x32_bf16 v[96:99], a[168:171], a[224:227], v[96:99]\n"
    "buffer_load_dwordx4 v233, s[32:35], s88 offen offset:1024 lds\n"
    "v_mfma_f32_16x16x32_bf16 v[96:99], a[184:187], a[240:243], v[96:99]\n"
    "ds_read_b64_tr_b16 a[136:137], v228 offset:16896\n"
    "ds_read_b64_tr_b16 a[138:139], v228 offset:18944\n"
    "ds_read_b64_tr_b16 a[152:153], v228 offset:20992\n"
    "v_mfma_f32_16x16x32_bf16 v[104:107], a[168:171], a[228:231], v[104:107]\n"
    "ds_read_b64_tr_b16 a[154:155], v228 offset:23040\n"
    "ds_read_b64_tr_b16 a[140:141], v229 offset:16896\n"
    "ds_read_b64_tr_b16 a[142:143], v229 offset:18944\n"
    "v_mfma_f32_16x16x32_bf16 v[104:107], a[184:187], a[244:247], v[104:107]\n"
    "buffer_load_dwordx4 v233, s[40:43], s88 offen offset:3072 lds\n"
    "v_mfma_f32_16x16x32_bf16 v[112:115], a[168:171], a[232:235], v[112:115]\n"
    "s_add_u32 m0, s94, 16384\n"
    "ds_read_b64_tr_b16 a[156:157], v229 offset:20992\n"
    "s_waitcnt lgkmcnt(10)\n"
    "ds_read_b64_tr_b16 a[158:159], v229 offset:23040\n"
    "v_mfma_f32_16x16x32_bf16 v[112:115], a[184:187], a[248:251], v[112:115]\n"
    "s_sub_u32 s88, s88, 32640\n"
    "v_lshlrev_b32 v216, 16, v216\n"
    "v_lshlrev_b32 v192, 16, v208\n"
    "v_mfma_f32_16x16x32_bf16 v[120:123], a[168:171], a[236:239], v[120:123]\n"
    "v_and_b32 v193, 0xffff0000, v208\n"
    "v_lshlrev_b32 v194, 16, v209\n"
    "v_mfma_f32_16x16x32_bf16 v[120:123], a[184:187], a[252:255], v[120:123]\n"
    "v_and_b32 v195, 0xffff0000, v209\n"
    "v_lshlrev_b32 v196, 16, v210\n"
    "v_mfma_f32_16x16x32_bf16 v[100:103], a[172:175], a[224:227], v[100:103]\n"
    "v_and_b32 v197, 0xffff0000, v210\n"
    "v_lshlrev_b32 v198, 16, v211\n"
    "v_mfma_f32_16x16x32_bf16 v[100:103], a[188:191], a[240:243], v[100:103]\n"
    "v_and_b32 v199, 0xffff0000, v211\n"
    "v_lshlrev_b32 v200, 16, v212\n"
    "v_mfma_f32_16x16x32_bf16 v[108:111], a[172:175], a[228:231], v[108:111]\n"
    "s_waitcnt lgkmcnt(0)\n"
    "buffer_load_dwordx4 v233, s[28:31], s88 offen offset:0 lds\n"
    "v_mfma_f32_16x16x32_bf16 v[108:111], a[188:191], a[244:247], v[108:111]\n"
    "ds_read_b128 a[224:227], v239 offset:0\n"
    "ds_read_b128 a[240:243], v239 offset:4096\n"
    "ds_read_b128 a[228:231], v239 offset:1024\n"
    "v_mfma_f32_16x16x32_bf16 v[116:119], a[172:175], a[232:235], v[116:119]\n"
    "ds_read_b128 a[244:247], v239 offset:5120\n"
    "v_and_b32 v201, 0xffff0000, v212\n"
    "v_mfma_f32_16x16x32_bf16 v[116:119], a[188:191], a[248:251], v[116:119]\n"
    "buffer_load_dwordx4 v233, s[36:39], s88 offen offset:2048 lds\n"
    "v_mfma_f32_16x16x32_bf16 v[124:127], a[172:175], a[236:239], v[124:127]\n"
    "ds_read_b128 a[232:235], v239 offset:2048\n"
    "ds_read_b128 a[248:251], v239 offset:6144\n"
    "ds_read_b128 a[236:239], v239 offset:3072\n"
    "v_mfma_f32_16x16x32_bf16 v[124:127], a[188:191], a[252:255], v[124:127]\n"
    "v_lshlrev_b32 v202, 16, v213\n"
    "v_and_b32 v203, 0xffff0000, v213\n"
    "v_mfma_f32_16x16x32_bf16 v[0:3], a[128:131], a[192:195], v[0:3]\n"
    "buffer_load_dword v227, v225, s[64:67], s20 offen offset:0 sc1\n"
    "v_mfma_f32_16x16x32_bf16 v[0:3], a[144:147], a[208:211], v[0:3]\n"
    "v_lshlrev_b32 v204, 16, v214\n"
    "v_and_b32 v205, 0xffff0000, v214\n"
    "v_mfma_f32_16x16x32_bf16 v[8:11], a[128:131], a[196:199], v[8:11]\n"
    "v_lshlrev_b32 v206, 16, v215\n"
    "v_and_b32 v207, 0xffff0000, v215\n"
    "v_mfma_f32_16x16x32_bf16 v[8:11], a[144:147], a[212:215], v[8:11]\n"
    "buffer_load_dwordx4 v233, s[32:35], s88 offen offset:1024 lds\n"
    "v_mfma_f32_16x16x32_bf16 v[16:19], a[128:131], a[200:203], v[16:19]\n"
    "v_mfma_f32_16x16x32_bf16 v[16:19], a[144:147], a[216:219], v[16:19]\n"
    "v_mfma_f32_16x16x32_bf16 v[24:27], a[128:131], a[204:207], v[24:27]\n"
    "buffer_load_dwordx4 v233, s[40:43], s88 offen offset:3072 lds\n"
    "v_mfma_f32_16x16x32_bf16 v[24:27], a[144:147], a[220:223], v[24:27]\n"
    "s_add_u32 m0, s94, 20480\n"
    "s_add_u32 s88, s88, 32768\n"
    "v_mfma_f32_16x16x32_bf16 v[4:7], a[132:135], a[192:195], v[4:7]\n"
    "v_mfma_f32_16x16x32_bf16 v[4:7], a[148:151], a[208:211], v[4:7]\n"
    "buffer_load_dwordx4 v233, s[28:31], s88 offen offset:0 lds\n"
    "v_mfma_f32_16x16x32_bf16 v[12:15], a[132:135], a[196:199], v[12:15]\n"
    "v_mfma_f32_16x16x32_bf16 v[12:15], a[148:151], a[212:215], v[12:15]\n"
    "v_mfma_f32_16x16x32_bf16 v[20:23], a[132:135], a[200:203], v[20:23]\n"
    "buffer_load_dwordx4 v233, s[36:39], s88 offen offset:2048 lds\n"
    "v_mfma_f32_16x16x32_bf16 v[20:23], a[148:151], a[216:219], v[20:23]\n"
    "s_waitcnt vmcnt(20)\n"
    "ds_read_b64_tr_b16 a[160:161], v228 offset:0\n"
    "s_waitcnt vmcnt(19)\n"
    "ds_read_b64_tr_b16 a[162:163], v228 offset:2048\n"
    "s_waitcnt vmcnt(16)\n"
    "ds_read_b64_tr_b16 a[176:177], v228 offset:4096\n"
    "v_mfma_f32_16x16x32_bf16 v[28:31], a[132:135], a[204:207], v[28:31]\n"
    "s_waitcnt vmcnt(15)\n"
    "ds_read_b64_tr_b16 a[178:179], v228 offset:6144\n"
    "ds_read_b64_tr_b16 a[164:165], v229 offset:0\n"
    "ds_read_b64_tr_b16 a[166:167], v229 offset:2048\n"
    "v_mfma_f32_16x16x32_bf16 v[28:31], a[148:151], a[220:223], v[28:31]\n"
    "buffer_load_dwordx4 v233, s[32:35], s88 offen offset:1024 lds\n"
    "v_mfma_f32_16x16x32_bf16 v[32:35], a[136:139], a[192:195], v[32:35]\n"
    "ds_read_b64_tr_b16 a[180:181], v229 offset:4096\n"
    "ds_read_b64_tr_b16 a[182:183], v229 offset:6144\n"
    "s_waitcnt lgkmcnt(8)\n"
    "ds_read_b64_tr_b16 a[168:169], v228 offset:512\n"
    "v_mfma_f32_16x16x32_bf16 v[32:35], a[152:155], a[208:211], v[32:35]\n"
    "ds_read_b64_tr_b16 a[170:171], v228 offset:2560\n"
    "ds_read_b64_tr_b16 a[184:185], v228 offset:4608\n"
    "ds_read_b64_tr_b16 a[186:187], v228 offset:6656\n"
    "v_mfma_f32_16x16x32_bf16 v[40:43], a[136:139], a[196:199], v[40:43]\n"
    "buffer_load_dwordx4 v233, s[40:43], s88 offen offset:3072 lds\n"
    "v_mfma_f32_16x16x32_bf16 v[40:43], a[152:155], a[212:215], v[40:43]\n"
    "s_add_u32 s88, s88, 32640\n"
    "ds_read_b64_tr_b16 a[172:173], v229 offset:512\n"
    "ds_read_b64_tr_b16 a[174:175], v229 offset:2560\n"
    "v_mfma_f32_16x16x32_bf16 v[48:51], a[136:139], a[200:203], v[48:51]\n"
    "s_add_u32 m0, s94, 0\n"
    "ds_read_b64_tr_b16 a[188:189], v229 offset:4608\n"
    "s_waitcnt lgkmcnt(9)\n"
    "ds_read_b64_tr_b16 a[190:191], v229 offset:6656\n"
    "v_mfma_f32_16x16x32_bf16 v[48:51], a[152:155], a[216:219], v[48:51]\n"
    "s_and_b32 s88, s88, 524287\n"
    "v_mfma_f32_16x16x32_bf16 v[56:59], a[136:139], a[204:207], v[56:59]\n"
    "v_mfma_f32_16x16x32_bf16 v[56:59], a[152:155], a[220:223], v[56:59]\n"
    "v_mfma_f32_16x16x32_bf16 v[36:39], a[140:143], a[192:195], v[36:39]\n"
    "v_mfma_f32_16x16x32_bf16 v[36:39], a[156:159], a[208:211], v[36:39]\n"
    "v_mfma_f32_16x16x32_bf16 v[44:47], a[140:143], a[196:199], v[44:47]\n"
    "v_mfma_f32_16x16x32_bf16 v[44:47], a[156:159], a[212:215], v[44:47]\n"
    "s_waitcnt lgkmcnt(0)\n"
    "buffer_load_dwordx4 v233, s[28:31], s88 offen offset:0 lds\n"
    "v_mfma_f32_16x16x32_bf16 v[52:55], a[140:143], a[200:203], v[52:55]\n"
    "v_mfma_f32_16x16x32_bf16 v[52:55], a[156:159], a[216:219], v[52:55]\n"
    "v_mfma_f32_16x16x32_bf16 v[60:63], a[140:143], a[204:207], v[60:63]\n"
    "buffer_load_dwordx4 v233, s[36:39], s88 offen offset:2048 lds\n"
    "v_mfma_f32_16x16x32_bf16 v[60:63], a[156:159], a[220:223], v[60:63]\n"
    "v_mfma_f32_16x16x32_bf16 v[64:67], a[160:163], a[192:195], v[64:67]\n"
    "v_mfma_f32_16x16x32_bf16 v[64:67], a[176:179], a[208:211], v[64:67]\n"
    "buffer_load_dwordx4 v233, s[32:35], s88 offen offset:1024 lds\n"
    "v_mfma_f32_16x16x32_bf16 v[72:75], a[160:163], a[196:199], v[72:75]\n"
    "v_mfma_f32_16x16x32_bf16 v[72:75], a[176:179], a[212:215], v[72:75]\n"
    "v_mfma_f32_16x16x32_bf16 v[80:83], a[160:163], a[200:203], v[80:83]\n"
    "buffer_load_dwordx4 v233, s[40:43], s88 offen offset:3072 lds\n"
    "v_mfma_f32_16x16x32_bf16 v[80:83], a[176:179], a[216:219], v[80:83]\n"
    "s_add_u32 m0, s94, 4096\n"
    "s_add_u32 s88, s88, 32768\n"
    "v_mfma_f32_16x16x32_bf16 v[88:91], a[160:163], a[204:207], v[88:91]\n"
    "v_mfma_f32_16x16x32_bf16 v[88:91], a[176:179], a[220:223], v[88:91]\n"
    "buffer_load_dwordx4 v233, s[28:31], s88 offen offset:0 lds\n"
    "v_mfma_f32_16x16x32_bf16 v[68:71], a[164:167], a[192:195], v[68:71]\n"
    "v_mfma_f32_16x16x32_bf16 v[68:71], a[180:183], a[208:211], v[68:71]\n"
    "v_mfma_f32_16x16x32_bf16 v[76:79], a[164:167], a[196:199], v[76:79]\n"
    "buffer_load_dwordx4 v233, s[36:39], s88 offen offset:2048 lds\n"
    "v_mfma_f32_16x16x32_bf16 v[76:79], a[180:183], a[212:215], v[76:79]\n"
    "v_mfma_f32_16x16x32_bf16 v[84:87], a[164:167], a[200:203], v[84:87]\n"
    "s_waitcnt vmcnt(20)\n"
    "ds_read_b64_tr_b16 a[128:129], v228 offset:8192\n"
    "s_waitcnt vmcnt(19)\n"
    "ds_read_b64_tr_b16 a[130:131], v228 offset:10240\n"
    "ds_read_b128 a[252:255], v239 offset:7168\n"
    "v_mfma_f32_16x16x32_bf16 v[84:87], a[180:183], a[216:219], v[84:87]\n"
    "buffer_load_dwordx4 v233, s[32:35], s88 offen offset:1024 lds\n"
    "v_mfma_f32_16x16x32_bf16 v[92:95], a[164:167], a[204:207], v[92:95]\n"
    "s_waitcnt vmcnt(17)\n"
    "ds_read_b64_tr_b16 a[144:145], v228 offset:12288\n"
    "s_waitcnt vmcnt(16)\n"
    "ds_read_b64_tr_b16 a[146:147], v228 offset:14336\n"
    "ds_read_b64_tr_b16 a[132:133], v229 offset:8192\n"
    "v_mfma_f32_16x16x32_bf16 v[92:95], a[180:183], a[220:223], v[92:95]\n"
    "ds_read_b64_tr_b16 a[134:135], v229 offset:10240\n"
    "ds_read_b64_tr_b16 a[148:149], v229 offset:12288\n"
    "ds_read_b64_tr_b16 a[150:151], v229 offset:14336\n"
    "v_mfma_f32_16x16x32_bf16 v[96:99], a[168:171], a[192:195], v[96:99]\n"
    "buffer_load_dwordx4 v233, s[40:43], s88 offen offset:3072 lds\n"
    "v_mfma_f32_16x16x32_bf16 v[96:99], a[184:187], a[208:211], v[96:99]\n"
    "s_sub_u32 s88, s88, 32640\n"
    "ds_read_b64_tr_b16 a[136:137], v228 offset:8704\n"
    "ds_read_b64_tr_b16 a[138:139], v228 offset:10752\n"
    "v_mfma_f32_16x16x32_bf16 v[104:107], a[168:171], a[196:199], v[104:107]\n"
    "s_add_u32 m0, s94, 8192\n"
    "ds_read_b64_tr_b16 a[152:153], v228 offset:12800\n"
    "ds_read_b64_tr_b16 a[154:155], v228 offset:14848\n"
    "v_mfma_f32_16x16x32_bf16 v[104:107], a[184:187], a[212:215], v[104:107]\n"
    "ds_read_b64_tr_b16 a[140:141], v229 offset:8704\n"
    "ds_read_b64_tr_b16 a[142:143], v229 offset:10752\n"
    "s_waitcnt lgkmcnt(10)\n"
    "ds_read_b64_tr_b16 a[156:157], v229 offset:12800\n"
    "v_mfma_f32_16x16x32_bf16 v[112:115], a[168:171], a[200:203], v[112:115]\n"
    "v_mfma_f32_16x16x32_bf16 v[112:115], a[184:187], a[216:219], v[112:115]\n"
    "ds_read_b64_tr_b16 a[158:159], v229 offset:14848\n"
    "v_mfma_f32_16x16x32_bf16 v[120:123], a[168:171], a[204:207], v[120:123]\n"
    "v_mfma_f32_16x16x32_bf16 v[120:123], a[184:187], a[220:223], v[120:123]\n"
    "v_mfma_f32_16x16x32_bf16 v[100:103], a[172:175], a[192:195], v[100:103]\n"
    "v_mfma_f32_16x16x32_bf16 v[100:103], a[188:191], a[208:211], v[100:103]\n"
    "ds_read_b128 a[192:195], v239 offset:8192\n"
    "ds_read_b128 a[208:211], v239 offset:12288\n"
    "v_mfma_f32_16x16x32_bf16 v[108:111], a[172:175], a[196:199], v[108:111]\n"
    "v_mfma_f32_16x16x32_bf16 v[108:111], a[188:191], a[212:215], v[108:111]\n"
    "s_waitcnt lgkmcnt(2)\n"
    "buffer_load_dwordx4 v233, s[28:31], s88 offen offset:0 lds\n"
    "v_mfma_f32_16x16x32_bf16 v[116:119], a[172:175], a[200:203], v[116:119]\n"
    "ds_read_b128 a[196:199], v239 offset:9216\n"
    "ds_read_b128 a[212:215], v239 offset:13312\n"
    "ds_read_b128 a[200:203], v239 offset:10240\n"
    "v_mfma_f32_16x16x32_bf16 v[116:119], a[188:191], a[216:219], v[116:119]\n"
    "v_mfma_f32_16x16x32_bf16 v[124:127], a[172:175], a[204:207], v[124:127]\n"
    "buffer_load_dwordx4 v233, s[36:39], s88 offen offset:2048 lds\n"
    "v_mfma_f32_16x16x32_bf16 v[124:127], a[188:191], a[220:223], v[124:127]\n"
    "ds_read_b128 a[216:219], v239 offset:14336\n"
    "ds_read_b128 a[204:207], v239 offset:11264\n"
    "ds_read_b128 a[220:223], v239 offset:15360\n"
    "s_waitcnt vmcnt(16)\n"
    "v_readfirstlane_b32 s23, v227\n"
    "s_nop 1\n"
    "s_cmp_lt_u32 s23, 28\n"
    "s_cbranch_scc1 K2_SP_U\n"
    "K2_SR_U:\n"
    "v_mfma_f32_16x16x32_bf16 v[0:3], a[128:131], a[224:227], v[0:3]\n"
    "buffer_load_dwordx4 a[0:3], v224, s[60:63], s89 offen offset:0 sc1\n"
    "v_mfma_f32_16x16x32_bf16 v[0:3], a[144:147], a[240:243], v[0:3]\n"
    "s_add_u32 s23, s89, 4096\n"
    "s_lshl_b32 s20, s90, 7\n"
    "s_add_u32 s20, s20, 1024\n"
    "v_mfma_f32_16x16x32_bf16 v[8:11], a[128:131], a[228:231], v[8:11]\n"
    "buffer_load_dwordx4 a[4:7], v224, s[60:63], s89 offen offset:1024 sc1\n"
    "v_mfma_f32_16x16x32_bf16 v[8:11], a[144:147], a[244:247], v[8:11]\n"
    "s_add_u32 s89, s89, 131072\n"
    "s_cmp_eq_u32 s89, 1835008\n"
    "s_cselect_b32 s89, 0, s89\n"
    "v_mfma_f32_16x16x32_bf16 v[16:19], a[128:131], a[232:235], v[16:19]\n"
    "buffer_load_dwordx4 a[8:11], v224, s[60:63], s23 offen offset:0 sc1\n"
    "v_mfma_f32_16x16x32_bf16 v[16:19], a[144:147], a[248:251], v[16:19]\n"
    "v_mfma_f32_16x16x32_bf16 v[24:27], a[128:131], a[236:239], v[24:27]\n"
    "buffer_load_dwordx4 a[12:15], v224, s[60:63], s23 offen offset:1024 sc1\n"
    "v_mfma_f32_16x16x32_bf16 v[24:27], a[144:147], a[252:255], v[24:27]\n"
    "s_add_u32 s23, s89, 4096\n"
    "v_mfma_f32_16x16x32_bf16 v[4:7], a[132:135], a[224:227], v[4:7]\n"
    "buffer_load_dwordx4 a[16:19], v224, s[60:63], s89 offen offset:0 sc1\n"
    "v_mfma_f32_16x16x32_bf16 v[4:7], a[148:151], a[240:243], v[4:7]\n"
    "v_mfma_f32_16x16x32_bf16 v[12:15], a[132:135], a[228:231], v[12:15]\n"
    "buffer_load_dwordx4 a[20:23], v224, s[60:63], s89 offen offset:1024 sc1\n"
    "v_mfma_f32_16x16x32_bf16 v[12:15], a[148:151], a[244:247], v[12:15]\n"
    "s_add_u32 s89, s89, 131072\n"
    "s_cmp_eq_u32 s89, 1835008\n"
    "s_cselect_b32 s89, 0, s89\n"
    "v_mfma_f32_16x16x32_bf16 v[20:23], a[132:135], a[232:235], v[20:23]\n"
    "buffer_load_dwordx4 a[24:27], v224, s[60:63], s23 offen offset:0 sc1\n"
    "v_mfma_f32_16x16x32_bf16 v[20:23], a[148:151], a[248:251], v[20:23]\n"
    "s_waitcnt vmcnt(22)\n"
    "ds_read_b64_tr_b16 a[160:161], v228 offset:16384\n"
    "s_waitcnt vmcnt(21)\n"
    "ds_read_b64_tr_b16 a[162:163], v228 offset:18432\n"
    "s_waitcnt vmcnt(18)\n"
    "ds_read_b64_tr_b16 a[176:177], v228 offset:20480\n"
    "v_mfma_f32_16x16x32_bf16 v[28:31], a[132:135], a[236:239], v[28:31]\n"
    "buffer_load_dwordx4 a[28:31], v224, s[60:63], s23 offen offset:1024 sc1\n"
    "v_mfma_f32_16x16x32_bf16 v[28:31], a[148:151], a[252:255], v[28:31]\n"
    "s_add_u32 s23, s89, 4096\n"
    "s_waitcnt vmcnt(18)\n"
    "ds_read_b64_tr_b16 a[178:179], v228 offset:22528\n"
    "ds_read_b64_tr_b16 a[164:165], v229 offset:16384\n"
    "v_mfma_f32_16x16x32_bf16 v[32:35], a[136:139], a[224:227], v[32:35]\n"
    "buffer_load_dwordx4 a[32:35], v224, s[60:63], s89 offen offset:0 sc1\n"
    "v_mfma_f32_16x16x32_bf16 v[32:35], a[152:155], a[240:243], v[32:35]\n"
    "ds_read_b64_tr_b16 a[166:167], v229 offset:18432\n"
    "ds_read_b64_tr_b16 a[180:181], v229 offset:20480\n"
    "s_waitcnt lgkmcnt(7)\n"
    "ds_read_b64_tr_b16 a[182:183], v229 offset:22528\n"
    "v_mfma_f32_16x16x32_bf16 v[40:43], a[136:139], a[228:231], v[40:43]\n"
    "buffer_load_dwordx4 a[36:39], v224, s[60:63], s89 offen offset:1024 sc1\n"
    "v_mfma_f32_16x16x32_bf16 v[40:43], a[152:155], a[244:247], v[40:43]\n"
    "ds_read_b64_tr_b16 a[168:169], v228 offset:16896\n"
    "ds_read_b64_tr_b16 a[170:171], v228 offset:18944\n"
    "ds_read_b64_tr_b16 a[184:185], v228 offset:20992\n"
    "v_mfma_f32_16x16x32_bf16 v[48:51], a[136:139], a[232:235], v[48:51]\n"
    "buffer_load_dwordx4 a[40:43], v224, s[60:63], s23 offen offset:0 sc1\n"
    "v_mfma_f32_16x16x32_bf16 v[48:51], a[152:155], a[248:251], v[48:51]\n"
    "ds_read_b64_tr_b16 a[186:187], v228 offset:23040\n"
    "ds_read_b64_tr_b16 a[172:173], v229 offset:16896\n"
    "ds_read_b64_tr_b16 a[174:175], v229 offset:18944\n"
    "v_mfma_f32_16x16x32_bf16 v[56:59], a[136:139], a[236:239], v[56:59]\n"
    "buffer_load_dwordx4 a[44:47], v224, s[60:63], s23 offen offset:1024 sc1\n"
    "v_mfma_f32_16x16x32_bf16 v[56:59], a[152:155], a[252:255], v[56:59]\n"
    "ds_read_b64_tr_b16 a[188:189], v229 offset:20992\n"
    "s_waitcnt lgkmcnt(7)\n"
    "ds_read_b64_tr_b16 a[190:191], v229 offset:23040\n"
    "v_mfma_f32_16x16x32_bf16 v[36:39], a[140:143], a[224:227], v[36:39]\n"
    "s_add_u32 s89, s89, 131072\n"
    "s_cmp_eq_u32 s89, 1835008\n"
    "s_cselect_b32 s89, 0, s89\n"
    "v_mfma_f32_16x16x32_bf16 v[36:39], a[156:159], a[240:243], v[36:39]\n"
    "buffer_load_dwordx4 v233, s[32:35], s88 offen offset:1024 lds\n"
    "v_mfma_f32_16x16x32_bf16 v[44:47], a[140:143], a[228:231], v[44:47]\n"
    "s_add_u32 s23, s89, 4096\n"
    "v_mfma_f32_16x16x32_bf16 v[44:47], a[156:159], a[244:247], v[44:47]\n"
    "buffer_load_dwordx4 a[48:51], v224, s[60:63], s89 offen offset:0 sc1\n"
    "v_mfma_f32_16x16x32_bf16 v[52:55], a[140:143], a[232:235], v[52:55]\n"
    "v_mfma_f32_16x16x32_bf16 v[52:55], a[156:159], a[248:251], v[52:55]\n"
    "buffer_load_dwordx4 a[52:55], v224, s[60:63], s89 offen offset:1024 sc1\n"
    "v_mfma_f32_16x16x32_bf16 v[60:63], a[140:143], a[236:239], v[60:63]\n"
    "s_add_u32 s89, s89, 131072\n"
    "s_cmp_eq_u32 s89, 1835008\n"
    "s_cselect_b32 s89, 0, s89\n"
    "v_mfma_f32_16x16x32_bf16 v[60:63], a[156:159], a[252:255], v[60:63]\n"
    "buffer_load_dwordx4 a[56:59], v224, s[60:63], s23 offen offset:0 sc1\n"
    "v_mfma_f32_16x16x32_bf16 v[64:67], a[160:163], a[224:227], v[64:67]\n"
    "v_mfma_f32_16x16x32_bf16 v[64:67], a[176:179], a[240:243], v[64:67]\n"
    "buffer_load_dwordx4 a[60:63], v224, s[60:63], s23 offen offset:1024 sc1\n"
    "v_mfma_f32_16x16x32_bf16 v[72:75], a[160:163], a[228:231], v[72:75]\n"
    "s_add_u32 s23, s89, 4096\n"
    "v_mfma_f32_16x16x32_bf16 v[72:75], a[176:179], a[244:247], v[72:75]\n"
    "buffer_load_dwordx4 a[64:67], v224, s[60:63], s89 offen offset:0 sc1\n"
    "v_mfma_f32_16x16x32_bf16 v[80:83], a[160:163], a[232:235], v[80:83]\n"
    "v_mfma_f32_16x16x32_bf16 v[80:83], a[176:179], a[248:251], v[80:83]\n"
    "buffer_load_dwordx4 a[68:71], v224, s[60:63], s89 offen offset:1024 sc1\n"
    "v_mfma_f32_16x16x32_bf16 v[88:91], a[160:163], a[236:239], v[88:91]\n"
    "s_add_u32 s89, s89, 131072\n"
    "s_cmp_eq_u32 s89, 1835008\n"
    "s_cselect_b32 s89, 0, s89\n"
    "v_mfma_f32_16x16x32_bf16 v[88:91], a[176:179], a[252:255], v[88:91]\n"
    "buffer_load_dwordx4 a[72:75], v224, s[60:63], s23 offen offset:0 sc1\n"
    "v_mfma_f32_16x16x32_bf16 v[68:71], a[164:167], a[224:227], v[68:71]\n"
    "v_mfma_f32_16x16x32_bf16 v[68:71], a[180:183], a[240:243], v[68:71]\n"
    "buffer_load_dwordx4 a[76:79], v224, s[60:63], s23 offen offset:1024 sc1\n"
    "v_mfma_f32_16x16x32_bf16 v[76:79], a[164:167], a[228:231], v[76:79]\n"
    "s_add_u32 s23, s89, 4096\n"
    "v_mfma_f32_16x16x32_bf16 v[76:79], a[180:183], a[244:247], v[76:79]\n"
    "buffer_load_dwordx4 v233, s[40:43], s88 offen offset:3072 lds\n"
    "v_mfma_f32_16x16x32_bf16 v[84:87], a[164:167], a[232:235], v[84:87]\n"
    "s_add_u32 s88, s88, 32768\n"
    "s_add_u32 m0, s94, 12288\n"
    "v_mfma_f32_16x16x32_bf16 v[84:87], a[180:183], a[248:251], v[84:87]\n"
    "buffer_load_dwordx4 a[80:83], v224, s[60:63], s89 offen offset:0 sc1\n"
    "v_mfma_f32_16x16x32_bf16 v[92:95], a[164:167], a[236:239], v[92:95]\n"
    "s_waitcnt vmcnt(30)\n"
    "ds_read_b64_tr_b16 a[128:129], v228 offset:0\n"
    "s_waitcnt vmcnt(29)\n"
    "ds_read_b64_tr_b16 a[130:131], v228 offset:2048\n"
    "s_waitcnt vmcnt(26)\n"
    "ds_read_b64_tr_b16 a[144:145], v228 offset:4096\n"
    "v_mfma_f32_16x16x32_bf16 v[92:95], a[180:183], a[252:255], v[92:95]\n"
    "buffer_load_dwordx4 v233, s[28:31], s88 offen offset:0 lds\n"
    "s_waitcnt lgkmcnt(3)\n"
    "v_mfma_f32_16x16x32_bf16 v[96:99], a[168:171], a[224:227], v[96:99]\n"
    "s_mov_b64 s[28:29], s[18:19]\n"
    "s_waitcnt vmcnt(26)\n"
    "ds_read_b64_tr_b16 a[146:147], v228 offset:6144\n"
    "ds_read_b64_tr_b16 a[132:133], v229 offset:0\n"
    "v_mfma_f32_16x16x32_bf16 v[96:99], a[184:187], a[240:243], v[96:99]\n"
    "buffer_load_dwordx4 v233, s[36:39], s88 offen offset:2048 lds\n"
    "v_mfma_f32_16x16x32_bf16 v[104:107], a[168:171], a[228:231], v[104:107]\n"
    "s_mov_b32 s30, s85\n"
    "ds_read_b64_tr_b16 a[134:135], v229 offset:2048\n"
    "ds_read_b64_tr_b16 a[148:149], v229 offset:4096\n"
    "v_mfma_f32_16x16x32_bf16 v[104:107], a[184:187], a[244:247], v[104:107]\n"
    "buffer_load_dwordx4 v233, s[32:35], s88 offen offset:1024 lds\n"
    "v_mfma_f32_16x16x32_bf16 v[112:115], a[168:171], a[232:235], v[112:115]\n"
    "s_mov_b32 s31, 0x20000\n"
    "ds_read_b64_tr_b16 a[150:151], v229 offset:6144\n"
    "ds_read_b64_tr_b16 a[136:137], v228 offset:512\n"
    "v_mfma_f32_16x16x32_bf16 v[112:115], a[184:187], a[248:251], v[112:115]\n"
    "buffer_load_dwordx4 v233, s[40:43], s88 offen offset:3072 lds\n"
    "v_mfma_f32_16x16x32_bf16 v[120:123], a[168:171], a[236:239], v[120:123]\n"
    "s_add_u32 m0, s94, 16384\n"
    "ds_read_b64_tr_b16 a[138:139], v228 offset:2560\n"
    "ds_read_b64_tr_b16 a[152:153], v228 offset:4608\n"
    "v_mfma_f32_16x16x32_bf16 v[120:123], a[184:187], a[252:255], v[120:123]\n"
    "ds_read_b64_tr_b16 a[154:155], v228 offset:6656\n"
    "ds_read_b64_tr_b16 a[140:141], v229 offset:512\n"
    "ds_read_b64_tr_b16 a[142:143], v229 offset:2560\n"
    "v_mfma_f32_16x16x32_bf16 v[100:103], a[172:175], a[224:227], v[100:103]\n"
    "ds_read_b64_tr_b16 a[156:157], v229 offset:4608\n"
    "s_waitcnt lgkmcnt(8)\n"
    "ds_read_b64_tr_b16 a[158:159], v229 offset:6656\n"
    "v_mfma_f32_16x16x32_bf16 v[100:103], a[188:191], a[240:243], v[100:103]\n"
    "buffer_load_dwordx4 v232, s[44:47], s91 offen offset:0 lds\n"
    "v_mfma_f32_16x16x32_bf16 v[108:111], a[172:175], a[228:231], v[108:111]\n"
    "s_add_u32 s28, s28, s99\n"
    "s_addc_u32 s29, s29, 0\n"
    "s_and_b32 s29, s29, 0xffff\n"
    "v_mfma_f32_16x16x32_bf16 v[108:111], a[188:191], a[244:247], v[108:111]\n"
    "buffer_load_dwordx4 v232, s[52:55], s91 offen offset:2048 lds\n"
    "v_mfma_f32_16x16x32_bf16 v[116:119], a[172:175], a[232:235], v[116:119]\n"
    "v_mfma_f32_16x16x32_bf16 v[116:119], a[188:191], a[248:251], v[116:119]\n"
    "buffer_load_dwordx4 v232, s[48:51], s91 offen offset:1024 lds\n"
    "v_mfma_f32_16x16x32_bf16 v[124:127], a[172:175], a[236:239], v[124:127]\n"
    "v_mfma_f32_16x16x32_bf16 v[124:127], a[188:191], a[252:255], v[124:127]\n"
    "buffer_load_dwordx4 v232, s[56:59], s91 offen offset:3072 lds\n"
    "v_mfma_f32_16x16x32_bf16 v[0:3], a[128:131], a[192:195], v[0:3]\n"
    "s_add_u32 m0, s94, 20480\n"
    "v_mfma_f32_16x16x32_bf16 v[0:3], a[144:147], a[208:211], v[0:3]\n"
    "buffer_load_dwordx4 a[84:87], v224, s[60:63], s89 offen offset:1024 sc1\n"
    "v_mfma_f32_16x16x32_bf16 v[8:11], a[128:131], a[196:199], v[8:11]\n"
    "s_add_u32 s91, s91, 131072\n"
    "s_cmp_eq_u32 s91, 1835008\n"
    "s_cselect_b32 s91, 0, s91\n"
    "v_mfma_f32_16x16x32_bf16 v[8:11], a[144:147], a[212:215], v[8:11]\n"
    "buffer_load_dwordx4 v232, s[44:47], s91 offen offset:0 lds\n"
    "v_mfma_f32_16x16x32_bf16 v[16:19], a[128:131], a[200:203], v[16:19]\n"
    "s_add_u32 s89, s89, 131072\n"
    "s_cmp_eq_u32 s89, 1835008\n"
    "s_cselect_b32 s89, 0, s89\n"
    "v_cvt_pk_bf16_f32 v0, v0, v1\n"
    "v_cvt_pk_bf16_f32 v1, v2, v3\n"
    "v_mfma_f32_16x16x32_bf16 v[16:19], a[144:147], a[216:219], v[16:19]\n"
    "buffer_load_dwordx4 v232, s[52:55], s91 offen offset:2048 lds\n"
    "v_mfma_f32_16x16x32_bf16 v[24:27], a[128:131], a[204:207], v[24:27]\n"
    "v_cvt_pk_bf16_f32 v8, v8, v9\n"
    "v_cvt_pk_bf16_f32 v9, v10, v11\n"
    "v_mfma_f32_16x16x32_bf16 v[24:27], a[144:147], a[220:223], v[24:27]\n"
    "buffer_load_dwordx4 v232, s[48:51], s91 offen offset:1024 lds\n"
    "v_mfma_f32_16x16x32_bf16 v[4:7], a[132:135], a[192:195], v[4:7]\n"
    "s_waitcnt lgkmcnt(0)\n"
    "v_mfma_f32_16x16x32_bf16 v[4:7], a[148:151], a[208:211], v[4:7]\n"
    "v_cvt_pk_bf16_f32 v16, v16, v17\n"
    "v_cvt_pk_bf16_f32 v17, v18, v19\n"
    "v_mfma_f32_16x16x32_bf16 v[12:15], a[132:135], a[196:199], v[12:15]\n"
    "buffer_load_dwordx4 v232, s[56:59], s91 offen offset:3072 lds\n"
    "v_cvt_pk_bf16_f32 v24, v24, v25\n"
    "v_cvt_pk_bf16_f32 v25, v26, v27\n"
    "v_mfma_f32_16x16x32_bf16 v[12:15], a[148:151], a[212:215], v[12:15]\n"
    "buffer_load_dwordx4 a[88:91], v224, s[60:63], s23 offen offset:0 sc1\n"
    "v_cvt_pk_bf16_f32 v2, v4, v5\n"
    "v_cvt_pk_bf16_f32 v3, v6, v7\n"
    "v_mfma_f32_16x16x32_bf16 v[20:23], a[132:135], a[200:203], v[20:23]\n"
    "buffer_load_dwordx4 a[92:95], v224, s[60:63], s23 offen offset:1024 sc1\n"
    "v_mfma_f32_16x16x32_bf16 v[20:23], a[148:151], a[216:219], v[20:23]\n"
    "s_add_u32 m0, s94, 0\n"
    "s_waitcnt vmcnt(25)\n"
    "ds_read_b64_tr_b16 a[160:161], v228 offset:8192\n"
    "s_waitcnt vmcnt(16)\n"
    "ds_read_b64_tr_b16 a[162:163], v228 offset:10240\n"
    "v_cvt_pk_bf16_f32 v10, v12, v13\n"
    "v_cvt_pk_bf16_f32 v11, v14, v15\n"
    "v_mfma_f32_16x16x32_bf16 v[28:31], a[132:135], a[204:207], v[28:31]\n"
    "s_add_u32 s23, s89, 4096\n"
    "s_waitcnt vmcnt(12)\n"
    "ds_read_b64_tr_b16 a[176:177], v228 offset:12288\n"
    "s_waitcnt vmcnt(11)\n"
    "ds_read_b64_tr_b16 a[178:179], v228 offset:14336\n"
    "v_mfma_f32_16x16x32_bf16 v[28:31], a[148:151], a[220:223], v[28:31]\n"
    "ds_read_b64_tr_b16 a[164:165], v229 offset:8192\n"
    "ds_read_b64_tr_b16 a[166:167], v229 offset:10240\n"
    "ds_read_b64_tr_b16 a[180:181], v229 offset:12288\n"
    "v_cvt_pk_bf16_f32 v18, v20, v21\n"
    "v_cvt_pk_bf16_f32 v19, v22, v23\n"
    "v_mfma_f32_16x16x32_bf16 v[32:35], a[136:139], a[192:195], v[32:35]\n"
    "ds_read_b64_tr_b16 a[182:183], v229 offset:14336\n"
    "ds_read_b64_tr_b16 a[168:169], v228 offset:8704\n"
    "v_mfma_f32_16x16x32_bf16 v[32:35], a[152:155], a[208:211], v[32:35]\n"
    "ds_read_b64_tr_b16 a[170:171], v228 offset:10752\n"
    "ds_read_b64_tr_b16 a[184:185], v228 offset:12800\n"
    "ds_read_b64_tr_b16 a[186:187], v228 offset:14848\n"
    "v_cvt_pk_bf16_f32 v26, v28, v29\n"
    "v_cvt_pk_bf16_f32 v27, v30, v31\n"
    "v_mfma_f32_16x16x32_bf16 v[40:43], a[136:139], a[196:199], v[40:43]\n"
    "ds_read_b64_tr_b16 a[172:173], v229 offset:8704\n"
    "ds_read_b64_tr_b16 a[174:175], v229 offset:10752\n"
    "v_mfma_f32_16x16x32_bf16 v[40:43], a[152:155], a[212:215], v[40:43]\n"
    "ds_read_b64_tr_b16 a[188:189], v229 offset:12800\n"
    "s_waitcnt lgkmcnt(10)\n"
    "ds_read_b64_tr_b16 a[190:191], v229 offset:14848\n"
    "v_mfma_f32_16x16x32_bf16 v[48:51], a[136:139], a[200:203], v[48:51]\n"
    "v_cvt_pk_bf16_f32 v32, v32, v33\n"
    "v_cvt_pk_bf16_f32 v33, v34, v35\n"
    "v_mfma_f32_16x16x32_bf16 v[48:51], a[152:155], a[216:219], v[48:51]\n"
    "s_add_u32 s91, s91, 131072\n"
    "s_cmp_eq_u32 s91, 1835008\n"
    "s_cselect_b32 s91, 0, s91\n"
    "v_mfma_f32_16x16x32_bf16 v[56:59], a[136:139], a[204:207], v[56:59]\n"
    "buffer_load_dwordx4 v232, s[44:47], s91 offen offset:0 lds\n"
    "v_cvt_pk_bf16_f32 v40, v40, v41\n"
    "v_cvt_pk_bf16_f32 v41, v42, v43\n"
    "v_mfma_f32_16x16x32_bf16 v[56:59], a[152:155], a[220:223], v[56:59]\n"
    "buffer_load_dwordx4 v232, s[52:55], s91 offen offset:2048 lds\n"
    "v_cvt_pk_bf16_f32 v48, v48, v49\n"
    "v_cvt_pk_bf16_f32 v49, v50, v51\n"
    "v_mfma_f32_16x16x32_bf16 v[36:39], a[140:143], a[192:195], v[36:39]\n"
    "buffer_load_dwordx4 v232, s[48:51], s91 offen offset:1024 lds\n"
    "v_mfma_f32_16x16x32_bf16 v[36:39], a[156:159], a[208:211], v[36:39]\n"
    "v_mfma_f32_16x16x32_bf16 v[44:47], a[140:143], a[196:199], v[44:47]\n"
    "s_nop 0\n"
    "v_cvt_pk_bf16_f32 v56, v56, v57\n"
    "v_cvt_pk_bf16_f32 v57, v58, v59\n"
    "v_mfma_f32_16x16x32_bf16 v[44:47], a[156:159], a[212:215], v[44:47]\n"
    "buffer_load_dwordx4 v232, s[56:59], s91 offen offset:3072 lds\n"
    "v_mfma_f32_16x16x32_bf16 v[52:55], a[140:143], a[200:203], v[52:55]\n"
    "s_add_u32 m0, s94, 4096\n"
    "v_mfma_f32_16x16x32_bf16 v[52:55], a[156:159], a[216:219], v[52:55]\n"
    "v_cvt_pk_bf16_f32 v34, v36, v37\n"
    "v_cvt_pk_bf16_f32 v35, v38, v39\n"
    "v_mfma_f32_16x16x32_bf16 v[60:63], a[140:143], a[204:207], v[60:63]\n"
    "s_add_u32 s91, s91, 131072\n"
    "s_cmp_eq_u32 s91, 1835008\n"
    "s_cselect_b32 s91, 0, s91\n"
    "v_cvt_pk_bf16_f32 v42, v44, v45\n"
    "v_cvt_pk_bf16_f32 v43, v46, v47\n"
    "v_mfma_f32_16x16x32_bf16 v[60:63], a[156:159], a[220:223], v[60:63]\n"
    "buffer_load_dwordx4 v232, s[44:47], s91 offen offset:0 lds\n"
    "v_mfma_f32_16x16x32_bf16 v[64:67], a[160:163], a[192:195], v[64:67]\n"
    "v_cvt_pk_bf16_f32 v50, v52, v53\n"
    "buffer_store_dwordx4 v[0:3], v224, s[68:71], s22 offen offset:0 sc1\n"
    "s_mov_b32 s22, 8192\n"
    "v_cvt_pk_bf16_f32 v51, v54, v55\n"
    "v_mfma_f32_16x16x32_bf16 v[64:67], a[176:179], a[208:211], v[64:67]\n"
    "s_nop 0\n"
    "v_cvt_pk_bf16_f32 v58, v60, v61\n"
    "v_cvt_pk_bf16_f32 v59, v62, v63\n"
    "v_mfma_f32_16x16x32_bf16 v[72:75], a[160:163], a[196:199], v[72:75]\n"
    "buffer_load_dwordx4 v232, s[52:55], s91 offen offset:2048 lds\n"
    "v_mfma_f32_16x16x32_bf16 v[72:75], a[176:179], a[212:215], v[72:75]\n"
    "v_mfma_f32_16x16x32_bf16 v[80:83], a[160:163], a[200:203], v[80:83]\n"
    "buffer_store_dwordx4 v[8:11], v224, s[68:71], s22 offen offset:0 sc1\n"
    "s_mov_b32 s22, 16384\n"
    "v_cvt_pk_bf16_f32 v64, v64, v65\n"
    "v_cvt_pk_bf16_f32 v65, v66, v67\n"
    "v_mfma_f32_16x16x32_bf16 v[80:83], a[176:179], a[216:219], v[80:83]\n"
    "buffer_load_dwordx4 v232, s[48:51], s91 offen offset:1024 lds\n"
    "s_nop 0\n"
    "v_cvt_pk_bf16_f32 v72, v72, v73\n"
    "v_cvt_pk_bf16_f32 v73, v74, v75\n"
    "v_mfma_f32_16x16x32_bf16 v[88:91], a[160:163], a[204:207], v[88:91]\n"
    "buffer_load_dwordx4 v232, s[56:59], s91 offen offset:3072 lds\n"
    "v_mfma_f32_16x16x32_bf16 v[88:91], a[176:179], a[220:223], v[88:91]\n"
    "s_add_u32 m0, s94, 8192\n"
    "s_waitcnt lgkmcnt(0)\n"
    "v_mfma_f32_16x16x32_bf16 v[68:71], a[164:167], a[192:195], v[68:71]\n"
    "buffer_store_dwordx4 v[16:19], v224, s[68:71], s22 offen offset:0 sc1\n"
    "s_mov_b32 s22, 24576\n"
    "v_cvt_pk_bf16_f32 v80, v80, v81\n"
    "v_cvt_pk_bf16_f32 v81, v82, v83\n"
    "v_mfma_f32_16x16x32_bf16 v[68:71], a[180:183], a[208:211], v[68:71]\n"
    "s_add_u32 s91, s91, 131072\n"
    "s_cmp_eq_u32 s91, 1835008\n"
    "s_cselect_b32 s91, 0, s91\n"
    "v_mfma_f32_16x16x32_bf16 v[76:79], a[164:167], a[196:199], v[76:79]\n"
    "v_cvt_pk_bf16_f32 v88, v88, v89\n"
    "v_cvt_pk_bf16_f32 v89, v90, v91\n"
    "v_mfma_f32_16x16x32_bf16 v[76:79], a[180:183], a[212:215], v[76:79]\n"
    "buffer_load_dwordx4 v232, s[44:47], s91 offen offset:0 lds\n"
    "v_mfma_f32_16x16x32_bf16 v[84:87], a[164:167], a[200:203], v[84:87]\n"
    "s_waitcnt vmcnt(20)\n"
    "ds_read_b64_tr_b16 a[96:97], v228 offset:16384\n"
    "s_waitcnt vmcnt(19)\n"
    "ds_read_b64_tr_b16 a[98:99], v228 offset:18432\n"
    "buffer_store_dwordx4 v[24:27], v224, s[68:71], s22 offen offset:0 sc1\n"
    "s_mov_b32 s22, 32768\n"
    "v_cvt_pk_bf16_f32 v66, v68, v69\n"
    "v_cvt_pk_bf16_f32 v67, v70, v71\n"
    "v_mfma_f32_16x16x32_bf16 v[84:87], a[180:183], a[216:219], v[84:87]\n"
    "buffer_load_dwordx4 v232, s[52:55], s91 offen offset:2048 lds\n"
    "v_cvt_pk_bf16_f32 v74, v76, v77\n"
    "v_cvt_pk_bf16_f32 v75, v78, v79\n"
    "v_mfma_f32_16x16x32_bf16 v[92:95], a[164:167], a[204:207], v[92:95]\n"
    "buffer_load_dwordx4 v232, s[48:51], s91 offen offset:1024 lds\n"
    "v_mfma_f32_16x16x32_bf16 v[92:95], a[180:183], a[220:223], v[92:95]\n"
    "ds_read_b64_tr_b16 a[100:101], v229 offset:16384\n"
    "ds_read_b64_tr_b16 a[102:103], v229 offset:18432\n"
    "ds_read_b64_tr_b16 a[104:105], v228 offset:16896\n"
    "v_cvt_pk_bf16_f32 v82, v84, v85\n"
    "v_cvt_pk_bf16_f32 v83, v86, v87\n"
    "v_mfma_f32_16x16x32_bf16 v[96:99], a[168:171], a[192:195], v[96:99]\n"
    "buffer_store_dwordx4 v[32:35], v224, s[68:71], s22 offen offset:0 sc1\n"
    "s_mov_b32 s22, 40960\n"
    "v_mfma_f32_16x16x32_bf16 v[96:99], a[184:187], a[208:211], v[96:99]\n"
    "ds_read_b64_tr_b16 a[106:107], v228 offset:18944\n"
    "ds_read_b64_tr_b16 a[108:109], v229 offset:16896\n"
    "v_cvt_pk_bf16_f32 v90, v92, v93\n"
    "v_cvt_pk_bf16_f32 v91, v94, v95\n"
    "v_mfma_f32_16x16x32_bf16 v[104:107], a[168:171], a[196:199], v[104:107]\n"
    "buffer_load_dwordx4 v232, s[56:59], s91 offen offset:3072 lds\n"
    "v_mfma_f32_16x16x32_bf16 v[104:107], a[184:187], a[212:215], v[104:107]\n"
    "s_add_u32 m0, s94, 12288\n"
    "v_mov_b32 v84, 0\n"
    "v_mov_b32 v85, 0\n"
    "v_mfma_f32_16x16x32_bf16 v[112:115], a[168:171], a[200:203], v[112:115]\n"
    "buffer_store_dwordx4 v[40:43], v224, s[68:71], s22 offen offset:0 sc1\n"
    "s_mov_b32 s22, 49152\n"
    "v_cvt_pk_bf16_f32 v96, v96, v97\n"
    "v_cvt_pk_bf16_f32 v97, v98, v99\n"
    "v_mfma_f32_16x16x32_bf16 v[112:115], a[184:187], a[216:219], v[112:115]\n"
    "ds_read_b64_tr_b16 a[110:111], v229 offset:18944\n"
    "v_mfma_f32_16x16x32_bf16 v[120:123], a[168:171], a[204:207], v[120:123]\n"
    "v_cvt_pk_bf16_f32 v104, v104, v105\n"
    "v_cvt_pk_bf16_f32 v105, v106, v107\n"
    "v_mfma_f32_16x16x32_bf16 v[120:123], a[184:187], a[220:223], v[120:123]\n"
    "s_add_u32 s91, s91, 131072\n"
    "s_cmp_eq_u32 s91, 1835008\n"
    "s_cselect_b32 s91, 0, s91\n"
    "v_mfma_f32_16x16x32_bf16 v[100:103], a[172:175], a[192:195], v[100:103]\n"
    "buffer_store_dwordx4 v[48:51], v224, s[68:71], s22 offen offset:0 sc1\n"
    "s_mov_b32 s22, 57344\n"
    "v_cvt_pk_bf16_f32 v112, v112, v113\n"
    "v_cvt_pk_bf16_f32 v113, v114, v115\n"
    "v_mfma_f32_16x16x32_bf16 v[100:103], a[188:191], a[208:211], v[100:103]\n"
    "buffer_load_dwordx4 v232, s[44:47], s91 offen offset:0 lds\n"
    "v_cvt_pk_bf16_f32 v120, v120, v121\n"
    "v_cvt_pk_bf16_f32 v121, v122, v123\n"
    "v_mfma_f32_16x16x32_bf16 v[108:111], a[172:175], a[196:199], v[108:111]\n"
    "buffer_load_dwordx4 v232, s[52:55], s91 offen offset:2048 lds\n"
    "v_mfma_f32_16x16x32_bf16 v[108:111], a[188:191], a[212:215], v[108:111]\n"
    "v_mfma_f32_16x16x32_bf16 v[116:119], a[172:175], a[200:203], v[116:119]\n"
    "buffer_store_dwordx4 v[56:59], v224, s[68:71], s22 offen offset:0 sc1\n"
    "s_mov_b32 s22, 65536\n"
    "v_cvt_pk_bf16_f32 v98, v100, v101\n"
    "v_cvt_pk_bf16_f32 v99, v102, v103\n"
    "v_mfma_f32_16x16x32_bf16 v[116:119], a[188:191], a[216:219], v[116:119]\n"
    "buffer_load_dwordx4 v232, s[48:51], s91 offen offset:1024 lds\n"
    "s_nop 0\n"
    "v_cvt_pk_bf16_f32 v106, v108, v109\n"
    "v_cvt_pk_bf16_f32 v107, v110, v111\n"
    "v_mfma_f32_16x16x32_bf16 v[124:127], a[172:175], a[204:207], v[124:127]\n"
    "buffer_load_dwordx4 v232, s[56:59], s91 offen offset:3072 lds\n"
    "v_mfma_f32_16x16x32_bf16 v[124:127], a[188:191], a[220:223], v[124:127]\n"
    "s_add_u32 m0, s94, 16384\n"
    "s_waitcnt vmcnt(27)\n"
    "ds_read_b64_tr_b16 a[112:113], v228 offset:20480\n"
    "s_waitcnt vmcnt(26)\n"
    "ds_read_b64_tr_b16 a[114:115], v228 offset:22528\n"
    "v_cvt_pk_bf16_f32 v114, v116, v117\n"
    "v_cvt_pk_bf16_f32 v115, v118, v119\n"
    "s_waitcnt lgkmcnt(2)\n"
    "v_mfma_f32_16x16x32_bf16 v[128:131], a[0:3], a[96:99], 0\n"
    "buffer_store_dwordx4 v[64:67], v224, s[68:71], s22 offen offset:0 sc1\n"
    "s_mov_b32 s22, 73728\n"
    "v_mfma_f32_16x16x32_bf16 v[144:147], a[0:3], a[100:103], 0\n"
    "ds_read_b64_tr_b16 a[116:117], v229 offset:20480\n"
    "ds_read_b64_tr_b16 a[118:119], v229 offset:22528\n"
    "v_cvt_pk_bf16_f32 v122, v124, v125\n"
    "v_cvt_pk_bf16_f32 v123, v126, v127\n"
    "v_mfma_f32_16x16x32_bf16 v[160:163], a[0:3], a[104:107], 0\n"
    "ds_read_b64_tr_b16 a[120:121], v228 offset:20992\n"
    "ds_read_b64_tr_b16 a[122:123], v228 offset:23040\n"
    "v_mfma_f32_16x16x32_bf16 v[176:179], a[0:3], a[108:111], 0\n"
    "buffer_load_dwordx4 a[0:3], v224, s[60:63], s89 offen offset:0 sc1\n"
    "v_mfma_f32_16x16x32_bf16 v[132:135], a[4:7], a[96:99], 0\n"
    "ds_read_b64_tr_b16 a[124:125], v229 offset:20992\n"
    "ds_read_b64_tr_b16 a[126:127], v229 offset:23040\n"
    "buffer_store_dwordx4 v[72:75], v224, s[68:71], s22 offen offset:0 sc1\n"
    "s_mov_b32 s22, 81920\n"
    "v_mfma_f32_16x16x32_bf16 v[148:151], a[4:7], a[100:103], 0\n"
    "s_add_u32 s91, s91, 131072\n"
    "s_cmp_eq_u32 s91, 1835008\n"
    "s_cselect_b32 s91, 0, s91\n"
    "v_mfma_f32_16x16x32_bf16 v[164:167], a[4:7], a[104:107], 0\n"
    "buffer_load_dwordx4 v232, s[44:47], s91 offen offset:0 lds\n"
    "v_mfma_f32_16x16x32_bf16 v[180:183], a[4:7], a[108:111], 0\n"
    "v_mfma_f32_16x16x32_bf16 v[136:139], a[8:11], a[96:99], 0\n"
    "buffer_store_dwordx4 v[80:83], v224, s[68:71], s22 offen offset:0 sc1\n"
    "s_mov_b32 s22, 90112\n"
    "v_mfma_f32_16x16x32_bf16 v[152:155], a[8:11], a[100:103], 0\n"
    "v_cndmask_b32 v82, 0, v240, vcc\n"
    "v_cmp_eq_u32 vcc, 1, v241\n"
    "v_mfma_f32_16x16x32_bf16 v[168:171], a[8:11], a[104:107], 0\n"
    "buffer_load_dwordx4 v232, s[52:55], s91 offen offset:2048 lds\n"
    "v_mfma_f32_16x16x32_bf16 v[184:187], a[8:11], a[108:111], 0\n"
    "v_cndmask_b32 v83, 0, v240, vcc\n"
    "v_mov_b32 v80, 0\n"
    "v_mfma_f32_16x16x32_bf16 v[140:143], a[12:15], a[96:99], 0\n"
    "buffer_store_dwordx4 v[88:91], v224, s[68:71], s22 offen offset:0 sc1\n"
    "s_mov_b32 s22, 98304\n"
    "v_mfma_f32_16x16x32_bf16 v[156:159], a[12:15], a[100:103], 0\n"
    "s_waitcnt vmcnt(28)\n"
    "ds_read_b64_tr_b16 a[96:97], v228 offset:0\n"
    "s_waitcnt vmcnt(27)\n"
    "ds_read_b64_tr_b16 a[98:99], v228 offset:2048\n"
    "ds_read_b64_tr_b16 a[100:101], v229 offset:0\n"
    "v_mfma_f32_16x16x32_bf16 v[172:175], a[12:15], a[104:107], 0\n"
    "buffer_load_dwordx4 v232, s[48:51], s91 offen offset:1024 lds\n"
    "v_mfma_f32_16x16x32_bf16 v[188:191], a[12:15], a[108:111], 0\n"
    "ds_read_b64_tr_b16 a[102:103], v229 offset:2048\n"
    "ds_read_b64_tr_b16 a[104:105], v228 offset:512\n"
    "ds_read_b64_tr_b16 a[106:107], v228 offset:2560\n"
    "s_waitcnt lgkmcnt(6)\n"
    "v_mfma_f32_16x16x32_bf16 v[128:131], a[16:19], a[112:115], v[128:131]\n"
    "buffer_store_dwordx4 v[96:99], v224, s[68:71], s22 offen offset:0 sc1\n"
    "s_mov_b32 s22, 106496\n"
    "v_mfma_f32_16x16x32_bf16 v[144:147], a[16:19], a[116:119], v[144:147]\n"
    "ds_read_b64_tr_b16 a[108:109], v229 offset:512\n"
    "ds_read_b64_tr_b16 a[110:111], v229 offset:2560\n"
    "v_mfma_f32_16x16x32_bf16 v[160:163], a[16:19], a[120:123], v[160:163]\n"
    "buffer_load_dwordx4 v232, s[56:59], s91 offen offset:3072 lds\n"
    "v_mfma_f32_16x16x32_bf16 v[176:179], a[16:19], a[124:127], v[176:179]\n"
    "s_add_u32 m0, s94, 20480\n"
    "v_mov_b32 v81, 0\n"
    "v_mfma_f32_16x16x32_bf16 v[132:135], a[20:23], a[112:115], v[132:135]\n"
    "buffer_store_dwordx4 v[104:107], v224, s[68:71], s22 offen offset:0 sc1\n"
    "s_mov_b32 s22, 114688\n"
    "v_mfma_f32_16x16x32_bf16 v[148:151], a[20:23], a[116:119], v[148:151]\n"
    "s_add_u32 s91, s91, 131072\n"
    "s_cmp_eq_u32 s91, 1835008\n"
    "s_cselect_b32 s91, 0, s91\n"
    "v_mfma_f32_16x16x32_bf16 v[164:167], a[20:23], a[120:123], v[164:167]\n"
    "buffer_load_dwordx4 a[4:7], v224, s[60:63], s89 offen offset:1024 sc1\n"
    "v_mfma_f32_16x16x32_bf16 v[180:183], a[20:23], a[124:127], v[180:183]\n"
    "s_add_u32 s89, s89, 131072\n"
    "s_cmp_eq_u32 s89, 1835008\n"
    "s_cselect_b32 s89, 0, s89\n"
    "v_mfma_f32_16x16x32_bf16 v[136:139], a[24:27], a[112:115], v[136:139]\n"
    "buffer_store_dwordx4 v[112:115], v224, s[68:71], s22 offen offset:0 sc1\n"
    "s_mov_b32 s22, 122880\n"
    "v_mfma_f32_16x16x32_bf16 v[152:155], a[24:27], a[116:119], v[152:155]\n"
    "v_mfma_f32_16x16x32_bf16 v[168:171], a[24:27], a[120:123], v[168:171]\n"
    "buffer_load_dwordx4 v232, s[44:47], s91 offen offset:0 lds\n"
    "v_mfma_f32_16x16x32_bf16 v[184:187], a[24:27], a[124:127], v[184:187]\n"
    "v_mfma_f32_16x16x32_bf16 v[140:143], a[28:31], a[112:115], v[140:143]\n"
    "buffer_store_dwordx4 v[120:123], v224, s[68:71], s22 offen offset:0 sc1\n"
    "v_mfma_f32_16x16x32_bf16 v[156:159], a[28:31], a[116:119], v[156:159]\n"
    "s_lshl_b32 s22, s97, 7\n"
    "s_waitcnt vmcnt(30)\n"
    "ds_read_b64_tr_b16 a[112:113], v228 offset:4096\n"
    "s_waitcnt vmcnt(29)\n"
    "ds_read_b64_tr_b16 a[114:115], v228 offset:6144\n"
    "v_mfma_f32_16x16x32_bf16 v[172:175], a[28:31], a[120:123], v[172:175]\n"
    "buffer_load_dwordx4 v232, s[52:55], s91 offen offset:2048 lds\n"
    "v_mfma_f32_16x16x32_bf16 v[188:191], a[28:31], a[124:127], v[188:191]\n"
    "s_add_u32 s22, s22, 1024\n"
    "ds_read_b64_tr_b16 a[116:117], v229 offset:4096\n"
    "ds_read_b64_tr_b16 a[118:119], v229 offset:6144\n"
    "s_waitcnt lgkmcnt(4)\n"
    "v_mfma_f32_16x16x32_bf16 v[128:131], a[32:35], a[96:99], v[128:131]\n"
    "ds_read_b64_tr_b16 a[120:121], v228 offset:4608\n"
    "ds_read_b64_tr_b16 a[122:123], v228 offset:6656\n"
    "ds_read_b64_tr_b16 a[124:125], v229 offset:4608\n"
    "v_mfma_f32_16x16x32_bf16 v[144:147], a[32:35], a[100:103], v[144:147]\n"
    "buffer_load_dwordx4 a[8:11], v224, s[60:63], s23 offen offset:0 sc1\n"
    "v_mfma_f32_16x16x32_bf16 v[160:163], a[32:35], a[104:107], v[160:163]\n"
    "v_mfma_f32_16x16x32_bf16 v[176:179], a[32:35], a[108:111], v[176:179]\n"
    "ds_read_b64_tr_b16 a[126:127], v229 offset:6656\n"
    "v_mfma_f32_16x16x32_bf16 v[132:135], a[36:39], a[96:99], v[132:135]\n"
    "buffer_load_dwordx4 a[12:15], v224, s[60:63], s23 offen offset:1024 sc1\n"
    "v_mfma_f32_16x16x32_bf16 v[148:151], a[36:39], a[100:103], v[148:151]\n"
    "s_add_u32 s23, s89, 4096\n"
    "v_mfma_f32_16x16x32_bf16 v[164:167], a[36:39], a[104:107], v[164:167]\n"
    "buffer_load_dwordx4 v232, s[48:51], s91 offen offset:1024 lds\n"
    "v_mfma_f32_16x16x32_bf16 v[180:183], a[36:39], a[108:111], v[180:183]\n"
    "v_mfma_f32_16x16x32_bf16 v[136:139], a[40:43], a[96:99], v[136:139]\n"
    "buffer_load_dwordx4 v232, s[56:59], s91 offen offset:3072 lds\n"
    "v_mfma_f32_16x16x32_bf16 v[152:155], a[40:43], a[100:103], v[152:155]\n"
    "s_add_u32 m0, s94, 0\n"
    "v_mfma_f32_16x16x32_bf16 v[168:171], a[40:43], a[104:107], v[168:171]\n"
    "buffer_load_dwordx4 a[16:19], v224, s[60:63], s89 offen offset:0 sc1\n"
    "v_mfma_f32_16x16x32_bf16 v[184:187], a[40:43], a[108:111], v[184:187]\n"
    "s_add_u32 s91, s91, 131072\n"
    "s_cmp_eq_u32 s91, 1835008\n"
    "s_cselect_b32 s91, 0, s91\n"
    "v_mfma_f32_16x16x32_bf16 v[140:143], a[44:47], a[96:99], v[140:143]\n"
    "buffer_load_dwordx4 a[20:23], v224, s[60:63], s89 offen offset:1024 sc1\n"
    "v_mfma_f32_16x16x32_bf16 v[156:159], a[44:47], a[100:103], v[156:159]\n"
    "s_waitcnt vmcnt(31)\n"
    "ds_read_b64_tr_b16 a[96:97], v228 offset:8192\n"
    "s_waitcnt vmcnt(29)\n"
    "ds_read_b64_tr_b16 a[98:99], v228 offset:10240\n"
    "ds_read_b64_tr_b16 a[100:101], v229 offset:8192\n"
    "v_mfma_f32_16x16x32_bf16 v[172:175], a[44:47], a[104:107], v[172:175]\n"
    "buffer_load_dwordx4 a[24:27], v224, s[60:63], s23 offen offset:0 sc1\n"
    "v_mfma_f32_16x16x32_bf16 v[188:191], a[44:47], a[108:111], v[188:191]\n"
    "ds_read_b64_tr_b16 a[102:103], v229 offset:10240\n"
    "ds_read_b64_tr_b16 a[104:105], v228 offset:8704\n"
    "ds_read_b64_tr_b16 a[106:107], v228 offset:10752\n"
    "s_waitcnt vmcnt(8)\n"
    "s_barrier\n"
    "s_waitcnt lgkmcnt(6)\n"
    "v_mfma_f32_16x16x32_bf16 v[128:131], a[48:51], a[112:115], v[128:131]\n"
    "buffer_load_dwordx4 a[28:31], v224, s[60:63], s23 offen offset:1024 sc1\n"
    "v_mfma_f32_16x16x32_bf16 v[144:147], a[48:51], a[116:119], v[144:147]\n"
    "s_mov_b64 exec, s[8:9]\n"
    "buffer_atomic_add v226, v225, s[64:67], s22 offen\n"
    "s_mov_b64 exec, -1\n"
    "v_mfma_f32_16x16x32_bf16 v[160:163], a[48:51], a[120:123], v[160:163]\n"
    "s_mov_b32 s22, 0\n"
    "ds_read_b64_tr_b16 a[108:109], v229 offset:8704\n"
    "ds_read_b64_tr_b16 a[110:111], v229 offset:10752\n"
    "v_mfma_f32_16x16x32_bf16 v[176:179], a[48:51], a[124:127], v[176:179]\n"
    "buffer_load_dwordx4 v232, s[44:47], s91 offen offset:0 lds\n"
    "v_mfma_f32_16x16x32_bf16 v[132:135], a[52:55], a[112:115], v[132:135]\n"
    "s_add_u32 s89, s89, 131072\n"
    "s_cmp_eq_u32 s89, 1835008\n"
    "s_cselect_b32 s89, 0, s89\n"
    "v_mfma_f32_16x16x32_bf16 v[148:151], a[52:55], a[116:119], v[148:151]\n"
    "buffer_load_dwordx4 v232, s[52:55], s91 offen offset:2048 lds\n"
    "v_mfma_f32_16x16x32_bf16 v[164:167], a[52:55], a[120:123], v[164:167]\n"
    "s_add_u32 s23, s89, 4096\n"
    "v_mfma_f32_16x16x32_bf16 v[180:183], a[52:55], a[124:127], v[180:183]\n"
    "buffer_load_dwordx4 a[32:35], v224, s[60:63], s89 offen offset:0 sc1\n"
    "v_mfma_f32_16x16x32_bf16 v[136:139], a[56:59], a[112:115], v[136:139]\n"
    "v_mfma_f32_16x16x32_bf16 v[152:155], a[56:59], a[116:119], v[152:155]\n"
    "buffer_load_dwordx4 a[36:39], v224, s[60:63], s89 offen offset:1024 sc1\n"
    "v_mfma_f32_16x16x32_bf16 v[168:171], a[56:59], a[120:123], v[168:171]\n"
    "s_add_u32 s89, s89, 131072\n"
    "s_cmp_eq_u32 s89, 1835008\n"
    "s_cselect_b32 s89, 0, s89\n"
    "v_mfma_f32_16x16x32_bf16 v[184:187], a[56:59], a[124:127], v[184:187]\n"
    "buffer_load_dwordx4 v232, s[48:51], s91 offen offset:1024 lds\n"
    "v_mfma_f32_16x16x32_bf16 v[140:143], a[60:63], a[112:115], v[140:143]\n"
    "ds_read_b64_tr_b16 a[112:113], v228 offset:12288\n"
    "ds_read_b64_tr_b16 a[114:115], v228 offset:14336\n"
    "v_mfma_f32_16x16x32_bf16 v[156:159], a[60:63], a[116:119], v[156:159]\n"
    "buffer_load_dwordx4 v232, s[56:59], s91 offen offset:3072 lds\n"
    "v_mfma_f32_16x16x32_bf16 v[172:175], a[60:63], a[120:123], v[172:175]\n"
    "s_add_u32 m0, s94, 4096\n"
    "ds_read_b64_tr_b16 a[116:117], v229 offset:12288\n"
    "ds_read_b64_tr_b16 a[118:119], v229 offset:14336\n"
    "v_mfma_f32_16x16x32_bf16 v[188:191], a[60:63], a[124:127], v[188:191]\n"
    "buffer_load_dwordx4 a[40:43], v224, s[60:63], s23 offen offset:0 sc1\n"
    "s_waitcnt lgkmcnt(4)\n"
    "v_mfma_f32_16x16x32_bf16 v[128:131], a[64:67], a[96:99], v[128:131]\n"
    "ds_read_b64_tr_b16 a[120:121], v228 offset:12800\n"
    "ds_read_b64_tr_b16 a[122:123], v228 offset:14848\n"
    "ds_read_b64_tr_b16 a[124:125], v229 offset:12800\n"
    "v_mfma_f32_16x16x32_bf16 v[144:147], a[64:67], a[100:103], v[144:147]\n"
    "buffer_load_dwordx4 a[44:47], v224, s[60:63], s23 offen offset:1024 sc1\n"
    "v_mfma_f32_16x16x32_bf16 v[160:163], a[64:67], a[104:107], v[160:163]\n"
    "s_add_u32 s23, s89, 4096\n"
    "v_mfma_f32_16x16x32_bf16 v[176:179], a[64:67], a[108:111], v[176:179]\n"
    "ds_read_b64_tr_b16 a[126:127], v229 offset:14848\n"
    "v_mfma_f32_16x16x32_bf16 v[132:135], a[68:71], a[96:99], v[132:135]\n"
    "buffer_load_dwordx4 a[48:51], v224, s[60:63], s89 offen offset:0 sc1\n"
    "v_mfma_f32_16x16x32_bf16 v[148:151], a[68:71], a[100:103], v[148:151]\n"
    "s_add_u32 s91, s91, 131072\n"
    "s_cmp_eq_u32 s91, 1835008\n"
    "s_cselect_b32 s91, 0, s91\n"
    "v_mfma_f32_16x16x32_bf16 v[164:167], a[68:71], a[104:107], v[164:167]\n"
    "buffer_load_dwordx4 a[52:55], v224, s[60:63], s89 offen offset:1024 sc1\n"
    "v_mfma_f32_16x16x32_bf16 v[180:183], a[68:71], a[108:111], v[180:183]\n"
    "s_add_u32 s89, s89, 131072\n"
    "s_cmp_eq_u32 s89, 1835008\n"
    "s_cselect_b32 s89, 0, s89\n"
    "v_mfma_f32_16x16x32_bf16 v[136:139], a[72:75], a[96:99], v[136:139]\n"
    "buffer_load_dwordx4 v232, s[44:47], s91 offen offset:0 lds\n"
    "v_mfma_f32_16x16x32_bf16 v[152:155], a[72:75], a[100:103], v[152:155]\n"
    "v_mfma_f32_16x16x32_bf16 v[168:171], a[72:75], a[104:107], v[168:171]\n"
    "buffer_load_dwordx4 v232, s[52:55], s91 offen offset:2048 lds\n"
    "v_mfma_f32_16x16x32_bf16 v[184:187], a[72:75], a[108:111], v[184:187]\n"
    "v_mfma_f32_16x16x32_bf16 v[140:143], a[76:79], a[96:99], v[140:143]\n"
    "buffer_load_dwordx4 a[56:59], v224, s[60:63], s23 offen offset:0 sc1\n"
    "v_mfma_f32_16x16x32_bf16 v[156:159], a[76:79], a[100:103], v[156:159]\n"
    "ds_read_b64_tr_b16 a[96:97], v228 offset:16384\n"
    "ds_read_b64_tr_b16 a[98:99], v228 offset:18432\n"
    "ds_read_b64_tr_b16 a[100:101], v229 offset:16384\n"
    "v_mfma_f32_16x16x32_bf16 v[172:175], a[76:79], a[104:107], v[172:175]\n"
    "buffer_load_dwordx4 a[60:63], v224, s[60:63], s23 offen offset:1024 sc1\n"
    "v_mfma_f32_16x16x32_bf16 v[188:191], a[76:79], a[108:111], v[188:191]\n"
    "s_add_u32 s23, s89, 4096\n"
    "ds_read_b64_tr_b16 a[102:103], v229 offset:18432\n"
    "ds_read_b64_tr_b16 a[104:105], v228 offset:16896\n"
    "s_waitcnt lgkmcnt(5)\n"
    "v_mfma_f32_16x16x32_bf16 v[128:131], a[80:83], a[112:115], v[128:131]\n"
    "buffer_load_dwordx4 v232, s[48:51], s91 offen offset:1024 lds\n"
    "v_mfma_f32_16x16x32_bf16 v[144:147], a[80:83], a[116:119], v[144:147]\n"
    "ds_read_b64_tr_b16 a[106:107], v228 offset:18944\n"
    "ds_read_b64_tr_b16 a[108:109], v229 offset:16896\n"
    "ds_read_b64_tr_b16 a[110:111], v229 offset:18944\n"
    "v_mfma_f32_16x16x32_bf16 v[160:163], a[80:83], a[120:123], v[160:163]\n"
    "buffer_load_dwordx4 v232, s[56:59], s91 offen offset:3072 lds\n"
    "v_mfma_f32_16x16x32_bf16 v[176:179], a[80:83], a[124:127], v[176:179]\n"
    "s_add_u32 m0, s94, 8192\n"
    "v_mfma_f32_16x16x32_bf16 v[132:135], a[84:87], a[112:115], v[132:135]\n"
    "buffer_load_dwordx4 a[64:67], v224, s[60:63], s89 offen offset:0 sc1\n"
    "v_mfma_f32_16x16x32_bf16 v[148:151], a[84:87], a[116:119], v[148:151]\n"
    "s_add_u32 s91, s91, 131072\n"
    "s_cmp_eq_u32 s91, 1835008\n"
    "s_cselect_b32 s91, 0, s91\n"
    "v_mfma_f32_16x16x32_bf16 v[164:167], a[84:87], a[120:123], v[164:167]\n"
    "buffer_load_dwordx4 a[68:71], v224, s[60:63], s89 offen offset:1024 sc1\n"
    "v_mfma_f32_16x16x32_bf16 v[180:183], a[84:87], a[124:127], v[180:183]\n"
    "s_add_u32 s89, s89, 131072\n"
    "s_cmp_eq_u32 s89, 1835008\n"
    "s_cselect_b32 s89, 0, s89\n"
    "v_mfma_f32_16x16x32_bf16 v[136:139], a[88:91], a[112:115], v[136:139]\n"
    "buffer_load_dwordx4 a[72:75], v224, s[60:63], s23 offen offset:0 sc1\n"
    "v_mfma_f32_16x16x32_bf16 v[152:155], a[88:91], a[116:119], v[152:155]\n"
    "v_mfma_f32_16x16x32_bf16 v[168:171], a[88:91], a[120:123], v[168:171]\n"
    "buffer_load_dwordx4 a[76:79], v224, s[60:63], s23 offen offset:1024 sc1\n"
    "v_mfma_f32_16x16x32_bf16 v[184:187], a[88:91], a[124:127], v[184:187]\n"
    "s_add_u32 s23, s89, 4096\n"
    "v_mfma_f32_16x16x32_bf16 v[140:143], a[92:95], a[112:115], v[140:143]\n"
    "buffer_load_dwordx4 v232, s[44:47], s91 offen offset:0 lds\n"
    "v_mfma_f32_16x16x32_bf16 v[156:159], a[92:95], a[116:119], v[156:159]\n"
    "s_waitcnt vmcnt(26)\n"
    "ds_read_b64_tr_b16 a[112:113], v228 offset:20480\n"
    "s_waitcnt vmcnt(25)\n"
    "ds_read_b64_tr_b16 a[114:115], v228 offset:22528\n"
    "ds_read_b64_tr_b16 a[116:117], v229 offset:20480\n"
    "v_mfma_f32_16x16x32_bf16 v[172:175], a[92:95], a[120:123], v[172:175]\n"
    "buffer_load_dwordx4 v232, s[52:55], s91 offen offset:2048 lds\n"
    "v_mfma_f32_16x16x32_bf16 v[188:191], a[92:95], a[124:127], v[188:191]\n"
    "ds_read_b64_tr_b16 a[118:119], v229 offset:22528\n"
    "ds_read_b64_tr_b16 a[120:121], v228 offset:20992\n"
    "ds_read_b64_tr_b16 a[122:123], v228 offset:23040\n"
    "s_waitcnt lgkmcnt(6)\n"
    "v_mfma_f32_16x16x32_bf16 v[128:131], a[0:3], a[96:99], v[128:131]\n"
    "buffer_load_dwordx4 a[80:83], v224, s[60:63], s89 offen offset:0 sc1\n"
    "v_mfma_f32_16x16x32_bf16 v[144:147], a[0:3], a[100:103], v[144:147]\n"
    "ds_read_b64_tr_b16 a[124:125], v229 offset:20992\n"
    "ds_read_b64_tr_b16 a[126:127], v229 offset:23040\n"
    "v_mfma_f32_16x16x32_bf16 v[160:163], a[0:3], a[104:107], v[160:163]\n"
    "buffer_load_dwordx4 a[84:87], v224, s[60:63], s89 offen offset:1024 sc1\n"
    "v_mfma_f32_16x16x32_bf16 v[176:179], a[0:3], a[108:111], v[176:179]\n"
    "s_add_u32 s89, s89, 131072\n"
    "s_cmp_eq_u32 s89, 1835008\n"
    "s_cselect_b32 s89, 0, s89\n"
    "v_mfma_f32_16x16x32_bf16 v[132:135], a[4:7], a[96:99], v[132:135]\n"
    "buffer_load_dwordx4 v232, s[48:51], s91 offen offset:1024 lds\n"
    "v_mfma_f32_16x16x32_bf16 v[148:151], a[4:7], a[100:103], v[148:151]\n"
    "v_mfma_f32_16x16x32_bf16 v[164:167], a[4:7], a[104:107], v[164:167]\n"
    "buffer_load_dwordx4 v232, s[56:59], s91 offen offset:3072 lds\n"
    "v_mfma_f32_16x16x32_bf16 v[180:183], a[4:7], a[108:111], v[180:183]\n"
    "s_add_u32 m0, s94, 12288\n"
    "v_mfma_f32_16x16x32_bf16 v[136:139], a[8:11], a[96:99], v[136:139]\n"
    "buffer_load_dwordx4 a[88:91], v224, s[60:63], s23 offen offset:0 sc1\n"
    "v_mfma_f32_16x16x32_bf16 v[152:155], a[8:11], a[100:103], v[152:155]\n"
    "s_add_u32 s91, s91, 131072\n"
    "s_cmp_eq_u32 s91, 1835008\n"
    "s_cselect_b32 s91, 0, s91\n"
    "v_mfma_f32_16x16x32_bf16 v[168:171], a[8:11], a[104:107], v[168:171]\n"
    "buffer_load_dwordx4 a[92:95], v224, s[60:63], s23 offen offset:1024 sc1\n"
    "v_mfma_f32_16x16x32_bf16 v[184:187], a[8:11], a[108:111], v[184:187]\n"
    "s_add_u32 s23, s89, 4096\n"
    "v_mfma_f32_16x16x32_bf16 v[140:143], a[12:15], a[96:99], v[140:143]\n"
    "buffer_load_dwordx4 a[0:3], v224, s[60:63], s89 offen offset:0 sc1\n"
    "v_mfma_f32_16x16x32_bf16 v[156:159], a[12:15], a[100:103], v[156:159]\n"
    "s_waitcnt vmcnt(24)\n"
    "ds_read_b64_tr_b16 a[96:97], v228 offset:0\n"
    "s_waitcnt vmcnt(23)\n"
    "ds_read_b64_tr_b16 a[98:99], v228 offset:2048\n"
    "ds_read_b64_tr_b16 a[100:101], v229 offset:0\n"
    "v_mfma_f32_16x16x32_bf16 v[172:175], a[12:15], a[104:107], v[172:175]\n"
    "buffer_load_dwordx4 a[4:7], v224, s[60:63], s89 offen offset:1024 sc1\n"
    "v_mfma_f32_16x16x32_bf16 v[188:191], a[12:15], a[108:111], v[188:191]\n"
    "ds_read_b64_tr_b16 a[102:103], v229 offset:2048\n"
    "ds_read_b64_tr_b16 a[104:105], v228 offset:512\n"
    "ds_read_b64_tr_b16 a[106:107], v228 offset:2560\n"
    "buffer_load_dword v227, v225, s[64:67], s20 offen offset:0 sc1\n"
    "s_waitcnt lgkmcnt(6)\n"
    "v_mfma_f32_16x16x32_bf16 v[128:131], a[16:19], a[112:115], v[128:131]\n"
    "ds_read_b64_tr_b16 a[108:109], v229 offset:512\n"
    "ds_read_b64_tr_b16 a[110:111], v229 offset:2560\n"
    "v_mfma_f32_16x16x32_bf16 v[144:147], a[16:19], a[116:119], v[144:147]\n"
    "buffer_load_dwordx4 a[8:11], v224, s[60:63], s23 offen offset:0 sc1\n"
    "v_mfma_f32_16x16x32_bf16 v[160:163], a[16:19], a[120:123], v[160:163]\n"
    "s_add_u32 s89, s89, 131072\n"
    "s_cmp_eq_u32 s89, 1835008\n"
    "s_cselect_b32 s89, 0, s89\n"
    "v_mfma_f32_16x16x32_bf16 v[176:179], a[16:19], a[124:127], v[176:179]\n"
    "buffer_load_dwordx4 a[12:15], v224, s[60:63], s23 offen offset:1024 sc1\n"
    "v_mfma_f32_16x16x32_bf16 v[132:135], a[20:23], a[112:115], v[132:135]\n"
    "s_add_u32 s23, s89, 4096\n"
    "v_mfma_f32_16x16x32_bf16 v[148:151], a[20:23], a[116:119], v[148:151]\n"
    "buffer_load_dwordx4 v232, s[44:47], s91 offen offset:0 lds\n"
    "v_mfma_f32_16x16x32_bf16 v[164:167], a[20:23], a[120:123], v[164:167]\n"
    "v_mfma_f32_16x16x32_bf16 v[180:183], a[20:23], a[124:127], v[180:183]\n"
    "buffer_load_dwordx4 v232, s[52:55], s91 offen offset:2048 lds\n"
    "v_mfma_f32_16x16x32_bf16 v[136:139], a[24:27], a[112:115], v[136:139]\n"
    "v_mfma_f32_16x16x32_bf16 v[152:155], a[24:27], a[116:119], v[152:155]\n"
    "buffer_load_dwordx4 v232, s[48:51], s91 offen offset:1024 lds\n"
    "v_mfma_f32_16x16x32_bf16 v[168:171], a[24:27], a[120:123], v[168:171]\n"
    "v_mfma_f32_16x16x32_bf16 v[184:187], a[24:27], a[124:127], v[184:187]\n"
    "buffer_load_dwordx4 a[24:27], v224, s[60:63], s23 offen offset:0 sc1\n"
    "v_mfma_f32_16x16x32_bf16 v[140:143], a[28:31], a[112:115], v[140:143]\n"
    "s_waitcnt vmcnt(22)\n"
    "ds_read_b64_tr_b16 a[112:113], v228 offset:4096\n"
    "s_waitcnt vmcnt(21)\n"
    "ds_read_b64_tr_b16 a[114:115], v228 offset:6144\n"
    "v_mfma_f32_16x16x32_bf16 v[156:159], a[28:31], a[116:119], v[156:159]\n"
    "buffer_load_dwordx4 v232, s[56:59], s91 offen offset:3072 lds\n"
    "v_mfma_f32_16x16x32_bf16 v[172:175], a[28:31], a[120:123], v[172:175]\n"
    "s_add_u32 m0, s94, 16384\n"
    "ds_read_b64_tr_b16 a[116:117], v229 offset:4096\n"
    "ds_read_b64_tr_b16 a[118:119], v229 offset:6144\n"
    "v_mfma_f32_16x16x32_bf16 v[188:191], a[28:31], a[124:127], v[188:191]\n"
    "buffer_load_dwordx4 a[28:31], v224, s[60:63], s23 offen offset:1024 sc1\n"
    "s_waitcnt vmcnt(8)\n"
    "v_readfirstlane_b32 s23, v227\n"
    "s_nop 1\n"
    "s_cmp_lt_u32 s23, 8\n"
    "s_cbranch_scc1 K2_SP_P\n"
    "K2_SR_P:\n"
    "s_waitcnt lgkmcnt(4)\n"
    "v_mfma_f32_16x16x32_bf16 v[128:131], a[32:35], a[96:99], v[128:131]\n"
    "buffer_load_dwordx4 a[16:19], v224, s[60:63], s89 offen offset:0 sc1\n"
    "v_mfma_f32_16x16x32_bf16 v[144:147], a[32:35], a[100:103], v[144:147]\n"
    "ds_read_b64_tr_b16 a[120:121], v228 offset:4608\n"
    "ds_read_b64_tr_b16 a[122:123], v228 offset:6656\n"
    "ds_read_b64_tr_b16 a[124:125], v229 offset:4608\n"
    "v_mfma_f32_16x16x32_bf16 v[160:163], a[32:35], a[104:107], v[160:163]\n"
    "buffer_load_dwordx4 a[20:23], v224, s[60:63], s89 offen offset:1024 sc1\n"
    "v_mfma_f32_16x16x32_bf16 v[176:179], a[32:35], a[108:111], v[176:179]\n"
    "s_add_u32 s91, s91, 131072\n"
    "s_cmp_eq_u32 s91, 1835008\n"
    "s_cselect_b32 s91, 0, s91\n"
    "v_mfma_f32_16x16x32_bf16 v[132:135], a[36:39], a[96:99], v[132:135]\n"
    "buffer_load_dwordx4 v232, s[44:47], s91 offen offset:0 lds\n"
    "v_mfma_f32_16x16x32_bf16 v[148:151], a[36:39], a[100:103], v[148:151]\n"
    "ds_read_b64_tr_b16 a[126:127], v229 offset:6656\n"
    "v_mfma_f32_16x16x32_bf16 v[164:167], a[36:39], a[104:107], v[164:167]\n"
    "buffer_load_dwordx4 v232, s[52:55], s91 offen offset:2048 lds\n"
    "v_mfma_f32_16x16x32_bf16 v[180:183], a[36:39], a[108:111], v[180:183]\n"
    "s_add_u32 s89, s89, 131072\n"
    "s_cmp_eq_u32 s89, 1835008\n"
    "s_cselect_b32 s89, 0, s89\n"
    "v_mfma_f32_16x16x32_bf16 v[136:139], a[40:43], a[96:99], v[136:139]\n"
    "buffer_load_dwordx4 v232, s[48:51], s91 offen offset:1024 lds\n"
    "v_mfma_f32_16x16x32_bf16 v[152:155], a[40:43], a[100:103], v[152:155]\n"
    "v_mfma_f32_16x16x32_bf16 v[168:171], a[40:43], a[104:107], v[168:171]\n"
    "buffer_load_dwordx4 v232, s[56:59], s91 offen offset:3072 lds\n"
    "v_mfma_f32_16x16x32_bf16 v[184:187], a[40:43], a[108:111], v[184:187]\n"
    "s_add_u32 m0, s94, 20480\n"
    "v_mfma_f32_16x16x32_bf16 v[140:143], a[44:47], a[96:99], v[140:143]\n"
    "ds_read_b64_tr_b16 a[96:97], v228 offset:8192\n"
    "ds_read_b64_tr_b16 a[98:99], v228 offset:10240\n"
    "v_mfma_f32_16x16x32_bf16 v[156:159], a[44:47], a[100:103], v[156:159]\n"
    "ds_read_b64_tr_b16 a[100:101], v229 offset:8192\n"
    "ds_read_b64_tr_b16 a[102:103], v229 offset:10240\n"
    "v_mfma_f32_16x16x32_bf16 v[172:175], a[44:47], a[104:107], v[172:175]\n"
    "ds_read_b64_tr_b16 a[104:105], v228 offset:8704\n"
    "ds_read_b64_tr_b16 a[106:107], v228 offset:10752\n"
    "v_mfma_f32_16x16x32_bf16 v[188:191], a[44:47], a[108:111], v[188:191]\n"
    "ds_read_b64_tr_b16 a[108:109], v229 offset:8704\n"
    "s_waitcnt lgkmcnt(7)\n"
    "ds_read_b64_tr_b16 a[110:111], v229 offset:10752\n"
    "v_mfma_f32_16x16x32_bf16 v[128:131], a[48:51], a[112:115], v[128:131]\n"
    "s_add_u32 s91, s91, 131072\n"
    "s_cmp_eq_u32 s91, 1835008\n"
    "s_cselect_b32 s91, 0, s91\n"
    "v_mfma_f32_16x16x32_bf16 v[144:147], a[48:51], a[116:119], v[144:147]\n"
    "buffer_load_dwordx4 v232, s[44:47], s91 offen offset:0 lds\n"
    "v_mfma_f32_16x16x32_bf16 v[160:163], a[48:51], a[120:123], v[160:163]\n"
    "v_mfma_f32_16x16x32_bf16 v[176:179], a[48:51], a[124:127], v[176:179]\n"
    "buffer_load_dwordx4 v232, s[52:55], s91 offen offset:2048 lds\n"
    "v_mfma_f32_16x16x32_bf16 v[132:135], a[52:55], a[112:115], v[132:135]\n"
    "v_mfma_f32_16x16x32_bf16 v[148:151], a[52:55], a[116:119], v[148:151]\n"
    "buffer_load_dwordx4 v232, s[48:51], s91 offen offset:1024 lds\n"
    "v_mfma_f32_16x16x32_bf16 v[164:167], a[52:55], a[120:123], v[164:167]\n"
    "v_mfma_f32_16x16x32_bf16 v[180:183], a[52:55], a[124:127], v[180:183]\n"
    "buffer_load_dwordx4 v232, s[56:59], s91 offen offset:3072 lds\n"
    "v_mfma_f32_16x16x32_bf16 v[136:139], a[56:59], a[112:115], v[136:139]\n"
    "v_mfma_f32_16x16x32_bf16 v[152:155], a[56:59], a[116:119], v[152:155]\n"
    "v_mfma_f32_16x16x32_bf16 v[168:171], a[56:59], a[120:123], v[168:171]\n"
    "v_mfma_f32_16x16x32_bf16 v[184:187], a[56:59], a[124:127], v[184:187]\n"
    "v_mfma_f32_16x16x32_bf16 v[140:143], a[60:63], a[112:115], v[140:143]\n"
    "s_waitcnt vmcnt(13)\n"
    "ds_read_b64_tr_b16 a[112:113], v228 offset:12288\n"
    "s_waitcnt vmcnt(11)\n"
    "ds_read_b64_tr_b16 a[114:115], v228 offset:14336\n"
    "v_mfma_f32_16x16x32_bf16 v[156:159], a[60:63], a[116:119], v[156:159]\n"
    "ds_read_b64_tr_b16 a[116:117], v229 offset:12288\n"
    "ds_read_b64_tr_b16 a[118:119], v229 offset:14336\n"
    "v_mfma_f32_16x16x32_bf16 v[172:175], a[60:63], a[120:123], v[172:175]\n"
    "ds_read_b64_tr_b16 a[120:121], v228 offset:12800\n"
    "ds_read_b64_tr_b16 a[122:123], v228 offset:14848\n"
    "v_mfma_f32_16x16x32_bf16 v[188:191], a[60:63], a[124:127], v[188:191]\n"
    "ds_read_b64_tr_b16 a[124:125], v229 offset:12800\n"
    "s_waitcnt lgkmcnt(7)\n"
    "ds_read_b64_tr_b16 a[126:127], v229 offset:14848\n"
    "v_mfma_f32_16x16x32_bf16 v[128:131], a[64:67], a[96:99], v[128:131]\n"
    "v_mfma_f32_16x16x32_bf16 v[144:147], a[64:67], a[100:103], v[144:147]\n"
    "v_mfma_f32_16x16x32_bf16 v[160:163], a[64:67], a[104:107], v[160:163]\n"
    "v_mfma_f32_16x16x32_bf16 v[176:179], a[64:67], a[108:111], v[176:179]\n"
    "v_mfma_f32_16x16x32_bf16 v[132:135], a[68:71], a[96:99], v[132:135]\n"
    "buffer_load_dwordx4 v[0:3], v224, s[76:79], s22 offen offset:0 sc1\n"
    "v_mfma_f32_16x16x32_bf16 v[148:151], a[68:71], a[100:103], v[148:151]\n"
    "s_mov_b32 s22, 65536\n"
    "v_mfma_f32_16x16x32_bf16 v[164:167], a[68:71], a[104:107], v[164:167]\n"
    "v_mfma_f32_16x16x32_bf16 v[180:183], a[68:71], a[108:111], v[180:183]\n"
    "v_mfma_f32_16x16x32_bf16 v[136:139], a[72:75], a[96:99], v[136:139]\n"
    "v_mfma_f32_16x16x32_bf16 v[152:155], a[72:75], a[100:103], v[152:155]\n"
    "v_mfma_f32_16x16x32_bf16 v[168:171], a[72:75], a[104:107], v[168:171]\n"
    "v_mfma_f32_16x16x32_bf16 v[184:187], a[72:75], a[108:111], v[184:187]\n"
    "v_mfma_f32_16x16x32_bf16 v[140:143], a[76:79], a[96:99], v[140:143]\n"
    "s_waitcnt vmcnt(6)\n"
    "ds_read_b64_tr_b16 a[96:97], v228 offset:16384\n"
    "s_waitcnt vmcnt(5)\n"
    "ds_read_b64_tr_b16 a[98:99], v228 offset:18432\n"
    "v_mfma_f32_16x16x32_bf16 v[156:159], a[76:79], a[100:103], v[156:159]\n"
    "ds_read_b64_tr_b16 a[100:101], v229 offset:16384\n"
    "ds_read_b64_tr_b16 a[102:103], v229 offset:18432\n"
    "v_mfma_f32_16x16x32_bf16 v[172:175], a[76:79], a[104:107], v[172:175]\n"
    "ds_read_b64_tr_b16 a[104:105], v228 offset:16896\n"
    "ds_read_b64_tr_b16 a[106:107], v228 offset:18944\n"
    "v_mfma_f32_16x16x32_bf16 v[188:191], a[76:79], a[108:111], v[188:191]\n"
    "ds_read_b64_tr_b16 a[108:109], v229 offset:16896\n"
    "s_waitcnt lgkmcnt(7)\n"
    "ds_read_b64_tr_b16 a[110:111], v229 offset:18944\n"
    "v_mfma_f32_16x16x32_bf16 v[128:131], a[80:83], a[112:115], v[128:131]\n"
    "v_mfma_f32_16x16x32_bf16 v[144:147], a[80:83], a[116:119], v[144:147]\n"
    "buffer_load_dwordx4 v[4:7], v224, s[76:79], s22 offen offset:0 sc1\n"
    "v_mfma_f32_16x16x32_bf16 v[160:163], a[80:83], a[120:123], v[160:163]\n"
    "s_mov_b32 s22, 0\n"
    "v_mfma_f32_16x16x32_bf16 v[176:179], a[80:83], a[124:127], v[176:179]\n"
    "v_mfma_f32_16x16x32_bf16 v[132:135], a[84:87], a[112:115], v[132:135]\n"
    "buffer_load_dwordx4 v[8:11], v224, s[76:79], s22 offen offset:1024 sc1\n"
    "v_mfma_f32_16x16x32_bf16 v[148:151], a[84:87], a[116:119], v[148:151]\n"
    "s_mov_b32 s22, 65536\n"
    "v_mfma_f32_16x16x32_bf16 v[164:167], a[84:87], a[120:123], v[164:167]\n"
    "v_mfma_f32_16x16x32_bf16 v[180:183], a[84:87], a[124:127], v[180:183]\n"
    "v_mfma_f32_16x16x32_bf16 v[136:139], a[88:91], a[112:115], v[136:139]\n"
    "buffer_load_dwordx4 v[12:15], v224, s[76:79], s22 offen offset:1024 sc1\n"
    "v_mfma_f32_16x16x32_bf16 v[152:155], a[88:91], a[116:119], v[152:155]\n"
    "s_mov_b32 s22, 0\n"
    "v_mfma_f32_16x16x32_bf16 v[168:171], a[88:91], a[120:123], v[168:171]\n"
    "v_mfma_f32_16x16x32_bf16 v[184:187], a[88:91], a[124:127], v[184:187]\n"
    "v_mfma_f32_16x16x32_bf16 v[140:143], a[92:95], a[112:115], v[140:143]\n"
    "buffer_load_dwordx4 v[16:19], v224, s[76:79], s22 offen offset:2048 sc1\n"
    "v_mfma_f32_16x16x32_bf16 v[156:159], a[92:95], a[116:119], v[156:159]\n"
    "s_mov_b32 s22, 65536\n"
    "s_waitcnt vmcnt(6)\n"
    "ds_read_b64_tr_b16 a[112:113], v228 offset:20480\n"
    "s_waitcnt vmcnt(5)\n"
    "ds_read_b64_tr_b16 a[114:115], v228 offset:22528\n"
    "v_mfma_f32_16x16x32_bf16 v[172:175], a[92:95], a[120:123], v[172:175]\n"
    "ds_read_b64_tr_b16 a[116:117], v229 offset:20480\n"
    "ds_read_b64_tr_b16 a[118:119], v229 offset:22528\n"
    "ds_read_b64_tr_b16 a[120:121], v228 offset:20992\n"
    "v_mfma_f32_16x16x32_bf16 v[188:191], a[92:95], a[124:127], v[188:191]\n"
    "buffer_load_dwordx4 v[20:23], v224, s[76:79], s22 offen offset:2048 sc1\n"
    "s_waitcnt lgkmcnt(5)\n"
    "v_mfma_f32_16x16x32_bf16 v[128:131], a[0:3], a[96:99], v[128:131]\n"
    "s_mov_b32 s22, 0\n"
    "ds_read_b64_tr_b16 a[122:123], v228 offset:23040\n"
    "ds_read_b64_tr_b16 a[124:125], v229 offset:20992\n"
    "v_mfma_f32_16x16x32_bf16 v[144:147], a[0:3], a[100:103], v[144:147]\n"
    "buffer_load_dwordx4 v[24:27], v224, s[76:79], s22 offen offset:3072 sc1\n"
    "v_mfma_f32_16x16x32_bf16 v[160:163], a[0:3], a[104:107], v[160:163]\n"
    "s_mov_b32 s22, 65536\n"
    "v_mfma_f32_16x16x32_bf16 v[176:179], a[0:3], a[108:111], v[176:179]\n"
    "v_mfma_f32_16x16x32_bf16 v[132:135], a[4:7], a[96:99], v[132:135]\n"
    "ds_read_b64_tr_b16 a[126:127], v229 offset:23040\n"
    "v_mfma_f32_16x16x32_bf16 v[148:151], a[4:7], a[100:103], v[148:151]\n"
    "v_mfma_f32_16x16x32_bf16 v[164:167], a[4:7], a[104:107], v[164:167]\n"
    "v_mfma_f32_16x16x32_bf16 v[180:183], a[4:7], a[108:111], v[180:183]\n"
    "v_mfma_f32_16x16x32_bf16 v[136:139], a[8:11], a[96:99], v[136:139]\n"
    "v_mfma_f32_16x16x32_bf16 v[152:155], a[8:11], a[100:103], v[152:155]\n"
    "buffer_load_dwordx4 v[28:31], v224, s[76:79], s22 offen offset:3072 sc1\n"
    "v_mfma_f32_16x16x32_bf16 v[168:171], a[8:11], a[104:107], v[168:171]\n"
    "s_xor_b32 s22, %1, 1\n"
    "s_lshl_b32 s22, s22, 14\n"
    "v_add_u32 v237, s22, v224\n"
    "v_mfma_f32_16x16x32_bf16 v[184:187], a[8:11], a[108:111], v[184:187]\n"
    "s_xor_b32 s22, %1, 2\n"
    "s_lshl_b32 s22, s22, 14\n"
    "v_add_u32 v238, s22, v224\n"
    "v_mfma_f32_16x16x32_bf16 v[140:143], a[12:15], a[96:99], v[140:143]\n"
    "s_xor_b32 s22, %1, 3\n"
    "s_lshl_b32 s22, s22, 14\n"
    "v_add_u32 v239, s22, v224\n"
    "v_mfma_f32_16x16x32_bf16 v[156:159], a[12:15], a[100:103], v[156:159]\n"
    "v_mfma_f32_16x16x32_bf16 v[172:175], a[12:15], a[104:107], v[172:175]\n"
    "v_mfma_f32_16x16x32_bf16 v[188:191], a[12:15], a[108:111], v[188:191]\n"
    "s_waitcnt lgkmcnt(0)\n"
    "v_mfma_f32_16x16x32_bf16 v[128:131], a[16:19], a[112:115], v[128:131]\n"
    "v_mfma_f32_16x16x32_bf16 v[144:147], a[16:19], a[116:119], v[144:147]\n"
    "v_mfma_f32_16x16x32_bf16 v[160:163], a[16:19], a[120:123], v[160:163]\n"
    "v_mfma_f32_16x16x32_bf16 v[176:179], a[16:19], a[124:127], v[176:179]\n"
    "v_mfma_f32_16x16x32_bf16 v[132:135], a[20:23], a[112:115], v[132:135]\n"
    "v_mfma_f32_16x16x32_bf16 v[148:151], a[20:23], a[116:119], v[148:151]\n"
    "s_nop 3\n"
    "ds_write_b128 v236, v[144:147] offset:0\n"
    "ds_write_b128 v236, v[160:163] offset:4096\n"
    "v_mfma_f32_16x16x32_bf16 v[164:167], a[20:23], a[120:123], v[164:167]\n"
    "ds_write_b128 v236, v[176:179] offset:8192\n"
    "v_mfma_f32_16x16x32_bf16 v[180:183], a[20:23], a[124:127], v[180:183]\n"
    "v_mfma_f32_16x16x32_bf16 v[136:139], a[24:27], a[112:115], v[136:139]\n"
    "ds_write_b128 v236, v[148:151] offset:1024\n"
    "v_mfma_f32_16x16x32_bf16 v[152:155], a[24:27], a[116:119], v[152:155]\n"
    "v_mfma_f32_16x16x32_bf16 v[168:171], a[24:27], a[120:123], v[168:171]\n"
    "s_nop 1\n"
    "ds_write_b128 v236, v[164:167] offset:5120\n"
    "s_nop 0\n"
    "ds_write_b128 v236, v[180:183] offset:9216\n"
    "v_mfma_f32_16x16x32_bf16 v[184:187], a[24:27], a[124:127], v[184:187]\n"
    "v_mfma_f32_16x16x32_bf16 v[140:143], a[28:31], a[112:115], v[140:143]\n"
    "ds_write_b128 v236, v[152:155] offset:2048\n"
    "v_mfma_f32_16x16x32_bf16 v[156:159], a[28:31], a[116:119], v[156:159]\n"
    "ds_write_b128 v236, v[168:171] offset:6144\n"
    "v_mfma_f32_16x16x32_bf16 v[172:175], a[28:31], a[120:123], v[172:175]\n"
    "v_mfma_f32_16x16x32_bf16 v[188:191], a[28:31], a[124:127], v[188:191]\n"
    "s_nop 1\n"
    "ds_write_b128 v236, v[184:187] offset:10240\n"
    "s_nop 1\n"
    "ds_write_b128 v236, v[156:159] offset:3072\n"
    "s_nop 0\n"
    "ds_write_b128 v236, v[172:175] offset:7168\n"
    "ds_write_b128 v236, v[188:191] offset:11264\n"
    "s_waitcnt lgkmcnt(0)\n"
    "s_barrier\n"
    "ds_read_b128 v[144:147], v237 offset:0\n"
    "ds_read_b128 v[148:151], v237 offset:1024\n"
    "ds_read_b128 v[152:155], v237 offset:2048\n"
    "ds_read_b128 v[156:159], v237 offset:3072\n"
    "ds_read_b128 v[160:163], v238 offset:4096\n"
    "ds_read_b128 v[164:167], v238 offset:5120\n"
    "ds_read_b128 v[168:171], v238 offset:6144\n"
    "ds_read_b128 v[172:175], v238 offset:7168\n"
    "ds_read_b128 v[176:179], v239 offset:8192\n"
    "ds_read_b128 v[180:183], v239 offset:9216\n"
    "ds_read_b128 v[184:187], v239 offset:10240\n"
    "ds_read_b128 v[188:191], v239 offset:11264\n"
    "s_mov_b32 s84, 0xbfb8aa3b\n"
    "s_mov_b32 s22, 4096\n"
    "buffer_load_dwordx4 v[32:35], v224, s[76:79], s22 offen offset:0 sc1\n"
    "v_add_f32 v128, v128, v216\n"
    "v_add_f32 v129, v129, v216\n"
    "v_add_f32 v130, v130, v216\n"
    "v_add_f32 v131, v131, v216\n"
    "v_add_f32 v132, v132, v216\n"
    "v_add_f32 v133, v133, v216\n"
    "v_add_f32 v134, v134, v216\n"
    "v_add_f32 v135, v135, v216\n"
    "v_add_f32 v136, v136, v216\n"
    "v_add_f32 v137, v137, v216\n"
    "v_add_f32 v138, v138, v216\n"
    "v_add_f32 v139, v139, v216\n"
    "v_add_f32 v140, v140, v216\n"
    "v_add_f32 v141, v141, v216\n"
    "v_add_f32 v142, v142, v216\n"
    "v_add_f32 v143, v143, v216\n"
    "s_mov_b32 s22, 69632\n"
    "buffer_load_dwordx4 v[36:39], v224, s[76:79], s22 offen offset:0 sc1\n"
    "s_waitcnt lgkmcnt(11)\n"
    "v_add_f32 v128, v128, v144\n"
    "v_add_f32 v129, v129, v145\n"
    "v_add_f32 v130, v130, v146\n"
    "v_add_f32 v131, v131, v147\n"
    "s_waitcnt lgkmcnt(10)\n"
    "v_add_f32 v132, v132, v148\n"
    "v_add_f32 v133, v133, v149\n"
    "v_add_f32 v134, v134, v150\n"
    "v_add_f32 v135, v135, v151\n"
    "s_waitcnt lgkmcnt(9)\n"
    "v_add_f32 v136, v136, v152\n"
    "v_add_f32 v137, v137, v153\n"
    "v_add_f32 v138, v138, v154\n"
    "v_add_f32 v139, v139, v155\n"
    "s_waitcnt lgkmcnt(8)\n"
    "v_add_f32 v140, v140, v156\n"
    "v_add_f32 v141, v141, v157\n"
    "v_add_f32 v142, v142, v158\n"
    "v_add_f32 v143, v143, v159\n"
    "s_mov_b32 s22, 4096\n"
    "buffer_load_dwordx4 v[40:43], v224, s[76:79], s22 offen offset:1024 sc1\n"
    "s_waitcnt lgkmcnt(7)\n"
    "v_add_f32 v128, v128, v160\n"
    "v_add_f32 v129, v129, v161\n"
    "v_add_f32 v130, v130, v162\n"
    "v_add_f32 v131, v131, v163\n"
    "s_waitcnt lgkmcnt(6)\n"
    "v_add_f32 v132, v132, v164\n"
    "v_add_f32 v133, v133, v165\n"
    "v_add_f32 v134, v134, v166\n"
    "v_add_f32 v135, v135, v167\n"
    "s_waitcnt lgkmcnt(5)\n"
    "v_add_f32 v136, v136, v168\n"
    "v_add_f32 v137, v137, v169\n"
    "v_add_f32 v138, v138, v170\n"
    "v_add_f32 v139, v139, v171\n"
    "s_waitcnt lgkmcnt(4)\n"
    "v_add_f32 v140, v140, v172\n"
    "v_add_f32 v141, v141, v173\n"
    "v_add_f32 v142, v142, v174\n"
    "v_add_f32 v143, v143, v175\n"
    "s_mov_b32 s22, 69632\n"
    "buffer_load_dwordx4 v[44:47], v224, s[76:79], s22 offen offset:1024 sc1\n"
    "s_waitcnt lgkmcnt(3)\n"
    "v_add_f32 v128, v128, v176\n"
    "v_add_f32 v129, v129, v177\n"
    "v_add_f32 v130, v130, v178\n"
    "v_add_f32 v131, v131, v179\n"
    "s_waitcnt lgkmcnt(2)\n"
    "v_add_f32 v132, v132, v180\n"
    "v_add_f32 v133, v133, v181\n"
    "v_add_f32 v134, v134, v182\n"
    "v_add_f32 v135, v135, v183\n"
    "s_waitcnt lgkmcnt(1)\n"
    "v_add_f32 v136, v136, v184\n"
    "v_add_f32 v137, v137, v185\n"
    "v_add_f32 v138, v138, v186\n"
    "v_add_f32 v139, v139, v187\n"
    "s_waitcnt lgkmcnt(0)\n"
    "v_add_f32 v140, v140, v188\n"
    "v_add_f32 v141, v141, v189\n"
    "v_add_f32 v142, v142, v190\n"
    "v_add_f32 v143, v143, v191\n"
    "s_mov_b32 s22, 4096\n"
    "buffer_load_dwordx4 v[48:51], v224, s[76:79], s22 offen offset:2048 sc1\n"
    "v_mul_f32 v144, s84, v128\n"
    "v_mul_f32 v145, s84, v129\n"
    "v_mul_f32 v146, s84, v130\n"
    "v_mul_f32 v147, s84, v131\n"
    "v_mul_f32 v148, s84, v132\n"
    "v_mul_f32 v149, s84, v133\n"
    "v_mul_f32 v150, s84, v134\n"
    "v_mul_f32 v151, s84, v135\n"
    "v_mul_f32 v152, s84, v136\n"
    "v_mul_f32 v153, s84, v137\n"
    "v_mul_f32 v154, s84, v138\n"
    "v_mul_f32 v155, s84, v139\n"
    "v_mul_f32 v156, s84, v140\n"
    "v_mul_f32 v157, s84, v141\n"
    "v_mul_f32 v158, s84, v142\n"
    "v_mul_f32 v159, s84, v143\n"
    "s_mov_b32 s22, 69632\n"
    "buffer_load_dwordx4 v[52:55], v224, s[76:79], s22 offen offset:2048 sc1\n"
    "v_exp_f32 v144, v144\n"
    "v_exp_f32 v145, v145\n"
    "v_exp_f32 v146, v146\n"
    "v_exp_f32 v147, v147\n"
    "v_exp_f32 v148, v148\n"
    "v_exp_f32 v149, v149\n"
    "v_exp_f32 v150, v150\n"
    "v_exp_f32 v151, v151\n"
    "s_mov_b32 s22, 4096\n"
    "buffer_load_dwordx4 v[56:59], v224, s[76:79], s22 offen offset:3072 sc1\n"
    "v_exp_f32 v152, v152\n"
    "v_exp_f32 v153, v153\n"
    "v_exp_f32 v154, v154\n"
    "v_exp_f32 v155, v155\n"
    "v_exp_f32 v156, v156\n"
    "v_exp_f32 v157, v157\n"
    "v_exp_f32 v158, v158\n"
    "v_exp_f32 v159, v159\n"
    "s_mov_b32 s22, 69632\n"
    "buffer_load_dwordx4 v[60:63], v224, s[76:79], s22 offen offset:3072 sc1\n"
    "v_add_f32 v144, 1.0, v144\n"
    "v_add_f32 v145, 1.0, v145\n"
    "v_add_f32 v146, 1.0, v146\n"
    "v_add_f32 v147, 1.0, v147\n"
    "v_add_f32 v148, 1.0, v148\n"
    "v_add_f32 v149, 1.0, v149\n"
    "v_add_f32 v150, 1.0, v150\n"
    "v_add_f32 v151, 1.0, v151\n"
    "v_add_f32 v152, 1.0, v152\n"
    "v_add_f32 v153, 1.0, v153\n"
    "v_add_f32 v154, 1.0, v154\n"
    "v_add_f32 v155, 1.0, v155\n"
    "v_add_f32 v156, 1.0, v156\n"
    "v_add_f32 v157, 1.0, v157\n"
    "v_add_f32 v158, 1.0, v158\n"
    "v_add_f32 v159, 1.0, v159\n"
    "v_rcp_f32 v144, v144\n"
    "v_rcp_f32 v145, v145\n"
    "v_rcp_f32 v146, v146\n"
    "v_rcp_f32 v147, v147\n"
    "v_rcp_f32 v148, v148\n"
    "v_rcp_f32 v149, v149\n"
    "v_rcp_f32 v150, v150\n"
    "v_rcp_f32 v151, v151\n"
    "v_rcp_f32 v152, v152\n"
    "v_rcp_f32 v153, v153\n"
    "v_rcp_f32 v154, v154\n"
    "v_rcp_f32 v155, v155\n"
    "v_rcp_f32 v156, v156\n"
    "v_rcp_f32 v157, v157\n"
    "v_rcp_f32 v158, v158\n"
    "v_rcp_f32 v159, v159\n"
    "v_mul_f32 v128, v128, v144\n"
    "v_mul_f32 v129, v129, v145\n"
    "v_mul_f32 v130, v130, v146\n"
    "v_mul_f32 v131, v131, v147\n"
    "v_mul_f32 v132, v132, v148\n"
    "v_mul_f32 v133, v133, v149\n"
    "v_mul_f32 v134, v134, v150\n"
    "v_mul_f32 v135, v135, v151\n"
    "v_mul_f32 v136, v136, v152\n"
    "v_mul_f32 v137, v137, v153\n"
    "v_mul_f32 v138, v138, v154\n"
    "v_mul_f32 v139, v139, v155\n"
    "v_mul_f32 v140, v140, v156\n"
    "v_mul_f32 v141, v141, v157\n"
    "v_mul_f32 v142, v142, v158\n"
    "v_mul_f32 v143, v143, v159\n"
    "s_waitcnt vmcnt(15)\n"
    "v_mfma_f32_16x16x32_bf16 v[64:67], v[82:85], v[0:3], 0\n"
    "s_waitcnt vmcnt(13)\n"
    "v_mfma_f32_16x16x32_bf16 v[64:67], v[82:85], v[8:11], v[64:67]\n"
    "s_waitcnt vmcnt(11)\n"
    "v_mfma_f32_16x16x32_bf16 v[64:67], v[82:85], v[16:19], v[64:67]\n"
    "s_waitcnt vmcnt(9)\n"
    "v_mfma_f32_16x16x32_bf16 v[64:67], v[82:85], v[24:27], v[64:67]\n"
    "v_mfma_f32_16x16x32_bf16 v[68:71], v[80:83], v[0:3], 0\n"
    "v_mfma_f32_16x16x32_bf16 v[68:71], v[80:83], v[8:11], v[68:71]\n"
    "v_mfma_f32_16x16x32_bf16 v[68:71], v[80:83], v[16:19], v[68:71]\n"
    "v_mfma_f32_16x16x32_bf16 v[68:71], v[80:83], v[24:27], v[68:71]\n"
    "v_mfma_f32_16x16x32_bf16 v[72:75], v[82:85], v[4:7], 0\n"
    "v_mfma_f32_16x16x32_bf16 v[72:75], v[82:85], v[12:15], v[72:75]\n"
    "v_mfma_f32_16x16x32_bf16 v[72:75], v[82:85], v[20:23], v[72:75]\n"
    "s_waitcnt vmcnt(8)\n"
    "v_mfma_f32_16x16x32_bf16 v[72:75], v[82:85], v[28:31], v[72:75]\n"
    "v_mfma_f32_16x16x32_bf16 v[76:79], v[80:83], v[4:7], 0\n"
    "v_mfma_f32_16x16x32_bf16 v[76:79], v[80:83], v[12:15], v[76:79]\n"
    "v_mfma_f32_16x16x32_bf16 v[76:79], v[80:83], v[20:23], v[76:79]\n"
    "v_mfma_f32_16x16x32_bf16 v[76:79], v[80:83], v[28:31], v[76:79]\n"
    "s_waitcnt vmcnt(7)\n"
    "v_mfma_f32_16x16x32_bf16 v[64:67], v[82:85], v[32:35], v[64:67]\n"
    "s_waitcnt vmcnt(5)\n"
    "v_mfma_f32_16x16x32_bf16 v[64:67], v[82:85], v[40:43], v[64:67]\n"
    "s_waitcnt vmcnt(3)\n"
    "v_mfma_f32_16x16x32_bf16 v[64:67], v[82:85], v[48:51], v[64:67]\n"
    "s_waitcnt vmcnt(1)\n"
    "v_mfma_f32_16x16x32_bf16 v[64:67], v[82:85], v[56:59], v[64:67]\n"
    "v_mfma_f32_16x16x32_bf16 v[68:71], v[80:83], v[32:35], v[68:71]\n"
    "v_mfma_f32_16x16x32_bf16 v[68:71], v[80:83], v[40:43], v[68:71]\n"
    "v_mfma_f32_16x16x32_bf16 v[68:71], v[80:83], v[48:51], v[68:71]\n"
    "v_mfma_f32_16x16x32_bf16 v[68:71], v[80:83], v[56:59], v[68:71]\n"
    "v_mfma_f32_16x16x32_bf16 v[72:75], v[82:85], v[36:39], v[72:75]\n"
    "v_mfma_f32_16x16x32_bf16 v[72:75], v[82:85], v[44:47], v[72:75]\n"
    "v_mfma_f32_16x16x32_bf16 v[72:75], v[82:85], v[52:55], v[72:75]\n"
    "s_waitcnt vmcnt(0)\n"
    "v_mfma_f32_16x16x32_bf16 v[72:75], v[82:85], v[60:63], v[72:75]\n"
    "v_mfma_f32_16x16x32_bf16 v[76:79], v[80:83], v[36:39], v[76:79]\n"
    "v_mfma_f32_16x16x32_bf16 v[76:79], v[80:83], v[44:47], v[76:79]\n"
    "v_mfma_f32_16x16x32_bf16 v[76:79], v[80:83], v[52:55], v[76:79]\n"
    "v_mfma_f32_16x16x32_bf16 v[76:79], v[80:83], v[60:63], v[76:79]\n"
    "v_fma_f32 v192, v128, v64, v192\n"
    "v_fma_f32 v193, v129, v65, v193\n"
    "v_fma_f32 v194, v130, v66, v194\n"
    "v_fma_f32 v195, v131, v67, v195\n"
    "v_fma_f32 v196, v132, v68, v196\n"
    "v_fma_f32 v197, v133, v69, v197\n"
    "v_fma_f32 v198, v134, v70, v198\n"
    "v_fma_f32 v199, v135, v71, v199\n"
    "v_fma_f32 v200, v136, v72, v200\n"
    "v_fma_f32 v201, v137, v73, v201\n"
    "v_fma_f32 v202, v138, v74, v202\n"
    "v_fma_f32 v203, v139, v75, v203\n"
    "v_fma_f32 v204, v140, v76, v204\n"
    "v_fma_f32 v205, v141, v77, v205\n"
    "v_fma_f32 v206, v142, v78, v206\n"
    "v_fma_f32 v207, v143, v79, v207\n"
    "v_cvt_pk_bf16_f32 v192, v192, v193\n"
    "v_cvt_pk_bf16_f32 v193, v194, v195\n"
    "v_cvt_pk_bf16_f32 v194, v196, v197\n"
    "v_cvt_pk_bf16_f32 v195, v198, v199\n"
    "v_cvt_pk_bf16_f32 v196, v200, v201\n"
    "v_cvt_pk_bf16_f32 v197, v202, v203\n"
    "v_cvt_pk_bf16_f32 v198, v204, v205\n"
    "v_cvt_pk_bf16_f32 v199, v206, v207\n"
    "buffer_store_dwordx2 v[192:193], v234, s[28:31], 0 offen offset:0\n"
    "buffer_store_dwordx2 v[194:195], v234, s[28:31], 0 offen offset:32\n"
    "buffer_store_dwordx2 v[196:197], v234, s[28:31], 0 offen offset:128\n"
    "buffer_store_dwordx2 v[198:199], v234, s[28:31], 0 offen offset:160\n"
    "s_branch K2_END\n"
    "K2_S1_1:\n"
    "s_call_b64 s[20:21], K2_RARE1_1\n"
    "s_branch K2_R1_1\n"
    "K2_S2_1:\n"
    "s_call_b64 s[20:21], K2_RARE2\n"
    "s_branch K2_R2_1\n"
    "K2_S1_2:\n"
    "s_call_b64 s[20:21], K2_RARE1_0\n"
    "s_branch K2_R1_2\n"
    "K2_S2_2:\n"
    "s_call_b64 s[20:21], K2_RARE2\n"
    "s_branch K2_R2_2\n"
    "K2_S1_3:\n"
    "s_call_b64 s[20:21], K2_RARE1_1\n"
    "s_branch K2_R1_3\n"
    "K2_S2_11:\n"
    "s_call_b64 s[20:21], K2_RARE2\n"
    "s_branch K2_R2_11\n"
    "K2_S1_12:\n"
    "s_call_b64 s[20:21], K2_RARE1_0\n"
    "s_branch K2_R1_12\n"
    "K2_S2_12:\n"
    "s_call_b64 s[20:21], K2_RARE2\n"
    "s_branch K2_R2_12\n"
    "K2_S1_13:\n"
    "s_call_b64 s[20:21], K2_RARE1_1\n"
    "s_branch K2_R1_13\n"
    "K2_S2_13:\n"
    "s_call_b64 s[20:21], K2_RARE2\n"
    "s_branch K2_R2_13\n"
    "K2_S1_14:\n"
    "s_call_b64 s[20:21], K2_RARE1_0\n"
    "s_branch K2_R1_14\n"
    "K2_S2_14:\n"
    "s_call_b64 s[20:21], K2_RARE2\n"
    "s_branch K2_R2_14\n"
    "K2_S1_15:\n"
    "s_call_b64 s[20:21], K2_RARE1_1\n"
    "s_branch K2_R1_15\n"
    "K2_S2_15:\n"
    "s_call_b64 s[20:21], K2_RARE2\n"
    "s_branch K2_R2_15\n"
    "K2_SP_U:\n"
    "s_mov_b32 s21, 262144\n"
    "K2_SL_U:\n"
    "s_sleep 1\n"
    "buffer_load_dword v227, v225, s[64:67], s20 offen sc1\n"
    "s_waitcnt vmcnt(0)\n"
    "v_readfirstlane_b32 s23, v227\n"
    "s_nop 3\n"
    "s_cmp_ge_u32 s23, 28\n"
    "s_cbranch_scc1 K2_SR_U\n"
    "s_sub_u32 s21, s21, 1\n"
    "s_cmp_lg_u32 s21, 0\n"
    "s_cbranch_scc1 K2_SL_U\n"
    "s_branch K2_SR_U\n"
    "K2_SP_P:\n"
    "s_mov_b32 s21, 262144\n"
    "K2_SL_P:\n"
    "s_sleep 1\n"
    "buffer_load_dword v227, v225, s[64:67], s20 offen sc1\n"
    "s_waitcnt vmcnt(0)\n"
    "v_readfirstlane_b32 s23, v227\n"
    "s_nop 3\n"
    "s_cmp_ge_u32 s23, 8\n"
    "s_cbranch_scc1 K2_SR_P\n"
    "s_sub_u32 s21, s21, 1\n"
    "s_cmp_lg_u32 s21, 0\n"
    "s_cbranch_scc1 K2_SL_P\n"
    "s_branch K2_SR_P\n"
    "K2_RARE1_0:\n"
    "s_nop 15\n"
    "v_max3_f32 v240, v128, v129, v130\n"
    "v_max3_f32 v240, v240, v131, v132\n"
    "v_max3_f32 v240, v240, v133, v134\n"
    "v_max3_f32 v240, v240, v135, 0\n"
    "v_mov_b32 v241, v240\n"
    "s_nop 1\n"
    "v_permlane32_swap_b32 v240, v241\n"
    "s_nop 1\n"
    "v_max_f32 v240, v240, v241\n"
    "v_mov_b32 v241, v240\n"
    "s_nop 1\n"
    "v_permlane16_swap_b32 v240, v241\n"
    "s_nop 1\n"
    "v_max_f32 v240, v240, v241\n"
    "v_sub_f32 v241, 0, v240\n"
    "v_exp_f32 v236, v241\n"
    "s_nop 1\n"
    "v_sub_f32 v128, v128, v240\n"
    "v_sub_f32 v129, v129, v240\n"
    "v_sub_f32 v130, v130, v240\n"
    "v_sub_f32 v131, v131, v240\n"
    "v_sub_f32 v132, v132, v240\n"
    "v_sub_f32 v133, v133, v240\n"
    "v_sub_f32 v134, v134, v240\n"
    "v_sub_f32 v135, v135, v240\n"
    "v_sub_f32 v192, v192, v240\n"
    "v_sub_f32 v193, v193, v240\n"
    "v_sub_f32 v194, v194, v240\n"
    "v_sub_f32 v195, v195, v240\n"
    "v_max3_f32 v240, v136, v137, v138\n"
    "v_max3_f32 v240, v240, v139, v140\n"
    "v_max3_f32 v240, v240, v141, v142\n"
    "v_max3_f32 v240, v240, v143, 0\n"
    "v_mov_b32 v241, v240\n"
    "s_nop 1\n"
    "v_permlane32_swap_b32 v240, v241\n"
    "s_nop 1\n"
    "v_max_f32 v240, v240, v241\n"
    "v_mov_b32 v241, v240\n"
    "s_nop 1\n"
    "v_permlane16_swap_b32 v240, v241\n"
    "s_nop 1\n"
    "v_max_f32 v240, v240, v241\n"
    "v_sub_f32 v241, 0, v240\n"
    "v_exp_f32 v237, v241\n"
    "s_nop 1\n"
    "v_sub_f32 v136, v136, v240\n"
    "v_sub_f32 v137, v137, v240\n"
    "v_sub_f32 v138, v138, v240\n"
    "v_sub_f32 v139, v139, v240\n"
    "v_sub_f32 v140, v140, v240\n"
    "v_sub_f32 v141, v141, v240\n"
    "v_sub_f32 v142, v142, v240\n"
    "v_sub_f32 v143, v143, v240\n"
    "v_sub_f32 v196, v196, v240\n"
    "v_sub_f32 v197, v197, v240\n"
    "v_sub_f32 v198, v198, v240\n"
    "v_sub_f32 v199, v199, v240\n"
    "v_max3_f32 v240, v144, v145, v146\n"
    "v_max3_f32 v240, v240, v147, v148\n"
    "v_max3_f32 v240, v240, v149, v150\n"
    "v_max3_f32 v240, v240, v151, 0\n"
    "v_mov_b32 v241, v240\n"
    "s_nop 1\n"
    "v_permlane32_swap_b32 v240, v241\n"
    "s_nop 1\n"
    "v_max_f32 v240, v240, v241\n"
    "v_mov_b32 v241, v240\n"
    "s_nop 1\n"
    "v_permlane16_swap_b32 v240, v241\n"
    "s_nop 1\n"
    "v_max_f32 v240, v240, v241\n"
    "v_sub_f32 v241, 0, v240\n"
    "v_exp_f32 v238, v241\n"
    "s_nop 1\n"
    "v_sub_f32 v144, v144, v240\n"
    "v_sub_f32 v145, v145, v240\n"
    "v_sub_f32 v146, v146, v240\n"
    "v_sub_f32 v147, v147, v240\n"
    "v_sub_f32 v148, v148, v240\n"
    "v_sub_f32 v149, v149, v240\n"
    "v_sub_f32 v150, v150, v240\n"
    "v_sub_f32 v151, v151, v240\n"
    "v_sub_f32 v200, v200, v240\n"
    "v_sub_f32 v201, v201, v240\n"
    "v_sub_f32 v202, v202, v240\n"
    "v_sub_f32 v203, v203, v240\n"
    "v_max3_f32 v240, v152, v153, v154\n"
    "v_max3_f32 v240, v240, v155, v156\n"
    "v_max3_f32 v240, v240, v157, v158\n"
    "v_max3_f32 v240, v240, v159, 0\n"
    "v_mov_b32 v241, v240\n"
    "s_nop 1\n"
    "v_permlane32_swap_b32 v240, v241\n"
    "s_nop 1\n"
    "v_max_f32 v240, v240, v241\n"
    "v_mov_b32 v241, v240\n"
    "s_nop 1\n"
    "v_permlane16_swap_b32 v240, v241\n"
    "s_nop 1\n"
    "v_max_f32 v240, v240, v241\n"
    "v_sub_f32 v241, 0, v240\n"
    "v_exp_f32 v239, v241\n"
    "s_nop 1\n"
    "v_sub_f32 v152, v152, v240\n"
    "v_sub_f32 v153, v153, v240\n"
    "v_sub_f32 v154, v154, v240\n"
    "v_sub_f32 v155, v155, v240\n"
    "v_sub_f32 v156, v156, v240\n"
    "v_sub_f32 v157, v157, v240\n"
    "v_sub_f32 v158, v158, v240\n"
    "v_sub_f32 v159, v159, v240\n"
    "v_sub_f32 v204, v204, v240\n"
    "v_sub_f32 v205, v205, v240\n"
    "v_sub_f32 v206, v206, v240\n"
    "v_sub_f32 v207, v207, v240\n"
    "s_nop 3\n"
    "s_setpc_b64 s[20:21]\n"
    "K2_RARE1_1:\n"
    "s_nop 15\n"
    "v_max3_f32 v240, v160, v161, v162\n"
    "v_max3_f32 v240, v240, v163, v164\n"
    "v_max3_f32 v240, v240, v165, v166\n"
    "v_max3_f32 v240, v240, v167, 0\n"
    "v_mov_b32 v241, v240\n"
    "s_nop 1\n"
    "v_permlane32_swap_b32 v240, v241\n"
    "s_nop 1\n"
    "v_max_f32 v240, v240, v241\n"
    "v_mov_b32 v241, v240\n"
    "s_nop 1\n"
    "v_permlane16_swap_b32 v240, v241\n"
    "s_nop 1\n"
    "v_max_f32 v240, v240, v241\n"
    "v_sub_f32 v241, 0, v240\n"
    "v_exp_f32 v236, v241\n"
    "s_nop 1\n"
    "v_sub_f32 v160, v160, v240\n"
    "v_sub_f32 v161, v161, v240\n"
    "v_sub_f32 v162, v162, v240\n"
    "v_sub_f32 v163, v163, v240\n"
    "v_sub_f32 v164, v164, v240\n"
    "v_sub_f32 v165, v165, v240\n"
    "v_sub_f32 v166, v166, v240\n"
    "v_sub_f32 v167, v167, v240\n"
    "v_sub_f32 v192, v192, v240\n"
    "v_sub_f32 v193, v193, v240\n"
    "v_sub_f32 v194, v194, v240\n"
    "v_sub_f32 v195, v195, v240\n"
    "v_max3_f32 v240, v168, v169, v170\n"
    "v_max3_f32 v240, v240, v171, v172\n"
    "v_max3_f32 v240, v240, v173, v174\n"
    "v_max3_f32 v240, v240, v175, 0\n"
    "v_mov_b32 v241, v240\n"
    "s_nop 1\n"
    "v_permlane32_swap_b32 v240, v241\n"
    "s_nop 1\n"
    "v_max_f32 v240, v240, v241\n"
    "v_mov_b32 v241, v240\n"
    "s_nop 1\n"
    "v_permlane16_swap_b32 v240, v241\n"
    "s_nop 1\n"
    "v_max_f32 v240, v240, v241\n"
    "v_sub_f32 v241, 0, v240\n"
    "v_exp_f32 v237, v241\n"
    "s_nop 1\n"
    "v_sub_f32 v168, v168, v240\n"
    "v_sub_f32 v169, v169, v240\n"
    "v_sub_f32 v170, v170, v240\n"
    "v_sub_f32 v171, v171, v240\n"
    "v_sub_f32 v172, v172, v240\n"
    "v_sub_f32 v173, v173, v240\n"
    "v_sub_f32 v174, v174, v240\n"
    "v_sub_f32 v175, v175, v240\n"
    "v_sub_f32 v196, v196, v240\n"
    "v_sub_f32 v197, v197, v240\n"
    "v_sub_f32 v198, v198, v240\n"
    "v_sub_f32 v199, v199, v240\n"
    "v_max3_f32 v240, v176, v177, v178\n"
    "v_max3_f32 v240, v240, v179, v180\n"
    "v_max3_f32 v240, v240, v181, v182\n"
    "v_max3_f32 v240, v240, v183, 0\n"
    "v_mov_b32 v241, v240\n"
    "s_nop 1\n"
    "v_permlane32_swap_b32 v240, v241\n"
    "s_nop 1\n"
    "v_max_f32 v240, v240, v241\n"
    "v_mov_b32 v241, v240\n"
    "s_nop 1\n"
    "v_permlane16_swap_b32 v240, v241\n"
    "s_nop 1\n"
    "v_max_f32 v240, v240, v241\n"
    "v_sub_f32 v241, 0, v240\n"
    "v_exp_f32 v238, v241\n"
    "s_nop 1\n"
    "v_sub_f32 v176, v176, v240\n"
    "v_sub_f32 v177, v177, v240\n"
    "v_sub_f32 v178, v178, v240\n"
    "v_sub_f32 v179, v179, v240\n"
    "v_sub_f32 v180, v180, v240\n"
    "v_sub_f32 v181, v181, v240\n"
    "v_sub_f32 v182, v182, v240\n"
    "v_sub_f32 v183, v183, v240\n"
    "v_sub_f32 v200, v200, v240\n"
    "v_sub_f32 v201, v201, v240\n"
    "v_sub_f32 v202, v202, v240\n"
    "v_sub_f32 v203, v203, v240\n"
    "v_max3_f32 v240, v184, v185, v186\n"
    "v_max3_f32 v240, v240, v187, v188\n"
    "v_max3_f32 v240, v240, v189, v190\n"
    "v_max3_f32 v240, v240, v191, 0\n"
    "v_mov_b32 v241, v240\n"
    "s_nop 1\n"
    "v_permlane32_swap_b32 v240, v241\n"
    "s_nop 1\n"
    "v_max_f32 v240, v240, v241\n"
    "v_mov_b32 v241, v240\n"
    "s_nop 1\n"
    "v_permlane16_swap_b32 v240, v241\n"
    "s_nop 1\n"
    "v_max_f32 v240, v240, v241\n"
    "v_sub_f32 v241, 0, v240\n"
    "v_exp_f32 v239, v241\n"
    "s_nop 1\n"
    "v_sub_f32 v184, v184, v240\n"
    "v_sub_f32 v185, v185, v240\n"
    "v_sub_f32 v186, v186, v240\n"
    "v_sub_f32 v187, v187, v240\n"
    "v_sub_f32 v188, v188, v240\n"
    "v_sub_f32 v189, v189, v240\n"
    "v_sub_f32 v190, v190, v240\n"
    "v_sub_f32 v191, v191, v240\n"
    "v_sub_f32 v204, v204, v240\n"
    "v_sub_f32 v205, v205, v240\n"
    "v_sub_f32 v206, v206, v240\n"
    "v_sub_f32 v207, v207, v240\n"
    "s_nop 3\n"
    "s_setpc_b64 s[20:21]\n"
    "K2_RARE2:\n"
    "s_nop 15\n"
    "v_mul_f32 v0, v0, v236\n"
    "v_mul_f32 v1, v1, v236\n"
    "v_mul_f32 v2, v2, v236\n"
    "v_mul_f32 v3, v3, v236\n"
    "v_mul_f32 v4, v4, v236\n"
    "v_mul_f32 v5, v5, v236\n"
    "v_mul_f32 v6, v6, v236\n"
    "v_mul_f32 v7, v7, v236\n"
    "v_mul_f32 v8, v8, v237\n"
    "v_mul_f32 v9, v9, v237\n"
    "v_mul_f32 v10, v10, v237\n"
    "v_mul_f32 v11, v11, v237\n"
    "v_mul_f32 v12, v12, v237\n"
    "v_mul_f32 v13, v13, v237\n"
    "v_mul_f32 v14, v14, v237\n"
    "v_mul_f32 v15, v15, v237\n"
    "v_mul_f32 v16, v16, v238\n"
    "v_mul_f32 v17, v17, v238\n"
    "v_mul_f32 v18, v18, v238\n"
    "v_mul_f32 v19, v19, v238\n"
    "v_mul_f32 v20, v20, v238\n"
    "v_mul_f32 v21, v21, v238\n"
    "v_mul_f32 v22, v22, v238\n"
    "v_mul_f32 v23, v23, v238\n"
    "v_mul_f32 v24, v24, v239\n"
    "v_mul_f32 v25, v25, v239\n"
    "v_mul_f32 v26, v26, v239\n"
    "v_mul_f32 v27, v27, v239\n"
    "v_mul_f32 v28, v28, v239\n"
    "v_mul_f32 v29, v29, v239\n"
    "v_mul_f32 v30, v30, v239\n"
    "v_mul_f32 v31, v31, v239\n"
    "v_mul_f32 v32, v32, v236\n"
    "v_mul_f32 v33, v33, v236\n"
    "v_mul_f32 v34, v34, v236\n"
    "v_mul_f32 v35, v35, v236\n"
    "v_mul_f32 v36, v36, v236\n"
    "v_mul_f32 v37, v37, v236\n"
    "v_mul_f32 v38, v38, v236\n"
    "v_mul_f32 v39, v39, v236\n"
    "v_mul_f32 v40, v40, v237\n"
    "v_mul_f32 v41, v41, v237\n"
    "v_mul_f32 v42, v42, v237\n"
    "v_mul_f32 v43, v43, v237\n"
    "v_mul_f32 v44, v44, v237\n"
    "v_mul_f32 v45, v45, v237\n"
    "v_mul_f32 v46, v46, v237\n"
    "v_mul_f32 v47, v47, v237\n"
    "v_mul_f32 v48, v48, v238\n"
    "v_mul_f32 v49, v49, v238\n"
    "v_mul_f32 v50, v50, v238\n"
    "v_mul_f32 v51, v51, v238\n"
    "v_mul_f32 v52, v52, v238\n"
    "v_mul_f32 v53, v53, v238\n"
    "v_mul_f32 v54, v54, v238\n"
    "v_mul_f32 v55, v55, v238\n"
    "v_mul_f32 v56, v56, v239\n"
    "v_mul_f32 v57, v57, v239\n"
    "v_mul_f32 v58, v58, v239\n"
    "v_mul_f32 v59, v59, v239\n"
    "v_mul_f32 v60, v60, v239\n"
    "v_mul_f32 v61, v61, v239\n"
    "v_mul_f32 v62, v62, v239\n"
    "v_mul_f32 v63, v63, v239\n"
    "v_mul_f32 v64, v64, v236\n"
    "v_mul_f32 v65, v65, v236\n"
    "v_mul_f32 v66, v66, v236\n"
    "v_mul_f32 v67, v67, v236\n"
    "v_mul_f32 v68, v68, v236\n"
    "v_mul_f32 v69, v69, v236\n"
    "v_mul_f32 v70, v70, v236\n"
    "v_mul_f32 v71, v71, v236\n"
    "v_mul_f32 v72, v72, v237\n"
    "v_mul_f32 v73, v73, v237\n"
    "v_mul_f32 v74, v74, v237\n"
    "v_mul_f32 v75, v75, v237\n"
    "v_mul_f32 v76, v76, v237\n"
    "v_mul_f32 v77, v77, v237\n"
    "v_mul_f32 v78, v78, v237\n"
    "v_mul_f32 v79, v79, v237\n"
    "v_mul_f32 v80, v80, v238\n"
    "v_mul_f32 v81, v81, v238\n"
    "v_mul_f32 v82, v82, v238\n"
    "v_mul_f32 v83, v83, v238\n"
    "v_mul_f32 v84, v84, v238\n"
    "v_mul_f32 v85, v85, v238\n"
    "v_mul_f32 v86, v86, v238\n"
    "v_mul_f32 v87, v87, v238\n"
    "v_mul_f32 v88, v88, v239\n"
    "v_mul_f32 v89, v89, v239\n"
    "v_mul_f32 v90, v90, v239\n"
    "v_mul_f32 v91, v91, v239\n"
    "v_mul_f32 v92, v92, v239\n"
    "v_mul_f32 v93, v93, v239\n"
    "v_mul_f32 v94, v94, v239\n"
    "v_mul_f32 v95, v95, v239\n"
    "v_mul_f32 v96, v96, v236\n"
    "v_mul_f32 v97, v97, v236\n"
    "v_mul_f32 v98, v98, v236\n"
    "v_mul_f32 v99, v99, v236\n"
    "v_mul_f32 v100, v100, v236\n"
    "v_mul_f32 v101, v101, v236\n"
    "v_mul_f32 v102, v102, v236\n"
    "v_mul_f32 v103, v103, v236\n"
    "v_mul_f32 v104, v104, v237\n"
    "v_mul_f32 v105, v105, v237\n"
    "v_mul_f32 v106, v106, v237\n"
    "v_mul_f32 v107, v107, v237\n"
    "v_mul_f32 v108, v108, v237\n"
    "v_mul_f32 v109, v109, v237\n"
    "v_mul_f32 v110, v110, v237\n"
    "v_mul_f32 v111, v111, v237\n"
    "v_mul_f32 v112, v112, v238\n"
    "v_mul_f32 v113, v113, v238\n"
    "v_mul_f32 v114, v114, v238\n"
    "v_mul_f32 v115, v115, v238\n"
    "v_mul_f32 v116, v116, v238\n"
    "v_mul_f32 v117, v117, v238\n"
    "v_mul_f32 v118, v118, v238\n"
    "v_mul_f32 v119, v119, v238\n"
    "v_mul_f32 v120, v120, v239\n"
    "v_mul_f32 v121, v121, v239\n"
    "v_mul_f32 v122, v122, v239\n"
    "v_mul_f32 v123, v123, v239\n"
    "v_mul_f32 v124, v124, v239\n"
    "v_mul_f32 v125, v125, v239\n"
    "v_mul_f32 v126, v126, v239\n"
    "v_mul_f32 v127, v127, v239\n"
    "v_mul_f32 v208, v208, v236\n"
    "v_mul_f32 v209, v209, v236\n"
    "v_mul_f32 v210, v210, v236\n"
    "v_mul_f32 v211, v211, v236\n"
    "v_mul_f32 v212, v212, v237\n"
    "v_mul_f32 v213, v213, v237\n"
    "v_mul_f32 v214, v214, v237\n"
    "v_mul_f32 v215, v215, v237\n"
    "v_mul_f32 v216, v216, v238\n"
    "v_mul_f32 v217, v217, v238\n"
    "v_mul_f32 v218, v218, v238\n"
    "v_mul_f32 v219, v219, v238\n"
    "v_mul_f32 v220, v220, v239\n"
    "v_mul_f32 v221, v221, v239\n"
    "v_mul_f32 v222, v222, v239\n"
    "v_mul_f32 v223, v223, v239\n"
    "s_nop 3\n"
    "s_setpc_b64 s[20:21]\n"
    "K2_END:\n"
    :: "s"(__builtin_amdgcn_kernarg_segment_ptr()), "s"(wid), "s"(blockIdx.x)
    : "v0","v1","v2","v3","v4","v5","v6","v7","v8","v9","v10","v11","v12","v13","v14","v15","v16","v17","v18","v19","v20","v21","v22","v23","v24","v25","v26","v27","v28","v29","v30","v31","v32","v33","v34","v35","v36","v37","v38","v39","v40","v41","v42","v43","v44","v45","v46","v47","v48","v49","v50","v51","v52","v53","v54","v55","v56","v57","v58","v59","v60","v61","v62","v63","v64","v65","v66","v67","v68","v69","v70","v71","v72","v73","v74","v75","v76","v77","v78","v79","v80","v81","v82","v83","v84","v85","v86","v87","v88","v89","v90","v91","v92","v93","v94","v95","v96","v97","v98","v99","v100","v101","v102","v103","v104","v105","v106","v107","v108","v109","v110","v111","v112","v113","v114","v115","v116","v117","v118","v119","v120","v121","v122","v123","v124","v125","v126","v127","v128","v129","v130","v131","v132","v133","v134","v135","v136","v137","v138","v139","v140","v141","v142","v143","v144","v145","v146","v147","v148","v149","v150","v151","v152","v153","v154","v155","v156","v157","v158","v159","v160","v161","v162","v163","v164","v165","v166","v167","v168","v169","v170","v171","v172","v173","v174","v175","v176","v177","v178","v179","v180","v181","v182","v183","v184","v185","v186","v187","v188","v189","v190","v191","v192","v193","v194","v195","v196","v197","v198","v199","v200","v201","v202","v203","v204","v205","v206","v207","v208","v209","v210","v211","v212","v213","v214","v215","v216","v217","v218","v219","v220","v221","v222","v223","v224","v225","v226","v227","v228","v229","v230","v231","v232","v233","v234","v235","v236","v237","v238","v239","v240","v241","v242","v243","v244","v245","v246","v247","v248","v249","v250","v251","v252","v253","v254","v255","a0","a1","a2","a3","a4","a5","a6","a7","a8","a9","a10","a11","a12","a13","a14","a15","a16","a17","a18","a19","a20","a21","a22","a23","a24","a25","a26","a27","a28","a29","a30","a31","a32","a33","a34","a35","a36","a37","a38","a39","a40","a41","a42","a43","a44","a45","a46","a47","a48","a49","a50","a51","a52","a53","a54","a55","a56","a57","a58","a59","a60","a61","a62","a63","a64","a65","a66","a67","a68","a69","a70","a71","a72","a73","a74","a75","a76","a77","a78","a79","a80","a81","a82","a83","a84","a85","a86","a87","a88","a89","a90","a91","a92","a93","a94","a95","a96","a97","a98","a99","a100","a101","a102","a103","a104","a105","a106","a107","a108","a109","a110","a111","a112","a113","a114","a115","a116","a117","a118","a119","a120","a121","a122","a123","a124","a125","a126","a127","a128","a129","a130","a131","a132","a133","a134","a135","a136","a137","a138","a139","a140","a141","a142","a143","a144","a145","a146","a147","a148","a149","a150","a151","a152","a153","a154","a155","a156","a157","a158","a159","a160","a161","a162","a163","a164","a165","a166","a167","a168","a169","a170","a171","a172","a173","a174","a175","a176","a177","a178","a179","a180","a181","a182","a183","a184","a185","a186","a187","a188","a189","a190","a191","a192","a193","a194","a195","a196","a197","a198","a199","a200","a201","a202","a203","a204","a205","a206","a207","a208","a209","a210","a211","a212","a213","a214","a215","a216","a217","a218","a219","a220","a221","a222","a223","a224","a225","a226","a227","a228","a229","a230","a231","a232","a233","a234","a235","a236","a237","a238","a239","a240","a241","a242","a243","a244","a245","a246","a247","a248","a249","a250","a251","a252","a253","a254","a255","s4","s5","s6","s7","s8","s9","s10","s11","s12","s13","s14","s15","s16","s17","s18","s19","s20","s21","s22","s23","s24","s25","s26","s27","s28","s29","s30","s31","s32","s33","s34","s35","s36","s37","s38","s39","s40","s41","s42","s43","s44","s45","s46","s47","s48","s49","s50","s51","s52","s53","s54","s55","s56","s57","s58","s59","s60","s61","s62","s63","s64","s65","s66","s67","s68","s69","s70","s71","s72","s73","s74","s75","s76","s77","s78","s79","s80","s81","s82","s83","s84","s85","s86","s87","s88","s89","s90","s91","s92","s93","s94","s95","s96","s97","s98","s99","vcc","scc","memory");
}

extern "C" void kernel_launch(void* const* d_in, const int* in_sizes, int n_in, void* d_out, int out_size, void* d_ws,
                              size_t ws_size, hipStream_t stream) {
  const u16* x = (const u16*)d_in[0]; const u16* kc = (const u16*)d_in[1]; const u16* vc = (const u16*)d_in[2];
  const u16* wq = (const u16*)d_in[3]; const u16* wo = (const u16*)d_in[4];
  const u16* w1 = (const u16*)d_in[6]; const u16* b1 = (const u16*)d_in[7];
  const u16* w2 = (const u16*)d_in[8]; const u16* b2 = (const u16*)d_in[9];
  char* ws = (char*)d_ws;
  (void)hipFuncSetAttribute((const void*)k2_main, hipFuncAttributeMaxDynamicSharedMemorySize, 163840);
  hipLaunchKernelGGL(k1_ln, dim3(PREFETCH ? 256 : 128), dim3(1024), 0, stream, x, ws, wq);
  hipLaunchKernelGGL(k2_main, dim3(256), dim3(256), 163840, stream, ws, wq, kc, vc, x, (u16*)d_out, w1, (const u16*)nullptr, wo, b1, w2, b2);
}
```
